```python
import torch

def _softmax(x, axis=-1):
    return torch.softmax(x, dim=axis)
def _gelu(x):
    return torch.nn.functional.gelu(x, approximate="tanh")
def _silu(x):
    return torch.nn.functional.silu(x)
def _sigmoid(x):
    return torch.sigmoid(x)
def _layernorm(x, axis=-1, eps=1e-5):
    m = x.mean(dim=axis, keepdim=True)
    v = x.var(dim=axis, keepdim=True, unbiased=False)
    return (x - m) / torch.sqrt(v + eps)
def _scatter_add(tgt, idx, src, axis=0):
    return tgt.clone().index_add_(axis, idx, src)
def _segment_sum(src, seg, n):
    out = torch.zeros((n,) + src.shape[1:], dtype=src.dtype)
    return out.index_add_(0, seg, src)
def _pad(x, pw):
    flat = tuple(v for pair in reversed(pw) for v in pair)
    return torch.nn.functional.pad(x, flat)

kernel_name = 'recurrent_scan__ssm_conv_s4__moe_arch_mod__prompt'

def setup_inputs():

    _w = (lambda a: (lambda t: t.to(torch.bfloat16) if t.is_floating_point() else t)(torch.as_tensor(a)))
    def _A_setup():
        B, L, D, N = 1, 2048, 512, 128
        A_re = -(lambda _x: torch.exp(torch.as_tensor(_x)))((lambda *s: torch.randn(*s, dtype=torch.bfloat16) * 6.4)(D, N))
        A_im = torch.pi * (lambda *s: torch.randn(*s, dtype=torch.bfloat16) * 0.05)(D, N)
        B_re = (lambda *s: torch.randn(*s, dtype=torch.bfloat16) * 0.1)(D, N)
        B_im = (lambda *s: torch.randn(*s, dtype=torch.bfloat16) * 0.1)(D, N)
        C_re = (lambda *s: torch.randn(*s, dtype=torch.bfloat16) * 0.1)(D, N)
        C_im = (lambda *s: torch.randn(*s, dtype=torch.bfloat16) * 0.1)(D, N)
        log_dt = (lambda *s: torch.randn(*s, dtype=torch.bfloat16) * 0.05)(D) * 0.5 - 4.0
        Dskip = (lambda *s: torch.randn(*s, dtype=torch.bfloat16) * 0.05)(D)
        return {
            "u": _w((lambda *s: torch.randn(*s, dtype=torch.bfloat16) * 0.05)(B, L, D)),
            "A_re": _w(A_re),
            "A_im": _w(A_im),
            "B_re": _w(B_re),
            "B_im": _w(B_im),
            "C_re": _w(C_re),
            "C_im": _w(C_im),
            "log_dt": _w(log_dt),
            "Dskip": _w(Dskip),
        }
    _A_in = _A_setup()

    def _B_setup():
        B, S, D, F, C = 1, 2048, 512, 1792, 2
        return {
            "x": _w((lambda *s: torch.randn(*s, dtype=torch.bfloat16) * 0.05)(B, S, D)),
            "w_r": _w((lambda *s: torch.randn(*s, dtype=torch.bfloat16) * 3.2)(D)),
            "w_gate": _w((lambda *s: torch.randn(*s, dtype=torch.bfloat16) * 0.05)(D, F)),
            "w_up": _w((lambda *s: torch.randn(*s, dtype=torch.bfloat16) * 0.05)(D, F)),
            "w_down": _w((lambda *s: torch.randn(*s, dtype=torch.bfloat16) * 0.05)(F, D)),
        }
    _B_in = _B_setup()

    return {
        "A_u": _A_in["u"],
        "A_A_re": _A_in["A_re"],
        "A_A_im": _A_in["A_im"],
        "A_B_re": _A_in["B_re"],
        "A_B_im": _A_in["B_im"],
        "A_C_re": _A_in["C_re"],
        "A_C_im": _A_in["C_im"],
        "A_log_dt": _A_in["log_dt"],
        "A_Dskip": _A_in["Dskip"],
        "B_x": _B_in["x"],
        "B_w_r": _B_in["w_r"],
        "B_w_gate": _B_in["w_gate"],
        "B_w_up": _B_in["w_up"],
        "B_w_down": _B_in["w_down"],
    }

def reference(A_u, A_A_re, A_A_im, A_B_re, A_B_im, A_C_re, A_C_im, A_log_dt, A_Dskip, B_x, B_w_r, B_w_gate, B_w_up, B_w_down):
    def _A_ref(u, A_re, A_im, B_re, B_im, C_re, C_im, log_dt, Dskip):
        Bsz, L, D = u.shape
        dt = torch.exp(log_dt)[:, None]
        A = A_re + 1j * A_im
        Bc = B_re + 1j * B_im
        Cc = C_re + 1j * C_im
        logdA = dt * A
        dB = dt * Bc
        l = (lambda x, dt: x.to(dt))(torch.arange(L), A_re.dtype)
        P = torch.exp(logdA[:, :, None] * l[None, None, :])
        K = torch.einsum("dn,dnl,dn->dl", Cc, P, dB).real
        L2 = 2 * L
        Kp = _pad(K, ((0, 0), (0, L)))
        up = (lambda x, *d: x.permute(*d))(u, 0, 2, 1)
        up = _pad(up, ((0, 0), (0, 0), (0, L)))
        Kf = (lambda x, axes=(-2,-1): torch.fft.rfft2(x.float(), dim=axes))(Kp[:, None, :], axes=(-2, -1))
        Uf = (lambda x, axes=(-2,-1): torch.fft.rfft2(x.float(), dim=axes))(up[:, :, None, :], axes=(-2, -1))
        Yf = Uf * Kf[None, :, :, :]
        y = (lambda x, s, axes=(-2,-1): torch.fft.irfft2(x, s=s, dim=axes).to(torch.bfloat16))(Yf, (1, L2), axes=(-2, -1))[:, :, 0, :L]
        y = (lambda x, *d: x.permute(*d))(y, 0, 2, 1) + u * Dskip[None, None, :]
        return y

    def _B_ref(x, w_r, w_gate, w_up, w_down):
        B, S, D = x.shape
        C = 2
        r = x @ w_r
        idx = torch.argsort(-r, axis=-1)[:, :C]
        idx_e = (lambda x, n, axis: torch.repeat_interleave(x, n, dim=axis))(idx.reshape(B, C, 1), D, axis=2)
        x_sel = (lambda x, i, axis: torch.take_along_dim(x, i, dim=axis))(x, idx_e, axis=1)
        g = _silu(x_sel @ w_gate)
        u = x_sel @ w_up
        y = (g * u) @ w_down
        r_sel = _sigmoid((lambda x, i, axis: torch.take_along_dim(x, i, dim=axis))(r, idx, axis=1))
        y = y * r_sel.reshape(B, C, 1)
        b_off = (torch.arange(B) * S).reshape(B, 1)
        flat_idx = (idx + b_off).reshape(B * C)
        zero = (x * 0.0).reshape(B * S, D)
        upd = _scatter_add(zero, flat_idx, y.reshape(B * C, D), axis=0)
        return x + upd.reshape(B, S, D)

    x = A_u
    xn = _layernorm(x, axis=-1)
    s = _A_ref(xn, A_A_re, A_A_im, A_B_re, A_B_im, A_C_re, A_C_im, A_log_dt, A_Dskip)
    g = _silu(_B_ref(xn, B_w_r, B_w_gate, B_w_up, B_w_down))
    return x + s * g
```

```cpp
#include <hip/hip_runtime.h>
#include <hip/hip_bf16.h>
#include <stdint.h>

typedef unsigned short u16;
#define L_ 2048
#define D_ 512
#define N_ 128
#define F_ 1792

__device__ __forceinline__ float bf2f(u16 v) { return __uint_as_float(((unsigned)v) << 16); }
__device__ __forceinline__ u16 f2bf(float f) { __bf16 b = (__bf16)f; return __builtin_bit_cast(u16, b); }
__device__ __forceinline__ float rbf(float f) { return bf2f(f2bf(f)); }

__global__ void k_ln(const u16* x, const u16* wr, u16* xn, float* r) {
    int t = blockIdx.x; int lane = threadIdx.x;
    float v[8]; float s = 0;
    for (int i = 0; i < 8; i++) { v[i] = bf2f(x[t * D_ + lane * 8 + i]); s += v[i]; }
    for (int o = 32; o; o >>= 1) s += __shfl_xor(s, o);
    float mean = s / D_;
    float q = 0;
    for (int i = 0; i < 8; i++) { float d = v[i] - mean; q += d * d; }
    for (int o = 32; o; o >>= 1) q += __shfl_xor(q, o);
    float var = rbf(q / D_);
    float m = rbf(mean);
    float sd = rbf(sqrtf(rbf(var + 1e-5f)));
    float acc = 0;
    for (int i = 0; i < 8; i++) { float z = rbf(rbf(v[i] - m) / sd); xn[t * D_ + lane * 8 + i] = f2bf(z); acc += z * bf2f(wr[lane * 8 + i]); }
    for (int o = 32; o; o >>= 1) acc += __shfl_xor(acc, o);
    if (lane == 0) r[t] = rbf(acc);
}

__global__ void k_kgen(const u16* Are, const u16* Aim, const u16* Bre, const u16* Bim, const u16* Cre, const u16* Cim, const u16* logdt, float* K) {
    int d = blockIdx.y; int l = blockIdx.x * 256 + threadIdx.x;
    float dt = rbf(expf(bf2f(logdt[d])));
    float lf = rbf((float)l);
    float acc = 0;
    for (int n = 0; n < N_; n++) {
        float ar = dt * bf2f(Are[d * N_ + n]), ai = dt * bf2f(Aim[d * N_ + n]);
        float br = dt * bf2f(Bre[d * N_ + n]), bi = dt * bf2f(Bim[d * N_ + n]);
        float cr = bf2f(Cre[d * N_ + n]), ci = bf2f(Cim[d * N_ + n]);
        float wr = cr * br - ci * bi, wi = cr * bi + ci * br;
        float e = expf(ar * lf); float sn, cs; sincosf(ai * lf, &sn, &cs);
        acc += e * (wr * cs - wi * sn);
    }
    K[d * L_ + l] = acc;
}

__global__ void k_conv(const u16* xn, const float* K, float* y) {
    int d = blockIdx.y * 64 + threadIdx.x; int t = blockIdx.x;
    float acc = 0;
    for (int j = 0; j <= t; j++) acc += K[d * L_ + j] * bf2f(xn[(t - j) * D_ + d]);
    y[t * D_ + d] = acc;
}

__global__ void k_top2(const float* r, int* idx) {
    if (threadIdx.x == 0) {
        int i0 = 0; for (int i = 1; i < L_; i++) if (r[i] > r[i0]) i0 = i;
        int i1 = (i0 == 0) ? 1 : 0; for (int i = 0; i < L_; i++) if (i != i0 && r[i] > r[i1]) i1 = i;
        idx[0] = i0; idx[1] = i1;
    }
}
__global__ void k_gateup(const u16* xn, const int* idx, const u16* wg, const u16* wu, float* h) {
    int f = blockIdx.x * 64 + threadIdx.x; int c = blockIdx.y; int t = idx[c];
    float g = 0, u = 0;
    for (int k = 0; k < D_; k++) { float xv = bf2f(xn[t * D_ + k]); g += xv * bf2f(wg[k * F_ + f]); u += xv * bf2f(wu[k * F_ + f]); }
    g = rbf(g); u = rbf(u);
    float sg = rbf(g / (1.f + expf(-g)));
    h[c * F_ + f] = rbf(sg * u);
}
__global__ void k_down(const float* h, const int* idx, const float* r, const u16* wd, float* upd) {
    int d = blockIdx.x * 64 + threadIdx.x; int c = blockIdx.y;
    float y = 0;
    for (int f = 0; f < F_; f++) y += h[c * F_ + f] * bf2f(wd[f * D_ + d]);
    y = rbf(y);
    float rs = rbf(1.f / (1.f + expf(-r[idx[c]])));
    upd[c * D_ + d] = rbf(y * rs);
}
__global__ void k_epi(const u16* x, const u16* xn, const float* y, const u16* Dskip, const int* idx, const float* upd, u16* out) {
    int d = blockIdx.y * 64 + threadIdx.x; int t = blockIdx.x;
    float z = bf2f(xn[t * D_ + d]);
    float s = rbf(rbf(y[t * D_ + d]) + rbf(z * bf2f(Dskip[d])));
    float b = z;
    if (t == idx[0]) b = rbf(z + upd[d]);
    if (t == idx[1]) b = rbf(z + upd[D_ + d]);
    float g = rbf(b / (1.f + expf(-b)));
    out[t * D_ + d] = f2bf(bf2f(x[t * D_ + d]) + rbf(s * g));
}

extern "C" void kernel_launch(void* const* d_in, const int* in_sizes, int n_in,
                              void* d_out, int out_size, void* d_ws, size_t ws_size,
                              hipStream_t stream) {
    const u16* x = (const u16*)d_in[0];
    char* ws = (char*)d_ws;
    u16* xn = (u16*)ws; ws += L_ * D_ * 2;
    float* K = (float*)ws; ws += D_ * L_ * 4;
    float* y = (float*)ws; ws += D_ * L_ * 4;
    float* r = (float*)ws; ws += L_ * 4;
    float* h = (float*)ws; ws += 2 * F_ * 4;
    float* upd = (float*)ws; ws += 2 * D_ * 4;
    int* idx = (int*)ws;
    k_ln<<<L_, 64, 0, stream>>>(x, (const u16*)d_in[10], xn, r);
    k_kgen<<<dim3(L_ / 256, D_), 256, 0, stream>>>((const u16*)d_in[1], (const u16*)d_in[2], (const u16*)d_in[3], (const u16*)d_in[4], (const u16*)d_in[5], (const u16*)d_in[6], (const u16*)d_in[7], K);
    k_conv<<<dim3(L_, D_ / 64), 64, 0, stream>>>(xn, K, y);
    k_top2<<<1, 64, 0, stream>>>(r, idx);
    k_gateup<<<dim3(F_ / 64, 2), 64, 0, stream>>>(xn, idx, (const u16*)d_in[11], (const u16*)d_in[12], h);
    k_down<<<dim3(D_ / 64, 2), 64, 0, stream>>>(h, idx, r, (const u16*)d_in[13], upd);
    k_epi<<<dim3(L_, D_ / 64), 64, 0, stream>>>(x, xn, y, (const u16*)d_in[8], idx, upd, (u16*)d_out);
}
```

```cpp
#include <hip/hip_runtime.h>
#include <stdint.h>

typedef unsigned short u16;
typedef unsigned int u32;
typedef unsigned long long u64;
typedef float f32x2 __attribute__((ext_vector_type(2)));
typedef float f32x4 __attribute__((ext_vector_type(4)));
typedef u32 u32x2 __attribute__((ext_vector_type(2)));
typedef u32 u32x4 __attribute__((ext_vector_type(4)));
typedef __bf16 bf16x2 __attribute__((ext_vector_type(2)));
typedef __bf16 bf16x8 __attribute__((ext_vector_type(8)));
typedef _Float16 f16x2 __attribute__((ext_vector_type(2)));
typedef _Float16 f16x8 __attribute__((ext_vector_type(8)));
#define GAS __attribute__((address_space(1)))
#define LAS __attribute__((address_space(3)))
typedef GAS u64 gu64;
typedef GAS u32 gu32;
#define RLX_AGENT __ATOMIC_RELAXED, __HIP_MEMORY_SCOPE_AGENT

constexpr int L_ = 2048, D_ = 512, F_ = 1792;
constexpr int NFFN = 56;
constexpr u32 WS_EPOCH = 0, WS_SEEN = 1024, WS_SEL = 4096, WS_CAND = 8192, WS_STATS = 32768, WS_PART = 262144;
constexpr u32 WS_DBG = 1u << 21;
constexpr int TBL_ROW = 272, TBL_SZ = 57 * TBL_ROW;
constexpr int LDS_U = 8 * TBL_SZ;
constexpr int U_CH = (256 + 2048) * 2;
constexpr int LDS_MISC = LDS_U + 4 * U_CH;
constexpr int LDS_LN = LDS_MISC;
constexpr int LDS_GU = LDS_LN + 2048;
constexpr int LDS_H = LDS_GU + 4096;
constexpr int LDS_UPD = LDS_H + 1024;
constexpr int LDS_TAG = LDS_UPD + 32;
constexpr int LDS_TOTAL = LDS_TAG + 32;
constexpr int P_OFF = 4096;
constexpr int Y_TILE = 1280, Y_CH = 4 * Y_TILE;
constexpr u32 SPIN_CAP = 1u << 16;

struct Args {
    const u16 *x, *are, *aim, *bre, *bim, *cre, *cim, *logdt, *dskip, *wr, *wg, *wu, *wd;
    u16* out;
    char* ws;
};

__device__ __forceinline__ float bflo(u32 v) { return __uint_as_float(v << 16); }
__device__ __forceinline__ float bfhi(u32 v) { return __uint_as_float(v & 0xffff0000u); }
__device__ __forceinline__ u32 pkbf(float a, float b) { f32x2 v = {a, b}; return __builtin_bit_cast(u32, __builtin_convertvector(v, bf16x2)); }
__device__ __forceinline__ u32 pkh(f32x2 v) { return __builtin_bit_cast(u32, __builtin_convertvector(v, f16x2)); }
__device__ __forceinline__ float rbf(float a) { return bflo(pkbf(a, a)); }
__device__ __forceinline__ f32x2 rbf2(f32x2 a) { u32 p = pkbf(a.x, a.y); return f32x2{bflo(p), bfhi(p)}; }
__device__ __forceinline__ f32x2 cmul(f32x2 a, f32x2 z, f32x2 zn) {
    return __builtin_elementwise_fma(a.yx, zn, a * z.xx);
}
__device__ __forceinline__ f32x2 csq(f32x2 a) { return cmul(a, a, f32x2{-a.y, a.y}); }
__device__ __forceinline__ void bar() { asm volatile("s_waitcnt lgkmcnt(0)\n\ts_barrier" ::: "memory"); }
__device__ __forceinline__ f32x4 mfma_bf(u32x4 a, u32x4 b, f32x4 c) {
    return __builtin_amdgcn_mfma_f32_16x16x32_bf16(__builtin_bit_cast(bf16x8, a), __builtin_bit_cast(bf16x8, b), c, 0, 0, 0);
}
__device__ __forceinline__ f32x4 mfma_h(u32x4 a, u32x4 b, f32x4 c) {
    return __builtin_amdgcn_mfma_f32_16x16x32_f16(__builtin_bit_cast(f16x8, a), __builtin_bit_cast(f16x8, b), c, 0, 0, 0);
}
template <int CTRL> __device__ __forceinline__ u32 dpp(u32 old, u32 src) { return __builtin_amdgcn_update_dpp(old, src, CTRL, 0xf, 0xf, false); }
template <int CTRL> __device__ __forceinline__ float dppf(float old, float src) { return __uint_as_float(dpp<CTRL>(__float_as_uint(old), __float_as_uint(src))); }
__device__ __forceinline__ u32 row_umax(u32 v) {
    v = max(v, dpp<0x128>(v, v)); v = max(v, dpp<0x124>(v, v)); v = max(v, dpp<0x122>(v, v)); v = max(v, dpp<0x121>(v, v));
    return v;
}
__device__ __forceinline__ u32 wave_umax(u32 v) {
    v = row_umax(v);
    auto a = __builtin_amdgcn_permlane16_swap(v, v, false, false); v = max(a[0], a[1]);
    auto b = __builtin_amdgcn_permlane32_swap(v, v, false, false); return max(b[0], b[1]);
}
__device__ __forceinline__ float wave_fmax(float f) {
    f = fmaxf(f, dppf<0x128>(f, f)); f = fmaxf(f, dppf<0x124>(f, f)); f = fmaxf(f, dppf<0x122>(f, f)); f = fmaxf(f, dppf<0x121>(f, f));
    auto a = __builtin_amdgcn_permlane16_swap(__float_as_uint(f), __float_as_uint(f), false, false); f = fmaxf(__uint_as_float(a[0]), __uint_as_float(a[1]));
    auto b = __builtin_amdgcn_permlane32_swap(__float_as_uint(f), __float_as_uint(f), false, false); return fmaxf(__uint_as_float(b[0]), __uint_as_float(b[1]));
}
__device__ __forceinline__ float wave_fsum(float f) {
    f += dppf<0x128>(f, f); f += dppf<0x124>(f, f); f += dppf<0x122>(f, f); f += dppf<0x121>(f, f);
    auto a = __builtin_amdgcn_permlane16_swap(__float_as_uint(f), __float_as_uint(f), false, false); f = __uint_as_float(a[0]) + __uint_as_float(a[1]);
    auto b = __builtin_amdgcn_permlane32_swap(__float_as_uint(f), __float_as_uint(f), false, false); return __uint_as_float(b[0]) + __uint_as_float(b[1]);
}
__device__ __forceinline__ u64 gld(const char* p) { return __hip_atomic_load((gu64*)p, RLX_AGENT); }
__device__ __forceinline__ void gst(char* p, u32 tag, u32 v) { __hip_atomic_store((gu64*)p, ((u64)tag << 32) | v, RLX_AGENT); }
__device__ __forceinline__ float sigm(float z) { return __builtin_amdgcn_rcpf(1.0f + __builtin_amdgcn_exp2f(z * -1.44269504089f)); }

#define STAMP(i)

template <int ROLE>
__device__ __forceinline__ void body(const Args& a, LAS char* lds, const int xcd, const int jj) {
    constexpr bool HEAVY = ROLE == 0;
    const int tid = threadIdx.x, lane = tid & 63;
    const int w = __builtin_amdgcn_readfirstlane(tid >> 6);
    const int q = lane & 15, g = lane >> 4;
    const int ch = w & 3, h = w >> 2;
    const int G = xcd * 16 + jj;
    const int tr = (tid + 64 * (jj & 7)) & 511;
    STAMP(0);

    u32 epoch = 0;
    if (w == 0) epoch = __hip_atomic_load((gu32*)(a.ws + WS_EPOCH), RLX_AGENT);
    u32x4 xa[2], wrv[2];
    if constexpr (!HEAVY) {
        const u16* xr = a.x + (G * 16 + q) * D_ + 64 * w + 8 * g;
        xa[0] = *(const u32x4*)xr; xa[1] = *(const u32x4*)(xr + 32);
        const u16* wp = a.wr + 64 * w + 8 * g;
        wrv[0] = *(const u32x4*)wp; wrv[1] = *(const u32x4*)(wp + 32);
    }
    const int pidx = (4 * G + ch) * 128 + 64 * h + lane;
    const u32 p_are = a.are[pidx], p_aim = a.aim[pidx], p_bre = a.bre[pidx], p_bim = a.bim[pidx], p_cre = a.cre[pidx], p_cim = a.cim[pidx];
    const u32 p_ldt = a.logdt[4 * G + ch];
    const u32x2 dsk = *(const u32x2*)(a.dskip + 4 * G);
    u32x2 xc[4];
    if constexpr (HEAVY) {
        const u16* xp = a.x + 2 * tr * D_ + 4 * G;
        xc[2] = *(const u32x2*)(xp + 1024 * D_); xc[3] = *(const u32x2*)(xp + 1025 * D_);
        xc[0] = *(const u32x2*)xp; xc[1] = *(const u32x2*)(xp + D_);
    }
    if (tid < 128) *(LAS u32x4*)(lds + LDS_U + (tid >> 5) * U_CH + (tid & 31) * 16) = u32x4{0, 0, 0, 0};
    if (w == 0) {
        if (lane == 0) *(LAS u32*)(lds + LDS_TAG) = epoch + 1;
        if (HEAVY && lane == 0) gst(a.ws + WS_SEEN + G * 8, epoch + 1, 0);
    }
    u32 tag = 0;

    if constexpr (!HEAVY) {
        const u32x4 ones = {0x3f803f80u, 0x3f803f80u, 0x3f803f80u, 0x3f803f80u};
        u32x4 cb[2];
#pragma unroll
        for (int s = 0; s < 2; s++)
#pragma unroll
            for (int i = 0; i < 4; i++) cb[s][i] = q == 0 ? ones[i] : (q == 1 ? wrv[s][i] : 0u);
        const f32x4 z4 = {0, 0, 0, 0};
        f32x4 d1 = mfma_bf(xa[0], cb[0], z4), d2 = mfma_bf(xa[0], xa[0], z4), d3 = mfma_bf(ones, cb[0], z4);
        d1 = mfma_bf(xa[1], cb[1], d1); d2 = mfma_bf(xa[1], xa[1], d2); d3 = mfma_bf(ones, cb[1], d3);
        LAS float* ln = (LAS float*)(lds + LDS_LN);
        if (q < 2) {
#pragma unroll
            for (int i = 0; i < 4; i++) ln[(q * 16 + 4 * g + i) * 8 + w] = d1[i];
        }
        const float dg = (q & 2) ? ((q & 1) ? d2[3] : d2[2]) : ((q & 1) ? d2[1] : d2[0]);
        if ((q >> 2) == g) ln[(32 + q) * 8 + w] = dg;
        if (lane == 1) ln[48 * 8 + w] = d3[0];
        bar();
        tag = *(LAS u32*)(lds + LDS_TAG);
        const LAS f32x4* lp = (const LAS f32x4*)(ln + (g == 3 ? 48 : g * 16 + q) * 8);
        const f32x4 s0 = lp[0], s1 = lp[1];
        const float v = ((s0[0] + s0[1]) + (s0[2] + s0[3])) + ((s1[0] + s1[1]) + (s1[2] + s1[3]));
        auto e = __builtin_amdgcn_permlane16_swap(__float_as_uint(v), __float_as_uint(v), false, false);
        auto e0 = __builtin_amdgcn_permlane32_swap(e[0], e[0], false, false);
        auto e1 = __builtin_amdgcn_permlane32_swap(e[1], e[1], false, false);
        const float mean = __uint_as_float(e0[0]) * (1.0f / D_);
        const float var = fmaxf(__uint_as_float(e0[1]) * (1.0f / D_) - mean * mean, 0.0f);
        const float m = rbf(mean);
        const float sd = rbf(__builtin_amdgcn_sqrtf(rbf(rbf(var) + 1e-5f)));
        const float r = (__uint_as_float(e1[0]) - m * __uint_as_float(e1[1])) * __builtin_amdgcn_rcpf(sd);
        if (lane < 16) gst(a.ws + WS_STATS + w * 16384 + (G * 16 + q) * 8, tag, pkbf(m, sd));
        const u32 rb = pkbf(r, r) & 0xffffu;
        const u32 key = (((rb & 0x8000u) ? (~rb & 0xffffu) : (rb | 0x8000u)) << 16) | (0xffffu - (G * 16 + q));
        const u32 k1 = row_umax(key);
        const u32 k2 = row_umax(key == k1 ? 0u : key);
        if (lane < 2) gst(a.ws + WS_CAND + w * 2048 + G * 16 + lane * 8, tag, lane ? k2 : k1);
        STAMP(1);
        const u16* xp = a.x + 2 * tr * D_ + 4 * G;
        xc[2] = *(const u32x2*)xp; xc[3] = *(const u32x2*)(xp + D_);
    }
    u32 wgu[2][2][8], wdn[2][8];
    const int f = xcd * 7 + jj;
    if constexpr (ROLE == 2) {
#pragma unroll
        for (int s = 0; s < 2; s++)
#pragma unroll
            for (int j = 0; j < 8; j++) {
                const int o = (64 * w + 32 * s + 8 * g + j) * F_ + 32 * f + 2 * q;
                wgu[0][s][j] = *(const u32*)(a.wg + o); wgu[1][s][j] = *(const u32*)(a.wu + o);
            }
#pragma unroll
        for (int u = 0; u < 2; u++)
#pragma unroll
            for (int j = 0; j < 8; j++) wdn[u][j] = *(const u32*)(a.wd + (32 * f + 8 * g + j) * D_ + 64 * w + 32 * u + 2 * q);
    }

    f32x4 kt[3];
    {
        const float dt = rbf(expf(bflo(p_ldt)));
        const float ar = dt * bflo(p_are), ai = dt * bflo(p_aim);
        const float br = dt * bflo(p_bre), bi = dt * bflo(p_bim), cr = bflo(p_cre), ci = bflo(p_cim);
        const f32x2 W = {cr * br - ci * bi, cr * bi + ci * br};
        const float lr = ar * 1.44269504089f;
        const float th = ai * 0.15915494309f;
        const float tl = __builtin_fmaf(ai, 0.15915494309f, -th) + ai * 6.4206382e-9f;
        const float gp = fmaxf(lr, 0.0f);
        const float lw = 0.5f * __builtin_amdgcn_logf(W.x * W.x + W.y * W.y) + 2168.0f * gp;
        const float E = __builtin_ceilf(fminf(fmaxf(wave_fmax(lw), -100.0f), 120.0f));
        const float sA = __builtin_amdgcn_exp2f(13.0f - E + 120.0f * gp), sB = __builtin_amdgcn_exp2f(-120.0f * gp);
        const float inv = __builtin_amdgcn_exp2f(E - 13.0f);
        const float t1 = __builtin_amdgcn_fractf(th), e1 = __builtin_amdgcn_exp2f(lr);
        const f32x2 z1 = {e1 * __builtin_amdgcn_cosf(t1), e1 * __builtin_amdgcn_sinf(t1)};
        const float t7 = __builtin_amdgcn_fractf(128.0f * th) + 128.0f * tl, e7 = __builtin_amdgcn_exp2f(128.0f * lr);
        const f32x2 z128 = {e7 * __builtin_amdgcn_cosf(t7), e7 * __builtin_amdgcn_sinf(t7)};
        const f32x2 z2 = csq(z1), z4 = csq(z2), z8 = csq(z4);
        const f32x2 n1 = {-z1.y, z1.y}, n2 = {-z2.y, z2.y}, n4 = {-z4.y, z4.y}, n128 = {-z128.y, z128.y};
        const f32x2 c8 = {z8.y, -z8.y};
        LAS u32* tb = (LAS u32*)(lds + w * TBL_SZ) + lane;
        f32x2 X = W * sA, B = {sB, 0.0f};
#pragma unroll
        for (int k = 0; k < 17; k++) {
            if (k < 16) { tb[(41 + k) * 68] = pkh(B); B = cmul(B, z8, c8); }
            if (k < 2) {
                f32x2 c = X;
#pragma unroll
                for (int i = 0; i < 8; i++) { tb[(8 * k + i) * 68] = pkh(c); if (i < 7) c = cmul(c, z1, n1); }
            } else if (k < 4) {
                f32x2 c = X;
#pragma unroll
                for (int i = 0; i < 4; i++) { tb[(16 + 4 * (k - 2) + i) * 68] = pkh(c); if (i < 3) c = cmul(c, z2, n2); }
            } else if (k < 8) {
                tb[(24 + 2 * (k - 4)) * 68] = pkh(X); tb[(25 + 2 * (k - 4)) * 68] = pkh(cmul(X, z4, n4));
            } else tb[(32 + k - 8) * 68] = pkh(X);
            if (k < 16) X = cmul(X, z128, n128);
        }
        STAMP(2);
        const LAS char* tf = lds + w * TBL_SZ + q * TBL_ROW + g * 16;
#pragma unroll
        for (int t = 0; t < 3; t++) kt[t] = f32x4{0, 0, 0, 0};
#pragma unroll
        for (int s = 0; s < 4; s++) {
            const u32x4 fb = *(const LAS u32x4*)(tf + 41 * TBL_ROW + s * 64);
#pragma unroll
            for (int t = 0; t < 3; t++) kt[t] = mfma_h(*(const LAS u32x4*)(tf + 16 * t * TBL_ROW + s * 64), fb, kt[t]);
        }
#pragma unroll
        for (int t = 0; t < 3; t++) { kt[t] *= inv; *(LAS f32x4*)(lds + w * TBL_SZ + t * 1024 + lane * 16) = kt[t]; }
    }
    bar();
    if constexpr (HEAVY) tag = *(LAS u32*)(lds + LDS_TAG);
#pragma unroll
    for (int t = 0; t < 3; t++) kt[t] += *(const LAS f32x4*)(lds + (w ^ 4) * TBL_SZ + t * 1024 + lane * 16);
    {
        LAS char* P = lds + ch * TBL_SZ + P_OFF + 96;
        if (h == 0) {
            const f32x4 v = kt[0];
            const int src = (g & 1) ? lane - 16 : (q ? lane + 15 : lane - 1);
            float pv = __uint_as_float(__builtin_amdgcn_ds_bpermute(src * 4, __float_as_uint(v[3])));
            pv = lane == 0 ? 0.0f : pv;
            *(LAS u32x4*)(P + 4 * (128 * (g >> 1) + 8 * q + 4 * (g & 1))) = u32x4{pkbf(v[0], pv), pkbf(v[1], v[0]), pkbf(v[2], v[1]), pkbf(v[3], v[2])};
            if (lane < 6) *(LAS u32x4*)(P - 96 + lane * 16) = u32x4{0, 0, 0, 0};
        } else {
            const f32x4 v = kt[1];
            const bool s2 = g < 2;
            const float k255 = __uint_as_float(__builtin_amdgcn_readlane(__float_as_uint(kt[0][3]), 63));
            const float k1024 = __uint_as_float(__builtin_amdgcn_readlane(__float_as_uint(kt[2][0]), 0));
            const float nw = dppf<0x130>(v[0], v[0]);
            const float nr = dppf<0x12f>(v[0], q == 0 ? v[2] : v[0]);
            const float nA = s2 ? nw : nr;
            const float nB = dppf<0x130>(k1024, q == 0 ? v[0] : v[2]);
            const float x2 = s2 ? v[1] : v[0], x3 = s2 ? v[2] : v[1], x6 = s2 ? v[3] : nA;
            const u32 aa = pkbf(v[0], v[0]), cc = pkbf(x3, x3);
            LAS char* PA = P + 4 * (s2 ? 256 + 128 * g + 8 * q : 256 * g + 8 * q);
            *(LAS u32x4*)PA = u32x4{lane == 0 ? pkbf(v[0], k255) : aa, aa, pkbf(x2, v[0]), pkbf(x3, x2)};
            *(LAS u32x4*)(PA + 16) = u32x4{cc, cc, pkbf(x6, x3), pkbf(nA, x6)};
            if (!s2) {
                const u32 a2 = pkbf(v[2], v[2]), b2 = pkbf(v[3], v[3]);
                *(LAS u32x4*)(PA + 512) = u32x4{a2, a2, a2, pkbf(v[3], v[2])};
                *(LAS u32x4*)(PA + 528) = u32x4{b2, b2, pkbf(nB, v[3]), pkbf(nB, nB)};
            }
        }
        if constexpr (HEAVY) {
            const f32x4 v = kt[2];
#pragma unroll
            for (int ii = 0; ii < 2; ii++) {
                const float va = h ? v[2 + ii] : v[ii];
                const float vn = h ? (ii ? v[0] : v[3]) : v[1 + ii];
                const float sv = q == 0 ? vn : va;
                const float n0 = dppf<0x12f>(sv, sv), n1_ = dppf<0x130>(sv, sv);
                const float n = (h && ii) ? n1_ : n0;
                const u32 vv = pkbf(va, va), nv = pkbf(n, va), nn = pkbf(n, n);
                if (g < 2) {
                    LAS char* PA = P + 4 * (128 * (8 + 4 * g + 2 * h + ii) + 8 * q);
                    *(LAS u32x4*)PA = u32x4{vv, vv, vv, vv};
                    *(LAS u32x4*)(PA + 16) = (q & 1) ? u32x4{nv, nn, nn, nn} : u32x4{vv, nv, nn, nn};
                }
            }
        }
    }
    bar();
    constexpr int NA = HEAVY ? 32 : 16;
    u32x4 A[NA];
    {
        const LAS u32* P = (const LAS u32*)(lds + ch * TBL_SZ + P_OFF + 96) + 32 * h + 16 + q - 8 * g;
#pragma unroll
        for (int m = 0; m < NA; m++) A[m] = u32x4{P[64 * m], P[64 * m - 2], P[64 * m - 4], P[64 * m - 6]};
    }
    STAMP(3);

    if constexpr (ROLE == 2) {
        u32x4 fg[2][2][2], fd[2][2];
#pragma unroll
        for (int p = 0; p < 2; p++) {
            const u32 sel = p ? 0x07060302u : 0x05040100u;
#pragma unroll
            for (int d = 0; d < 4; d++) {
#pragma unroll
                for (int s = 0; s < 2; s++) {
                    fg[0][s][p][d] = __builtin_amdgcn_perm(wgu[0][s][2 * d + 1], wgu[0][s][2 * d], sel);
                    fg[1][s][p][d] = __builtin_amdgcn_perm(wgu[1][s][2 * d + 1], wgu[1][s][2 * d], sel);
                    fd[s][p][d] = __builtin_amdgcn_perm(wdn[s][2 * d + 1], wdn[s][2 * d], sel);
                }
            }
        }
        u32 k[4];
        for (u32 spin = 0; spin < SPIN_CAP; spin++) {
            bool ok = true;
#pragma unroll
            for (int i = 0; i < 4; i++) { const u64 v = gld(a.ws + WS_CAND + xcd * 2048 + (i * 64 + lane) * 8); k[i] = (u32)v; ok &= (u32)(v >> 32) == tag; }
            if (__all(ok)) break;
            __builtin_amdgcn_s_sleep(2);
        }
        STAMP(4);
        const u32 k1 = wave_umax(max(max(k[0], k[1]), max(k[2], k[3])));
#pragma unroll
        for (int i = 0; i < 4; i++) k[i] = k[i] == k1 ? 0u : k[i];
        const u32 k2 = wave_umax(max(max(k[0], k[1]), max(k[2], k[3])));
        const u32 kk = (lane & 1) ? k2 : k1;
        const u32 row = (0xffffu - (kk & 0xffffu)) & 2047u;
        const u16* xr = a.x + row * D_ + 64 * w + 8 * g;
        const u32x4 xs0 = *(const u32x4*)xr, xs1 = *(const u32x4*)(xr + 32);
        u32 st;
        for (u32 spin = 0; spin < SPIN_CAP; spin++) {
            const u64 v = gld(a.ws + WS_STATS + xcd * 16384 + row * 8); st = (u32)v;
            if (__all((u32)(v >> 32) == tag)) break;
        }
        if (jj == 0 && w == 0 && lane < 2) {
            const u32 kb = kk >> 16, rb = (kb & 0x8000u) ? (kb & 0x7fffu) : (~kb & 0xffffu);
            const float sg = sigm(bflo(rb));
            gst(a.ws + WS_SEL + xcd * 128 + lane * 8, tag, row | (pkbf(sg, sg) << 16));
        }
        const float m = bflo(st), rs = __builtin_amdgcn_rcpf(bfhi(st));
        u32x4 an[2];
#pragma unroll
        for (int d = 0; d < 4; d++) {
            f32x2 t0 = rbf2(f32x2{bflo(xs0[d]), bfhi(xs0[d])} - m) * rs, t1 = rbf2(f32x2{bflo(xs1[d]), bfhi(xs1[d])} - m) * rs;
            an[0][d] = pkbf(t0.x, t0.y); an[1][d] = pkbf(t1.x, t1.y);
        }
        LAS float* gu = (LAS float*)(lds + LDS_GU);
#pragma unroll
        for (int mt = 0; mt < 2; mt++)
#pragma unroll
            for (int p = 0; p < 2; p++) {
                f32x4 c = mfma_bf(an[0], fg[mt][0][p], f32x4{0, 0, 0, 0});
                c = mfma_bf(an[1], fg[mt][1][p], c);
                if (g == 0) { gu[w * 128 + mt * 64 + 2 * q + p] = c[0]; gu[w * 128 + mt * 64 + 32 + 2 * q + p] = c[1]; }
            }
        bar();
        float ga = 0.0f, ua = 0.0f;
#pragma unroll
        for (int i = 0; i < 8; i++) { ga += gu[i * 128 + lane]; ua += gu[i * 128 + 64 + lane]; }
        ga = rbf(ga);
        const float hv = rbf(ga * sigm(ga)) * rbf(ua);
        LAS u16* hb = (LAS u16*)(lds + LDS_H + w * 128);
        hb[lane] = (u16)pkbf(hv, hv);
        const u32x4 ah = *(const LAS u32x4*)(hb + (q & 1) * 32 + 8 * g);
        f32x4 c00 = mfma_bf(ah, fd[0][0], f32x4{0, 0, 0, 0}), c01 = mfma_bf(ah, fd[0][1], f32x4{0, 0, 0, 0});
        f32x4 c10 = mfma_bf(ah, fd[1][0], f32x4{0, 0, 0, 0}), c11 = mfma_bf(ah, fd[1][1], f32x4{0, 0, 0, 0});
        const float y0 = (g & 2) ? ((g & 1) ? c11[0] : c10[0]) : ((g & 1) ? c01[0] : c00[0]);
        const float y1 = (g & 2) ? ((g & 1) ? c11[1] : c10[1]) : ((g & 1) ? c01[1] : c00[1]);
        const int d = 64 * w + 32 * (g >> 1) + 2 * q + (g & 1);
        char* pp = a.ws + WS_PART + (((d >> 2) * NFFN + f) * 4 + (d & 3)) * 8;
        gst(pp, tag, __float_as_uint(y0));
        gst(pp + 128 * NFFN * 32, tag, __float_as_uint(y1));
        STAMP(5);
    }

    u32 st[4];
    {
        const char* sp = a.ws + WS_STATS + xcd * 16384 + tr * 16;
        const bool upd = HEAVY && G == 0;
        for (u32 spin = 0; spin < SPIN_CAP; spin++) {
            bool ok = true;
#pragma unroll
            for (int i = HEAVY ? 0 : 2; i < 4; i++) {
                const u64 v = gld(sp + (i & 1) * 8 + ((HEAVY && i >= 2) ? 8192 : 0)); st[i] = (u32)v; ok &= (u32)(v >> 32) == tag;
            }
            if (upd && tid < 128) ok &= (u32)(gld(a.ws + WS_SEEN + tid * 8) >> 32) == tag;
            if (__all(ok)) break;
            __builtin_amdgcn_s_sleep(2);
        }
    }
    STAMP(6);
    float mr[2], rr[2];
#pragma unroll
    for (int p = HEAVY ? 0 : 1; p < 2; p++) {
        const f32x2 rs = {__builtin_amdgcn_rcpf(bfhi(st[2 * p])), __builtin_amdgcn_rcpf(bfhi(st[2 * p + 1]))};
        const f32x2 mm = {bflo(st[2 * p]), bflo(st[2 * p + 1])};
        const f32x2 nb = -mm * rs;
        if (p == 1) { mr[0] = mm.x; mr[1] = mm.y; rr[0] = rs.x; rr[1] = rs.y; }
#pragma unroll
        for (int c = 0; c < 4; c++) {
            const u32 w0 = xc[2 * p][c >> 1], w1 = xc[2 * p + 1][c >> 1];
            const f32x2 xv = (c & 1) ? f32x2{bfhi(w0), bfhi(w1)} : f32x2{bflo(w0), bflo(w1)};
            const f32x2 z = __builtin_elementwise_fma(xv, rs, nb);
            *(LAS u32*)(lds + LDS_U + c * U_CH + 512 + 4 * tr + ((HEAVY && p) ? 2048 : 0)) = pkbf(z.x, z.y);
        }
    }
    bar();
    if (HEAVY && G == 0 && tid == 0) __hip_atomic_store((gu32*)(a.ws + WS_EPOCH), tag, RLX_AGENT);
    STAMP(7);

    f32x4 acc[4];
#pragma unroll
    for (int i = 0; i < 4; i++) acc[i] = f32x4{0, 0, 0, 0};
    {
        const LAS char* ub = lds + LDS_U + ch * U_CH + 480 + 32 * q + 16 * g - 64 * h + (HEAVY ? 2048 : 0);
#pragma unroll
        for (int b = 12; b >= (HEAVY ? -19 : -3); b--) {
            const u32x4 B = *(const LAS u32x4*)(ub + 128 * b);
#pragma unroll
            for (int i = 0; i < 4; i++) {
                const int mp = 4 * i - b;
                if (mp >= 0 && mp < NA) acc[i] = mfma_bf(A[mp], B, acc[i]);
            }
        }
    }
    const char* selp = a.ws + WS_SEL + xcd * 128 + (lane & 1) * 8;
    const char* prtp = a.ws + WS_PART + ((h * 128 + G) * NFFN + (lane < NFFN ? lane : 0)) * 32 + ch * 8;
    u64 selv = gld(selp), prtv = gld(prtp);
    f32x2 xf[4], t1[4], gt[4], zn[4];
#pragma unroll
    for (int c = 0; c < 4; c++) {
        const u32 w0 = xc[2][c >> 1], w1 = xc[3][c >> 1];
        xf[c] = (c & 1) ? f32x2{bfhi(w0), bfhi(w1)} : f32x2{bflo(w0), bflo(w1)};
        zn[c] = rbf2(rbf2(xf[c] - f32x2{mr[0], mr[1]}) * f32x2{rr[0], rr[1]});
        t1[c] = rbf2(zn[c] * ((c & 1) ? bfhi(dsk[c >> 1]) : bflo(dsk[c >> 1])));
        gt[c] = rbf2(zn[c] * f32x2{sigm(zn[c].x), sigm(zn[c].y)});
    }
    STAMP(8);
#pragma unroll
    for (int i = 0; i < 4; i++) *(LAS f32x4*)(lds + (h * 4 + ch) * Y_CH + i * Y_TILE + g * 320 + q * 16) = acc[i];
    bar();
    f32x2 sv[4];
    {
        const int t = (2 * tr) & 255;
        const LAS char* yp = lds + (tr >> 7) * Y_TILE + ((t >> 2) & 3) * 320 + (t >> 4) * 16 + (t & 3) * 4;
#pragma unroll
        for (int c = 0; c < 4; c++) {
            const f32x2 y = rbf2(*(const LAS f32x2*)(yp + c * Y_CH) + *(const LAS f32x2*)(yp + (4 + c) * Y_CH));
            sv[c] = rbf2(y + t1[c]);
        }
    }
    const int row0 = 2 * tr + (HEAVY ? 1024 : 0);
    u16* op = a.out + row0 * D_ + 4 * G;
    for (u32 spin = 0; spin < SPIN_CAP && !__all((u32)(selv >> 32) == tag); spin++) { __builtin_amdgcn_s_sleep(2); selv = gld(selp); }
    const u32 s0 = __builtin_amdgcn_readlane((u32)selv, 0), s1 = __builtin_amdgcn_readlane((u32)selv, 1);
    const int i0 = s0 & 2047, i1 = s1 & 2047;
    {
        f32x2 o[4];
#pragma unroll
        for (int c = 0; c < 4; c++) o[c] = xf[c] + rbf2(sv[c] * gt[c]);
        if (row0 != i0 && row0 != i1) *(u32x2*)op = u32x2{pkbf(o[0].x, o[1].x), pkbf(o[2].x, o[3].x)};
        if (row0 + 1 != i0 && row0 + 1 != i1) *(u32x2*)(op + D_) = u32x2{pkbf(o[0].y, o[1].y), pkbf(o[2].y, o[3].y)};
    }
    STAMP(9);
    const bool own0 = (i0 >= 1024) == HEAVY, own1 = (i1 >= 1024) == HEAVY;
    if (own0 || own1) {
        if (h ? own1 : own0) {
            for (u32 spin = 0; spin < SPIN_CAP && !__all((u32)(prtv >> 32) == tag); spin++) { __builtin_amdgcn_s_sleep(2); prtv = gld(prtp); }
            const float y = wave_fsum(lane < NFFN ? __uint_as_float((u32)prtv) : 0.0f);
            if (lane == 0) *(LAS float*)(lds + LDS_UPD + (h * 4 + ch) * 4) = rbf(rbf(y) * bfhi(h ? s1 : s0));
        }
        bar();
#pragma unroll
        for (int e = 0; e < 2; e++) {
            const int row = row0 + e;
            if (row == i0 || row == i1) {
                const f32x4 up = *(const LAS f32x4*)(lds + LDS_UPD + (row == i0 ? 0 : 16));
                float o[4];
#pragma unroll
                for (int c = 0; c < 4; c++) {
                    const float z = rbf(zn[c][e] + up[c]);
                    o[c] = xf[c][e] + rbf(sv[c][e] * rbf(z * sigm(z)));
                }
                *(u32x2*)(op + e * D_) = u32x2{pkbf(o[0], o[1]), pkbf(o[2], o[3])};
            }
        }
    }
    STAMP(10);
}

__global__ void __launch_bounds__(512, 2) s4_mod_fused(Args a) {
    extern __shared__ __attribute__((aligned(16))) char smem[];
    LAS char* lds = (LAS char*)smem;
    const int xcd = blockIdx.x & 7, j = blockIdx.x >> 3;
    const int jj = j & 15;
    if (j >= 16) body<0>(a, lds, xcd, jj);
    else if (jj >= 7) body<1>(a, lds, xcd, jj);
    else body<2>(a, lds, xcd, jj);
}

extern "C" void kernel_launch(void* const* d_in, const int* in_sizes, int n_in, void* d_out, int out_size, void* d_ws,
                              size_t ws_size, hipStream_t stream) {
    Args a;
    a.x = (const u16*)d_in[0]; a.are = (const u16*)d_in[1]; a.aim = (const u16*)d_in[2]; a.bre = (const u16*)d_in[3];
    a.bim = (const u16*)d_in[4]; a.cre = (const u16*)d_in[5]; a.cim = (const u16*)d_in[6]; a.logdt = (const u16*)d_in[7];
    a.dskip = (const u16*)d_in[8]; a.wr = (const u16*)d_in[10]; a.wg = (const u16*)d_in[11]; a.wu = (const u16*)d_in[12];
    a.wd = (const u16*)d_in[13]; a.out = (u16*)d_out; a.ws = (char*)d_ws;
    (void)hipFuncSetAttribute((const void*)s4_mod_fused, hipFuncAttributeMaxDynamicSharedMemorySize, LDS_TOTAL);
    hipLaunchKernelGGL(s4_mod_fused, dim3(256), dim3(512), LDS_TOTAL, stream, a);
}
```

```cpp
#include <hip/hip_runtime.h>
#include <stdint.h>

typedef unsigned short u16;
typedef unsigned int u32;
typedef unsigned long long u64;
typedef float f32x2 __attribute__((ext_vector_type(2)));
typedef float f32x4 __attribute__((ext_vector_type(4)));
typedef u32 u32x2 __attribute__((ext_vector_type(2)));
typedef u32 u32x4 __attribute__((ext_vector_type(4)));
typedef __bf16 bf16x2 __attribute__((ext_vector_type(2)));
typedef __bf16 bf16x8 __attribute__((ext_vector_type(8)));
typedef _Float16 f16x2 __attribute__((ext_vector_type(2)));
typedef _Float16 f16x8 __attribute__((ext_vector_type(8)));
#define GAS __attribute__((address_space(1)))
#define LAS __attribute__((address_space(3)))
typedef GAS u64 gu64;
typedef GAS u32 gu32;
#define RLX_AGENT __ATOMIC_RELAXED, __HIP_MEMORY_SCOPE_AGENT

constexpr int D_ = 512, F_ = 1792;
constexpr int NFFN = 56;
constexpr u32 WS_EPOCH = 0, WS_SEEN = 1024, WS_SEL = 4096, WS_CAND = 8192, WS_STATS = 32768, WS_PART = 262144;
constexpr u32 WS_DBG = 1u << 21;
constexpr int U_CH = (256 + 2048) * 2;
constexpr int LDS_LN = 4 * U_CH;
constexpr int LDS_GU = LDS_LN + 2048;
constexpr int LDS_H = LDS_GU + 4096;
constexpr int LDS_UPD = LDS_H + 1024;
constexpr int LDS_TAG = LDS_UPD + 32;
constexpr int LDS_TBL = LDS_TAG + 32;
constexpr int TBL_ROW = 272, TBL_SZ = 57 * TBL_ROW;
constexpr int LDS_TOTAL = LDS_TBL + 8 * TBL_SZ;
constexpr int C_OFF = 3072 + 288;
constexpr int P_OFF = 4096;
constexpr int Y_TILE = 1280, Y_CH = 4 * Y_TILE;
constexpr u32 SPIN_CAP = 1u << 16;

struct Args {
    const u16 *x, *are, *aim, *bre, *bim, *cre, *cim, *logdt, *dskip, *wr, *wg, *wu, *wd;
    u16* out;
    char* ws;
};

template <typename T> __device__ __forceinline__ T ldg(const void* base, u32 off) { return *(const GAS T*)((const GAS char*)base + off); }
template <typename T> __device__ __forceinline__ void stg(void* base, u32 off, T v) { *(GAS T*)((GAS char*)base + off) = v; }
__device__ __forceinline__ u64 gld(const char* base, u32 off) { return __hip_atomic_load((gu64*)((GAS char*)base + off), RLX_AGENT); }
__device__ __forceinline__ void gst(char* base, u32 off, u32 tag, u32 v) { __hip_atomic_store((gu64*)((GAS char*)base + off), ((u64)tag << 32) | v, RLX_AGENT); }
__device__ __forceinline__ float bflo(u32 v) { return __uint_as_float(v << 16); }
__device__ __forceinline__ float bfhi(u32 v) { return __uint_as_float(v & 0xffff0000u); }
__device__ __forceinline__ u32 pkbf(float a, float b) { f32x2 v = {a, b}; return __builtin_bit_cast(u32, __builtin_convertvector(v, bf16x2)); }
__device__ __forceinline__ u32 pkh(f32x2 v) { return __builtin_bit_cast(u32, __builtin_convertvector(v, f16x2)); }
__device__ __forceinline__ float rbf(float a) { return bflo(pkbf(a, a)); }
__device__ __forceinline__ f32x2 cmul(f32x2 a, f32x2 z, f32x2 zn) {
    return __builtin_elementwise_fma(a.yx, zn, a * z.xx);
}
__device__ __forceinline__ f32x2 csq(f32x2 a) { return cmul(a, a, f32x2{-a.y, a.y}); }
__device__ __forceinline__ void bar() { asm volatile("s_waitcnt lgkmcnt(0)\n\ts_barrier" ::: "memory"); }
#define PIN(x) asm volatile("" : "+v"(x))
__device__ __forceinline__ f32x4 mfma_bf(u32x4 a, u32x4 b, f32x4 c) {
    return __builtin_amdgcn_mfma_f32_16x16x32_bf16(__builtin_bit_cast(bf16x8, a), __builtin_bit_cast(bf16x8, b), c, 0, 0, 0);
}
__device__ __forceinline__ f32x4 mfma_h(u32x4 a, u32x4 b, f32x4 c) {
    return __builtin_amdgcn_mfma_f32_16x16x32_f16(__builtin_bit_cast(f16x8, a), __builtin_bit_cast(f16x8, b), c, 0, 0, 0);
}
template <int CTRL> __device__ __forceinline__ float dppf(float old, float src) {
    return __uint_as_float(__builtin_amdgcn_update_dpp(__float_as_uint(old), __float_as_uint(src), CTRL, 0xf, 0xf, false));
}
template <int CTRL, int RM = 0xf> __device__ __forceinline__ u32 dpp0(u32 src) { return __builtin_amdgcn_update_dpp(0u, src, CTRL, RM, 0xf, false); }
__device__ __forceinline__ u32 row_umax(u32 v) {
    v = max(v, dpp0<0x128>(v)); v = max(v, dpp0<0x124>(v)); v = max(v, dpp0<0x122>(v)); v = max(v, dpp0<0x121>(v));
    return v;
}
__device__ __forceinline__ u32 wave_umax(u32 v) {
    v = row_umax(v); v = max(v, dpp0<0x142, 0xa>(v)); v = max(v, dpp0<0x143, 0xc>(v));
    return __builtin_amdgcn_readlane(v, 63);
}
__device__ __forceinline__ float wave_fsum(float f) {
    f += __uint_as_float(dpp0<0x128>(__float_as_uint(f))); f += __uint_as_float(dpp0<0x124>(__float_as_uint(f)));
    f += __uint_as_float(dpp0<0x122>(__float_as_uint(f))); f += __uint_as_float(dpp0<0x121>(__float_as_uint(f)));
    f += __uint_as_float(dpp0<0x142, 0xa>(__float_as_uint(f))); f += __uint_as_float(dpp0<0x143, 0xc>(__float_as_uint(f)));
    return __uint_as_float(__builtin_amdgcn_readlane(__float_as_uint(f), 63));
}
__device__ __forceinline__ float sigm(float z) { return __builtin_amdgcn_rcpf(1.0f + __builtin_amdgcn_exp2f(z * -1.44269504089f)); }
__device__ __forceinline__ bool allok(bool ok) { return __builtin_amdgcn_ballot_w64(!ok) == 0; }

#define STAMP(i)
#define STAMPS_DECL
#define STAMPS_OUT

template <int ROLE>
__device__ __forceinline__ void body(const Args& a, LAS char* lds, const int xcd, const int jj) {
    constexpr bool HEAVY = ROLE == 0;
    const int tid = threadIdx.x, lane = tid & 63;
    const int w = __builtin_amdgcn_readfirstlane(tid >> 6);
    const int q = lane & 15, g = lane >> 4;
    const int ch = w & 3, h = w >> 2;
    const int G = xcd * 16 + jj;
    const int tr = (tid + 64 * (jj & 7)) & 511;
    const f32x4 z4 = {0, 0, 0, 0};
    STAMPS_DECL;
    STAMP(0);

    u32x4 xa[4], cb[4];
    const int lnb = jj * 8 + xcd;
    if constexpr (ROLE == 1) {
        const u32 xo = (lnb * 32 + (w & 1) * 16 + q) * 1024 + (w >> 1) * 256 + g * 16;
#pragma unroll
        for (int s = 0; s < 4; s++) xa[s] = ldg<u32x4>(a.x, xo + 64 * s);
        const u32 o1 = q == 0 ? 0x3f803f80u : 0u;
#pragma unroll
        for (int s = 0; s < 4; s++) cb[s] = u32x4{o1, o1, o1, o1};
        if (q == 1) {
#pragma unroll
            for (int s = 0; s < 4; s++) cb[s] = ldg<u32x4>(a.wr, (w >> 1) * 256 + g * 16 + 64 * s);
        }
    }
    const u32 po = ((4 * G + ch) * 128 + 64 * h + lane) * 2;
    const u32 p_ldt = ldg<u16>(a.logdt, (4 * G + ch) * 2);
    const u32 p_are = ldg<u16>(a.are, po), p_aim = ldg<u16>(a.aim, po), p_bre = ldg<u16>(a.bre, po), p_bim = ldg<u16>(a.bim, po);
    const u32 p_cre = ldg<u16>(a.cre, po), p_cim = ldg<u16>(a.cim, po);
    const u32 epoch = *(const __attribute__((address_space(4))) u32*)(a.ws + WS_EPOCH);
    u32x2 xc[4];
    const u32 xo = tr * 2048 + G * 8;
    if constexpr (HEAVY) {
        xc[2] = ldg<u32x2>(a.x, xo + 1024 * 1024); xc[3] = ldg<u32x2>(a.x, xo + 1025 * 1024);
        xc[0] = ldg<u32x2>(a.x, xo); xc[1] = ldg<u32x2>(a.x, xo + 1024);
    } else if constexpr (ROLE != 1) {
        xc[2] = ldg<u32x2>(a.x, xo); xc[3] = ldg<u32x2>(a.x, xo + 1024);
    }
    u32 wgu[2][2][8], wdn[2][8];
    const int f = xcd * 7 + jj - 8;
    if constexpr (ROLE == 2) {
        const u32 o = ((64 * w + 8 * g) * F_ + 32 * f + 2 * q) * 2;
#pragma unroll
        for (int s = 0; s < 2; s++)
#pragma unroll
            for (int j = 0; j < 8; j++) {
                wgu[0][s][j] = ldg<u32>((const char*)a.wg + (32 * s + j) * F_ * 2, o);
                wgu[1][s][j] = ldg<u32>((const char*)a.wu + (32 * s + j) * F_ * 2, o);
            }
        const u32 od = ((32 * f + 8 * g) * D_ + 64 * w + 2 * q) * 2;
#pragma unroll
        for (int u = 0; u < 2; u++)
#pragma unroll
            for (int j = 0; j < 8; j++) wdn[u][j] = ldg<u32>((const char*)a.wd + j * D_ * 2 + u * 64, od);
    }
    const u32x2 dsk = ldg<u32x2>(a.dskip, G * 8);
    if (tid < 128) *(LAS u32x4*)(lds + (tid >> 5) * U_CH + (tid & 31) * 16) = u32x4{0, 0, 0, 0};
    u32x4 band;
    if constexpr (HEAVY) {
        const u32 bits = ((h ? 0x7fu : 0x1ffu) << (q + 4 + h)) >> (8 * g);
#pragma unroll
        for (int d = 0; d < 4; d++) band[d] = __umul24(((bits >> (2 * d)) & 1u) | ((bits << (15 - 2 * d)) & 0x10000u), 0x3f80u);
        PIN(band);
    }
    u32 tag;

    if constexpr (ROLE == 1) {
        const u32x4 ones = {0x3f803f80u, 0x3f803f80u, 0x3f803f80u, 0x3f803f80u};
        f32x4 d1 = z4, d2 = z4, d3 = z4;
#pragma unroll
        for (int s = 0; s < 4; s++) { d1 = mfma_bf(xa[s], cb[s], d1); d2 = mfma_bf(xa[s], xa[s], d2); d3 = mfma_bf(ones, cb[s], d3); }
        LAS float* ln = (LAS float*)(lds + LDS_LN) + (w & 1) * 64 + (w >> 1);
        if (q < 2) {
#pragma unroll
            for (int i = 0; i < 4; i++) ln[q * 128 + 16 * g + 4 * i] = d1[i];
            if (lane == 1) ln[384] = d3[0];
        }
        const float da = (q & 1) ? d2[1] : d2[0], db = (q & 1) ? d2[3] : d2[2];
        if ((q >> 2) == g) ln[256 + 4 * q] = (q & 2) ? db : da;
        xc[2] = ldg<u32x2>(a.x, xo); xc[3] = ldg<u32x2>(a.x, xo + 1024);
        if (tid == 0) *(LAS u32*)(lds + LDS_TAG) = epoch + 1;
        bar();
        if (w == 0) {
            __builtin_amdgcn_s_setprio(3);
            tag = epoch + 1;
            const int r = lane & 31, hf = lane >> 5;
            const LAS char* lp = lds + LDS_LN + r * 16 + hf * 512;
            const f32x4 s0 = *(const LAS f32x4*)lp, s1 = *(const LAS f32x4*)(hf ? lds + LDS_LN + 1536 : lp + 1024);
            const float va = (s0[0] + s0[1]) + (s0[2] + s0[3]), vb = (s1[0] + s1[1]) + (s1[2] + s1[3]);
            auto e0 = __builtin_amdgcn_permlane32_swap(__float_as_uint(va), __float_as_uint(va), false, false);
            auto e1 = __builtin_amdgcn_permlane32_swap(__float_as_uint(vb), __float_as_uint(vb), false, false);
            const float mean = __uint_as_float(e0[0]) * (1.0f / D_);
            const float var = fmaxf(__uint_as_float(e1[0]) * (1.0f / D_) - mean * mean, 0.0f);
            const float sd = rbf(__builtin_amdgcn_sqrtf(rbf(rbf(var) + 1e-5f)));
            const u32 ms = pkbf(mean, sd);
            const u32 so = WS_STATS + hf * 16384 + (lnb * 32 + r) * 8;
#pragma unroll
            for (int i = 0; i < 4; i++) gst(a.ws + i * 32768, so, tag, ms);
            const float rr = (__uint_as_float(e0[1]) - bflo(ms) * __uint_as_float(e1[1])) * __builtin_amdgcn_rcpf(sd);
            const u32 rb = pkbf(rr, rr);
            const u32 key = ((rb ^ (((int)rb >> 31) | 0x80000000u)) & 0xffff0000u) | (0xffffu - (lnb * 32 + r));
            u32 k1 = row_umax(key);
            auto m1 = __builtin_amdgcn_permlane16_swap(k1, k1, false, false); k1 = max(m1[0], m1[1]);
            u32 k2 = row_umax(key == k1 ? 0u : key);
            auto m2 = __builtin_amdgcn_permlane16_swap(k2, k2, false, false); k2 = max(m2[0], m2[1]);
            if (lane < 16) gst(a.ws, WS_CAND + (lane >> 1) * 1024 + lnb * 16 + (lane & 1) * 8, tag, (lane & 1) ? k2 : k1);
            __builtin_amdgcn_s_setprio(0);
        }
        STAMP(1);
    } else {
        if (tid == 0) {
            *(LAS u32*)(lds + LDS_TAG) = epoch + 1;
            gst(a.ws, WS_SEEN + blockIdx.x * 8, epoch + 1, 0);
        }
    }

    f32x4 kt[3];
    LAS char* const tw = lds + LDS_TBL + w * TBL_SZ;
    u64 sg[4];
    const u32 so = WS_STATS + xcd * 16384 + tr * 16;
    {
        const float ld = bflo(p_ldt);
        const float t0 = ld * 1.44269504089f;
        const float te = __builtin_fmaf(ld, 1.92596303e-8f, __builtin_fmaf(ld, 1.44269504089f, -t0));
        const float ex = __builtin_amdgcn_exp2f(t0);
        const float dt = rbf(__builtin_fmaf(ex * te, 0.69314718056f, ex));
        const float lr = (dt * 1.44269504089f) * bflo(p_are);
        const float ai = dt * bflo(p_aim);
        const float th = ai * 0.15915494309f;
        const float tl = __builtin_fmaf(ai, 6.4206382e-9f, __builtin_fmaf(ai, 0.15915494309f, -th));
        const float bi = bflo(p_bim);
        const f32x2 W = cmul(f32x2{bflo(p_cre), bflo(p_cim)}, f32x2{bflo(p_bre), bi}, f32x2{-bi, bi}) * dt;
        const float gp = fmaxf(lr, 0.0f);
        const int en = __builtin_amdgcn_frexp_expf(fmaxf(fabsf(W.x), fabsf(W.y))) + (int)(2168.0f * gp);
        const int E = min(max((int)wave_umax((u32)(en + 201)) - 200, -100), 120);
        const float sA = __builtin_amdgcn_ldexpf(__builtin_amdgcn_exp2f(120.0f * gp), 13 - E), sB = __builtin_amdgcn_exp2f(-120.0f * gp);
        const float inv = __uint_as_float((u32)(E - 13 + 127) << 23);
        const float t1 = __builtin_amdgcn_fractf(th), e1 = __builtin_amdgcn_exp2f(lr);
        const f32x2 z1 = f32x2{__builtin_amdgcn_cosf(t1), __builtin_amdgcn_sinf(t1)} * e1;
        const float t7 = __builtin_fmaf(128.0f, tl, __builtin_amdgcn_fractf(128.0f * th)), e7 = __builtin_amdgcn_exp2f(128.0f * lr);
        const f32x2 z128 = f32x2{__builtin_amdgcn_cosf(t7), __builtin_amdgcn_sinf(t7)} * e7;
        const f32x2 z2 = csq(z1), zz4 = csq(z2), z8 = csq(zz4);
        const f32x2 n1 = {-z1.y, z1.y}, n2 = {-z2.y, z2.y}, n4 = {-zz4.y, zz4.y}, n128 = {-z128.y, z128.y};
        const f32x2 c8 = {z8.y, -z8.y};
        LAS u32* tb = (LAS u32*)tw + lane;
        f32x2 X = W * sA, B = {sB, 0.0f};
        constexpr int KX = HEAVY ? 16 : 8;
#pragma unroll
        for (int k = 0; k < 17; k++) {
            if (k < 16) { tb[(41 + k) * 68] = pkh(B); if (k < 15) B = cmul(B, z8, c8); }
            if (k < 2) {
                f32x2 c = X;
#pragma unroll
                for (int i = 0; i < 8; i++) { tb[(8 * k + i) * 68] = pkh(c); if (i < 7) c = cmul(c, z1, n1); }
            } else if (k < 4) {
                f32x2 c = X;
#pragma unroll
                for (int i = 0; i < 4; i++) { tb[(16 + 4 * (k - 2) + i) * 68] = pkh(c); if (i < 3) c = cmul(c, z2, n2); }
            } else if (k < 8) {
                tb[(24 + 2 * (k - 4)) * 68] = pkh(X); tb[(25 + 2 * (k - 4)) * 68] = pkh(cmul(X, zz4, n4));
            } else if (k <= KX) tb[(32 + k - 8) * 68] = pkh(X);
            if (k < KX) X = cmul(X, z128, n128);
        }
        STAMP(2);
        if constexpr (ROLE != 2) {
#pragma unroll
            for (int i = HEAVY ? 0 : 2; i < 4; i++) sg[i] = gld(a.ws + (i & 1) * 8 + ((HEAVY && i >= 2) ? 8192 : 0), so);
        }
        const LAS char* tf = tw + q * TBL_ROW + g * 16;
#pragma unroll
        for (int t = 0; t < 3; t++) kt[t] = z4;
#pragma unroll
        for (int s = 0; s < 4; s++) {
            const u32x4 fb = *(const LAS u32x4*)(tf + 41 * TBL_ROW + s * 64);
#pragma unroll
            for (int t = 0; t < 3; t++) kt[t] = mfma_h(*(const LAS u32x4*)(tf + 16 * t * TBL_ROW + s * 64), fb, kt[t]);
        }
#pragma unroll
        for (int t = 0; t < 3; t++) { kt[t] *= inv; *(LAS f32x4*)(tw + t * 1024 + lane * 16) = kt[t]; }
    }
    bar();
    tag = *(LAS u32*)(lds + LDS_TAG);
#pragma unroll
    for (int t = 0; t < 3; t++) kt[t] += *(const LAS f32x4*)(lds + LDS_TBL + (w ^ 4) * TBL_SZ + t * 1024 + lane * 16);
    LAS char* const P = lds + LDS_TBL + ch * TBL_SZ + P_OFF;
    const float k1024 = __uint_as_float(__builtin_amdgcn_readlane(__float_as_uint(kt[2][0]), 0));
    if (h == 0) {
        const f32x4 v = kt[0];
        const int src = (g & 1) ? lane - 16 : (q ? lane + 15 : lane - 1);
        float pv = __uint_as_float(__builtin_amdgcn_ds_bpermute(src * 4, __float_as_uint(v[3])));
        pv = lane == 0 ? 0.0f : pv;
        *(LAS u32x4*)(P + 4 * (1052 - 128 * (g >> 1) - 8 * q - 4 * (g & 1))) = u32x4{pkbf(v[3], v[2]), pkbf(v[2], v[1]), pkbf(v[1], v[0]), pkbf(v[0], pv)};
        if (lane < 12) {
            const u32 kk = pkbf(k1024, k1024);
            const u32 f7 = lane == 7 ? kk : 0u, f6 = lane == 6 ? kk : 0u;
            *(LAS u32x4*)(P + lane * 16 + (lane >= 8 ? 4096 : 0)) = u32x4{f7, f7, f7 | (f6 & 0xffff0000u), f7 | f6};
        }
    } else {
        const f32x4 v = kt[1];
        const bool s2 = g < 2;
        const float k255 = __uint_as_float(__builtin_amdgcn_readlane(__float_as_uint(kt[0][3]), 63));
        const float nw = dppf<0x130>(v[0], v[0]);
        const float nr = dppf<0x12f>(v[0], q == 0 ? v[2] : v[0]);
        const float nA = s2 ? nw : nr;
        const float nB = dppf<0x130>(k1024, q == 0 ? v[0] : v[2]);
        const float x2 = s2 ? v[1] : v[0], x3 = s2 ? v[2] : v[1], x6 = s2 ? v[3] : nA;
        const u32 aa = pkbf(v[0], v[0]), cc = pkbf(x3, x3);
        LAS char* PA = P + 4 * (1048 - 8 * q - (s2 ? 256 + 128 * g : 256 * g));
        *(LAS u32x4*)PA = u32x4{pkbf(nA, x6), pkbf(x6, x3), cc, cc};
        *(LAS u32x4*)(PA + 16) = u32x4{pkbf(x3, x2), pkbf(x2, v[0]), aa, pkbf(v[0], lane == 0 ? k255 : v[0])};
        if (!s2) {
            const u32 a2 = pkbf(v[2], v[2]), b2 = pkbf(v[3], v[3]);
            *(LAS u32x4*)(PA - 512) = u32x4{pkbf(nB, nB), pkbf(nB, v[3]), b2, b2};
            *(LAS u32x4*)(PA - 496) = u32x4{pkbf(v[3], v[2]), a2, a2, a2};
        }
    }
    u32x4 KT[4];
    if constexpr (HEAVY) {
        LAS char* C = tw + C_OFF;
        if (lane < 4) *(LAS u32x4*)(C + 252 + 4 + lane * 16) = u32x4{0, 0, 0, 0};
        if (lane == 0) *(LAS u32*)(C + 252) = 0;
        if ((q & 1) == h) {
            LAS char* cp = C + 4 * (64 - h - 32 * g - (q >> 1));
#pragma unroll
            for (int i = 0; i < 4; i++) {
                const float vv = (i == 0 && lane == 0) ? 0.0f : kt[2][i];
                const u16 b = (u16)pkbf(vv, vv);
                *(LAS u16*)(cp - 32 * i) = b; *(LAS u16*)(cp - 32 * i - 2) = b;
            }
        }
        const LAS u32* cr = (const LAS u32*)C + 15 - q + 4 * g;
        KT[3] = u32x4{cr[0], cr[2], cr[16], cr[18]};
        KT[2] = u32x4{cr[16], cr[18], cr[32], cr[34]};
        KT[1] = u32x4{cr[32], cr[34], cr[48], cr[50]};
        KT[0] = u32x4{cr[48], cr[50], 0u, 0u};
    }
    bar();
    u32x4 A[17];
    {
        const LAS u32* pr = (const LAS u32*)P + 15 - q + 8 * g;
        if (HEAVY && h == 0) A[16] = u32x4{pr[0], pr[2], pr[4], pr[6]};
        pr += 64 - 32 * h;
#pragma unroll
        for (int m = 0; m < 16; m++) A[m] = u32x4{pr[64 * (15 - m)], pr[64 * (15 - m) + 2], pr[64 * (15 - m) + 4], pr[64 * (15 - m) + 6]};
    }
    STAMP(3);

    if constexpr (ROLE == 2) {
        const u32 co = WS_CAND + xcd * 1024 + lane * 8;
        u64 c0 = gld(a.ws, co), c1 = gld(a.ws + 512, co);
        u32x4 fg[2][2][2], fd[2][2];
#pragma unroll
        for (int p = 0; p < 2; p++) {
            const u32 sel = p ? 0x07060302u : 0x05040100u;
#pragma unroll
            for (int s = 0; s < 2; s++) {
#pragma unroll
                for (int d = 0; d < 4; d++) {
                    fg[0][s][p][d] = __builtin_amdgcn_perm(wgu[0][s][2 * d + 1], wgu[0][s][2 * d], sel);
                    fg[1][s][p][d] = __builtin_amdgcn_perm(wgu[1][s][2 * d + 1], wgu[1][s][2 * d], sel);
                    fd[s][p][d] = __builtin_amdgcn_perm(wdn[s][2 * d + 1], wdn[s][2 * d], sel);
                }
                PIN(fg[0][s][p]); PIN(fg[1][s][p]); PIN(fd[s][p]);
            }
        }
        u32x4 an[2];
        an[0] = an[1] = u32x4{0x3f803f80u, 0x3f803f80u, 0x3f803f80u, 0x3f803f80u};
        PIN(an[0]); PIN(an[1]);
#pragma clang loop unroll(disable)
        for (u32 spin = 0; spin < SPIN_CAP && !allok((u32)(c0 >> 32) == tag && (u32)(c1 >> 32) == tag); spin++) {
            __builtin_amdgcn_s_sleep(2);
            c0 = gld(a.ws, co); c1 = gld(a.ws + 512, co);
        }
        STAMP(4);
        const u32 ka = max((u32)c0, (u32)c1), kb = min((u32)c0, (u32)c1);
        const u32 k1 = wave_umax(ka), k2 = wave_umax(ka == k1 ? kb : ka);
        const u32 kk = (lane & 1) ? k2 : k1;
        const u32 row = ~kk & 2047u;
        if (q < 2) {
            const u32 o = row * 1024 + w * 128 + g * 16;
            an[0] = ldg<u32x4>(a.x, o); an[1] = ldg<u32x4>(a.x, o + 64);
        }
        const u32 sro = WS_STATS + xcd * 16384 + row * 8;
        u64 sr = gld(a.ws, sro);
        sg[2] = gld(a.ws, so); sg[3] = gld(a.ws + 8, so);
        if (jj == 8 && tid < 2) {
            const float sv = sigm(__uint_as_float((kk ^ ((~(int)kk >> 31) | 0x80000000u)) & 0xffff0000u));
            gst(a.ws, WS_SEL + xcd * 128 + lane * 8, tag, row | (pkbf(sv, sv) << 16));
        }
        f32x4 c[2][2];
#pragma unroll
        for (int mt = 0; mt < 2; mt++)
#pragma unroll
            for (int p = 0; p < 2; p++) c[mt][p] = mfma_bf(an[1], fg[mt][1][p], mfma_bf(an[0], fg[mt][0][p], z4));
#pragma clang loop unroll(disable)
        for (u32 spin = 0; spin < SPIN_CAP && !allok((u32)(sr >> 32) == tag); spin++) sr = gld(a.ws, sro);
        const u32 s0 = __builtin_amdgcn_readlane((u32)sr, 0), s1 = __builtin_amdgcn_readlane((u32)sr, 1);
        LAS float* gu = (LAS float*)(lds + LDS_GU);
        if (g == 0) {
#pragma unroll
            for (int mt = 0; mt < 2; mt++) {
                *(LAS f32x2*)(gu + w * 128 + mt * 64 + 2 * q) = f32x2{c[mt][0][0], c[mt][1][0]} - bflo(s0) * f32x2{c[mt][0][2], c[mt][1][2]};
                *(LAS f32x2*)(gu + w * 128 + mt * 64 + 32 + 2 * q) = f32x2{c[mt][0][1], c[mt][1][1]} - bflo(s1) * f32x2{c[mt][0][2], c[mt][1][2]};
            }
        }
        const float rs = __builtin_amdgcn_rcpf(bfhi(lane < 32 ? s0 : s1));
        bar();
        f32x2 gs = {0.0f, 0.0f};
#pragma unroll
        for (int i = 0; i < 8; i++) gs += f32x2{gu[i * 128 + lane], gu[i * 128 + 64 + lane]};
        const u32 gb = pkbf(gs.x * rs, gs.y * rs);
        const float ga = bflo(gb);
        const float hv = rbf(ga * sigm(ga)) * bfhi(gb);
        LAS u16* hb = (LAS u16*)(lds + LDS_H + w * 128);
        hb[lane] = (u16)pkbf(hv, hv);
        const u32x4 ah = *(const LAS u32x4*)(hb + (q & 1) * 32 + 8 * g);
        const f32x4 c00 = mfma_bf(ah, fd[0][0], z4), c01 = mfma_bf(ah, fd[0][1], z4), c10 = mfma_bf(ah, fd[1][0], z4), c11 = mfma_bf(ah, fd[1][1], z4);
        const float u0 = (g & 1) ? c01[0] : c00[0], u1 = (g & 1) ? c11[0] : c10[0], v0 = (g & 1) ? c01[1] : c00[1], v1 = (g & 1) ? c11[1] : c10[1];
        const u32 pp = WS_PART + (((16 * w + 8 * (g >> 1) + (q >> 1)) * NFFN + f) * 4 + 2 * (q & 1) + (g & 1)) * 8;
        gst(a.ws, pp, tag, __float_as_uint((g & 2) ? u1 : u0));
        gst(a.ws + 128 * NFFN * 32, pp, tag, __float_as_uint((g & 2) ? v1 : v0));
        STAMP(5);
    }

    {
        bool ok = (u32)(sg[2] >> 32) == tag && (u32)(sg[3] >> 32) == tag;
        if constexpr (HEAVY) ok = ok && (u32)(sg[0] >> 32) == tag && (u32)(sg[1] >> 32) == tag;
#pragma clang loop unroll(disable)
        for (u32 spin = 0; spin < SPIN_CAP && !allok(ok); spin++) {
            ok = true;
#pragma unroll
            for (int i = HEAVY ? 0 : 2; i < 4; i++) {
                sg[i] = gld(a.ws + (i & 1) * 8 + ((HEAVY && i >= 2) ? 8192 : 0), so); ok = ok && (u32)(sg[i] >> 32) == tag;
            }
        }
    }
    STAMP(6);
    f32x2 xf[4], zn[4];
#pragma unroll
    for (int p = HEAVY ? 0 : 1; p < 2; p++) {
        const u32 sa = (u32)sg[2 * p], sb = (u32)sg[2 * p + 1];
        const f32x2 rs = {__builtin_amdgcn_rcpf(bfhi(sa)), __builtin_amdgcn_rcpf(bfhi(sb))};
        const f32x2 nb = f32x2{bflo(sa), bflo(sb)} * rs;
#pragma unroll
        for (int c = 0; c < 4; c++) {
            const u32 w0 = xc[2 * p][c >> 1], w1 = xc[2 * p + 1][c >> 1];
            const f32x2 xv = (c & 1) ? f32x2{bfhi(w0), bfhi(w1)} : f32x2{bflo(w0), bflo(w1)};
            const f32x2 z = __builtin_elementwise_fma(xv, rs, -nb);
            if (p == 1) { xf[c] = xv; zn[c] = z; }
            *(LAS u32*)(lds + c * U_CH + 512 + 4 * tr + ((HEAVY && p) ? 2048 : 0)) = pkbf(z.x, z.y);
        }
    }
    bar();
    STAMP(7);

    f32x4 acc[4];
#pragma unroll
    for (int i = 0; i < 4; i++) acc[i] = z4;
    const LAS char* ub = lds + ch * U_CH + 480 + 32 * q + 16 * g - 64 * h + (HEAVY ? 2048 : 0);
    if constexpr (HEAVY) {
        const LAS char* uw = lds + ch * U_CH + 464 + 32 * q + 16 * g + 16 * h;
        f32x4 dw[4];
#pragma unroll
        for (int t = 0; t < 4; t++) dw[t] = mfma_bf(*(const LAS u32x4*)(uw + 512 * t), band, z4);
        if (h == 0) {
#pragma unroll
            for (int i = 0; i < 4; i++) acc[i] = mfma_bf(A[16], *(const LAS u32x4*)(ub + 128 * (4 * i - 16)), z4);
        }
        const u32x4 S0 = {pkbf(dw[0][0], dw[0][1]), pkbf(dw[0][2], dw[0][3]), pkbf(dw[1][0], dw[1][1]), pkbf(dw[1][2], dw[1][3])};
        const u32x4 S1 = {pkbf(dw[2][0], dw[2][1]), pkbf(dw[2][2], dw[2][3]), pkbf(dw[3][0], dw[3][1]), pkbf(dw[3][2], dw[3][3])};
#pragma unroll
        for (int i = 0; i < 4; i++) acc[i] = mfma_bf(S0, KT[i], acc[i]);
        acc[2] = mfma_bf(S1, KT[0], acc[2]); acc[3] = mfma_bf(S1, KT[1], acc[3]);
    }
#pragma unroll
    for (int b = 12; b >= (HEAVY ? -15 : -3); b--) {
        const u32x4 B = *(const LAS u32x4*)(ub + 128 * b);
#pragma unroll
        for (int i = 0; i < 4; i++) {
            const int mp = 4 * i - b;
            if (mp >= 0 && mp < 16) acc[i] = mfma_bf(A[mp], B, acc[i]);
        }
    }
    f32x2 t1[4], gt[4];
#pragma unroll
    for (int c = 0; c < 4; c++) {
        t1[c] = zn[c] * ((c & 1) ? bfhi(dsk[c >> 1]) : bflo(dsk[c >> 1]));
        const f32x2 e = zn[c] * -1.44269504089f;
        const f32x2 d = f32x2{__builtin_amdgcn_exp2f(e.x), __builtin_amdgcn_exp2f(e.y)} + 1.0f;
        gt[c] = zn[c] * f32x2{__builtin_amdgcn_rcpf(d.x), __builtin_amdgcn_rcpf(d.y)};
    }
    const u32 selo = WS_SEL + xcd * 128 + (lane & 1) * 8;
    const u32 prto = WS_PART + ((h * 128 + G) * NFFN + min(lane, NFFN - 1)) * 32 + ch * 8;
    u64 selv = gld(a.ws, selo), prtv = gld(a.ws, prto);
    u64 seen = 0;
    if (HEAVY && G == 0 && tid < 256) seen = gld(a.ws, WS_SEEN + tid * 8);
    STAMP(8);
    LAS char* const Y = lds + LDS_TBL;
#pragma unroll
    for (int i = 0; i < 4; i++) *(LAS f32x4*)(Y + w * Y_CH + i * Y_TILE + g * 320 + q * 16) = acc[i];
    bar();
    f32x2 sv[4], o[4];
    {
        const int t = (2 * tr) & 255;
        const LAS char* yp = Y + (tr >> 7) * Y_TILE + ((t >> 2) & 3) * 320 + (t >> 4) * 16 + (t & 3) * 4;
#pragma unroll
        for (int c = 0; c < 4; c++) {
            sv[c] = (*(const LAS f32x2*)(yp + c * Y_CH) + t1[c]) + *(const LAS f32x2*)(yp + (4 + c) * Y_CH);
            o[c] = __builtin_elementwise_fma(sv[c], gt[c], xf[c]);
        }
    }
    const u32 oo = xo + (HEAVY ? 1024 * 1024 : 0);
    stg(a.out, oo, u32x2{pkbf(o[0].x, o[1].x), pkbf(o[2].x, o[3].x)});
    stg(a.out, oo + 1024, u32x2{pkbf(o[0].y, o[1].y), pkbf(o[2].y, o[3].y)});
    STAMP(9);
#pragma clang loop unroll(disable)
    for (u32 spin = 0; spin < SPIN_CAP && !allok((u32)(selv >> 32) == tag); spin++) { __builtin_amdgcn_s_sleep(2); selv = gld(a.ws, selo); }
    const u32 s0 = __builtin_amdgcn_readlane((u32)selv, 0), s1 = __builtin_amdgcn_readlane((u32)selv, 1);
    const int i0 = s0 & 2047, i1 = s1 & 2047;
    const bool own0 = (i0 >= 1024) == HEAVY, own1 = (i1 >= 1024) == HEAVY;
    if (own0 || own1) {
        if (h ? own1 : own0) {
#pragma clang loop unroll(disable)
            for (u32 spin = 0; spin < SPIN_CAP && !allok((u32)(prtv >> 32) == tag); spin++) { __builtin_amdgcn_s_sleep(2); prtv = gld(a.ws, prto); }
            const float y = wave_fsum(lane < NFFN ? __uint_as_float((u32)prtv) : 0.0f);
            if (lane == 0) *(LAS float*)(lds + LDS_UPD + w * 4) = rbf(rbf(y) * bfhi(h ? s1 : s0));
        }
        bar();
        const int row0 = 2 * tr + (HEAVY ? 1024 : 0);
#pragma unroll
        for (int e = 0; e < 2; e++) {
            const int row = row0 + e;
            if (row == i0 || row == i1) {
                const f32x4 up = *(const LAS f32x4*)(lds + LDS_UPD + (row == i0 ? 0 : 16));
                float oe[4];
#pragma unroll
                for (int c = 0; c < 4; c++) {
                    const float z = rbf(rbf(zn[c][e]) + up[c]);
                    oe[c] = __builtin_fmaf(sv[c][e], z * sigm(z), xf[c][e]);
                }
                stg(a.out, oo + e * 1024, u32x2{pkbf(oe[0], oe[1]), pkbf(oe[2], oe[3])});
            }
        }
    }
    if (HEAVY && G == 0) {
        if (tid < 256) {
            const bool ln = ((tid >> 3) & 31) < 8;
#pragma clang loop unroll(disable)
            for (u32 spin = 0; spin < SPIN_CAP && !allok(ln || (u32)(seen >> 32) == tag); spin++) seen = gld(a.ws, WS_SEEN + tid * 8);
        }
        bar();
        if (tid == 0) __hip_atomic_store((gu32*)(a.ws + WS_EPOCH), tag, RLX_AGENT);
    }
    STAMPS_OUT;
}

__global__ void __launch_bounds__(512, 2) s4_mod_fused(Args a) {
    extern __shared__ __attribute__((aligned(16))) char smem[];
    LAS char* lds = (LAS char*)smem;
    const int xcd = blockIdx.x & 7, j = blockIdx.x >> 3;
    const int jj = j & 15;
    if (j >= 16) body<0>(a, lds, xcd, jj);
    else if (jj < 8) body<1>(a, lds, xcd, jj);
    else if (jj < 15) body<2>(a, lds, xcd, jj);
    else body<3>(a, lds, xcd, jj);
}
#define ROLE_OF(b) (((b) >> 3) >= 16 ? 0 : ((((b) >> 3) & 15) >= 8 && (((b) >> 3) & 15) < 15 ? 2 : 1))

extern "C" void kernel_launch(void* const* d_in, const int* in_sizes, int n_in, void* d_out, int out_size, void* d_ws,
                              size_t ws_size, hipStream_t stream) {
    Args a;
    a.x = (const u16*)d_in[0]; a.are = (const u16*)d_in[1]; a.aim = (const u16*)d_in[2]; a.bre = (const u16*)d_in[3];
    a.bim = (const u16*)d_in[4]; a.cre = (const u16*)d_in[5]; a.cim = (const u16*)d_in[6]; a.logdt = (const u16*)d_in[7];
    a.dskip = (const u16*)d_in[8]; a.wr = (const u16*)d_in[10]; a.wg = (const u16*)d_in[11]; a.wu = (const u16*)d_in[12];
    a.wd = (const u16*)d_in[13]; a.out = (u16*)d_out; a.ws = (char*)d_ws;
    (void)hipFuncSetAttribute((const void*)s4_mod_fused, hipFuncAttributeMaxDynamicSharedMemorySize, LDS_TOTAL);
    hipLaunchKernelGGL(s4_mod_fused, dim3(256), dim3(512), LDS_TOTAL, stream, a);
}
```

```cpp
#include <hip/hip_runtime.h>
#include <stdint.h>

typedef unsigned short u16;
typedef unsigned int u32;
typedef unsigned long long u64;
typedef float f32x2 __attribute__((ext_vector_type(2)));
typedef float f32x4 __attribute__((ext_vector_type(4)));
typedef u32 u32x2 __attribute__((ext_vector_type(2)));
typedef u32 u32x4 __attribute__((ext_vector_type(4)));
typedef __bf16 bf16x2 __attribute__((ext_vector_type(2)));
typedef __bf16 bf16x8 __attribute__((ext_vector_type(8)));
typedef _Float16 f16x2 __attribute__((ext_vector_type(2)));
typedef _Float16 f16x8 __attribute__((ext_vector_type(8)));
#define GAS __attribute__((address_space(1)))
#define LAS __attribute__((address_space(3)))
#define CAS __attribute__((address_space(4)))
typedef GAS u64 gu64;
typedef GAS u32 gu32;
#define RLX_AGENT __ATOMIC_RELAXED, __HIP_MEMORY_SCOPE_AGENT

constexpr int D_ = 512, F_ = 1792;
constexpr int NFFN = 56;
constexpr u32 WS_EPOCH = 0, WS_SEEN = 1024, WS_SEL = 4096, WS_CAND = 8192, WS_STATS = 32768, WS_PART = 262144;
constexpr u32 WS_DBG = 1u << 21;
constexpr int U_CH = (256 + 2048) * 2;
constexpr int LDS_LN = 4 * U_CH;
constexpr int LDS_GU = LDS_LN + 2048;
constexpr int LDS_H = LDS_GU + 4096;
constexpr int LDS_UPD = LDS_H + 1024;
constexpr int LDS_TAG = LDS_UPD + 32;
constexpr int LDS_TBL = LDS_TAG + 32;
constexpr int TBL_ROW = 272, TBL_SZ = 57 * TBL_ROW;
constexpr int LDS_TOTAL = LDS_TBL + 8 * TBL_SZ;
constexpr int C_OFF = 3072 + 288;
constexpr int P_OFF = 4096;
constexpr int Y_TILE = 1280, Y_CH = 4 * Y_TILE;
constexpr u32 SPIN_CAP = 1u << 16;

struct Args {
    const u16 *x, *are, *aim, *bre, *bim, *cre, *cim, *logdt, *dskip, *wr, *wg, *wu, *wd;
    u16* out;
    char* ws;
};

template <typename T> __device__ __forceinline__ T ldg(const void* base, u32 off) { return *(const GAS T*)((const GAS char*)base + off); }
template <typename T> __device__ __forceinline__ void stg(void* base, u32 off, T v) { *(GAS T*)((GAS char*)base + off) = v; }
__device__ __forceinline__ u64 gld(const char* base, u32 off) { return __hip_atomic_load((gu64*)((GAS char*)base + off), RLX_AGENT); }
__device__ __forceinline__ void gst(char* base, u32 off, u32 tag, u32 v) { __hip_atomic_store((gu64*)((GAS char*)base + off), ((u64)tag << 32) | v, RLX_AGENT); }
__device__ __forceinline__ float bflo(u32 v) { return __uint_as_float(v << 16); }
__device__ __forceinline__ float bfhi(u32 v) { return __uint_as_float(v & 0xffff0000u); }
__device__ __forceinline__ u32 pkbf(float a, float b) { f32x2 v = {a, b}; return __builtin_bit_cast(u32, __builtin_convertvector(v, bf16x2)); }
__device__ __forceinline__ u32 pkh(f32x2 v) { return __builtin_bit_cast(u32, __builtin_convertvector(v, f16x2)); }
__device__ __forceinline__ float rbf(float a) { return bflo(pkbf(a, a)); }
__device__ __forceinline__ f32x2 cmul(f32x2 a, f32x2 z, f32x2 zn) {
    return __builtin_elementwise_fma(a.yx, zn, a * z.xx);
}
__device__ __forceinline__ f32x2 csq(f32x2 a) { return cmul(a, a, f32x2{-a.y, a.y}); }
__device__ __forceinline__ void bar() { asm volatile("s_waitcnt lgkmcnt(0)\n\ts_barrier" ::: "memory"); }
#define PIN(x) asm volatile("" : "+v"(x))
__device__ __forceinline__ f32x4 mfma_bf(u32x4 a, u32x4 b, f32x4 c) {
    return __builtin_amdgcn_mfma_f32_16x16x32_bf16(__builtin_bit_cast(bf16x8, a), __builtin_bit_cast(bf16x8, b), c, 0, 0, 0);
}
__device__ __forceinline__ f32x4 mfma_h(u32x4 a, u32x4 b, f32x4 c) {
    return __builtin_amdgcn_mfma_f32_16x16x32_f16(__builtin_bit_cast(f16x8, a), __builtin_bit_cast(f16x8, b), c, 0, 0, 0);
}
template <int CTRL> __device__ __forceinline__ float dppf(float old, float src) {
    return __uint_as_float(__builtin_amdgcn_update_dpp(__float_as_uint(old), __float_as_uint(src), CTRL, 0xf, 0xf, false));
}
template <int CTRL, int RM = 0xf> __device__ __forceinline__ u32 dpp0(u32 src) { return __builtin_amdgcn_update_dpp(0u, src, CTRL, RM, 0xf, false); }
__device__ __forceinline__ u32 row_umax(u32 v) {
    v = max(v, dpp0<0x128>(v)); v = max(v, dpp0<0x124>(v)); v = max(v, dpp0<0x122>(v)); v = max(v, dpp0<0x121>(v));
    return v;
}
__device__ __forceinline__ u32 wave_umax(u32 v) {
    v = row_umax(v); v = max(v, dpp0<0x142, 0xa>(v)); v = max(v, dpp0<0x143, 0xc>(v));
    return __builtin_amdgcn_readlane(v, 63);
}
__device__ __forceinline__ float wave_fsum(float f) {
    f += __uint_as_float(dpp0<0x128>(__float_as_uint(f))); f += __uint_as_float(dpp0<0x124>(__float_as_uint(f)));
    f += __uint_as_float(dpp0<0x122>(__float_as_uint(f))); f += __uint_as_float(dpp0<0x121>(__float_as_uint(f)));
    f += __uint_as_float(dpp0<0x142, 0xa>(__float_as_uint(f))); f += __uint_as_float(dpp0<0x143, 0xc>(__float_as_uint(f)));
    return __uint_as_float(__builtin_amdgcn_readlane(__float_as_uint(f), 63));
}
__device__ __forceinline__ float sigm(float z) { return __builtin_amdgcn_rcpf(1.0f + __builtin_amdgcn_exp2f(z * -1.44269504089f)); }
__device__ __forceinline__ bool allok(bool ok) { return __builtin_amdgcn_ballot_w64(!ok) == 0; }

#define STAMP(i)
#define STAMPS_DECL
#define STAMPS_OUT

template <int ROLE>
__device__ __forceinline__ void body(const Args& a, LAS char* lds, const int xcd, const int jj) {
    constexpr bool HEAVY = ROLE == 0;
    const int tid = threadIdx.x, lane = tid & 63;
    const int w = __builtin_amdgcn_readfirstlane(tid >> 6);
    const int q = lane & 15, g = lane >> 4;
    const int ch = w & 3, h = w >> 2;
    const int G = xcd * 16 + jj;
    const int tr = (tid + 64 * (jj & 7)) & 511;
    const f32x4 z4 = {0, 0, 0, 0};
    STAMPS_DECL;
    STAMP(0);

    u32x4 xa[4], cb[4];
    const int lnb = jj * 8 + xcd;
    if constexpr (ROLE == 1) {
        const u32 xo = (lnb * 32 + (w & 1) * 16 + q) * 1024 + (w >> 1) * 256 + g * 16;
#pragma unroll
        for (int s = 0; s < 4; s++) xa[s] = ldg<u32x4>(a.x, xo + 64 * s);
        const u32 o1 = q == 0 ? 0x3f803f80u : 0u;
#pragma unroll
        for (int s = 0; s < 4; s++) cb[s] = u32x4{o1, o1, o1, o1};
        if (q == 1) {
#pragma unroll
            for (int s = 0; s < 4; s++) cb[s] = ldg<u32x4>(a.wr, (w >> 1) * 256 + g * 16 + 64 * s);
        }
    }
    const u32 po = ((4 * G + ch) * 128 + 64 * h + lane) * 2;
    u32x2 ldt2 = *(const CAS u32x2*)((const CAS char*)a.logdt + G * 8);
    u32x2 dsk = *(const CAS u32x2*)((const CAS char*)a.dskip + G * 8);
    const u32 p_are = ldg<u16>(a.are, po), p_aim = ldg<u16>(a.aim, po), p_bre = ldg<u16>(a.bre, po), p_bim = ldg<u16>(a.bim, po);
    const u32 p_cre = ldg<u16>(a.cre, po), p_cim = ldg<u16>(a.cim, po);
    u32 epoch = *(const CAS u32*)(a.ws + WS_EPOCH);
    const u32 xo = tr * 2048 + G * 8;
    u32 warm = 0;
    if (HEAVY) warm = ldg<u32>(a.x, (jj * 128 + w * 16 + q) * 1024 + G * 8);
    if (tid < 128) *(LAS u32x4*)(lds + (tid >> 5) * U_CH + (tid & 31) * 16) = u32x4{0, 0, 0, 0};
    u32x4 band;
    if constexpr (HEAVY) {
        const u32 bits = ((h ? 0x7fu : 0x1ffu) << (q + 4 + h)) >> (8 * g);
#pragma unroll
        for (int d = 0; d < 4; d++) band[d] = __umul24(((bits >> (2 * d)) & 1u) | ((bits << (15 - 2 * d)) & 0x10000u), 0x3f80u);
        PIN(band);
    }
    u32 ya = LDS_TBL + w * Y_CH + g * 320 + q * 16;
    u32 yb = LDS_TBL + (tr >> 7) * Y_TILE + ((tr >> 1) & 3) * 320 + ((tr >> 3) & 15) * 16 + (tr & 1) * 8;
    u32 selo = WS_SEL + xcd * 128 + (lane & 1) * 8;
    u32 prto = WS_PART + ((h * 128 + G) * NFFN + min(lane, NFFN - 1)) * 32 + ch * 8;
    u32 so = WS_STATS + xcd * 16384 + tr * 16;
    PIN(ya); PIN(yb); PIN(selo); PIN(prto); PIN(so);
    asm volatile("" : "+s"(epoch), "+s"(ldt2), "+s"(dsk));
    u32 tag;
    u32x2 xc[4];
    u32 wgu[2][2][8], wdn[2][8];
    const int f = xcd * 7 + jj - 8;

    if constexpr (ROLE == 1) {
        const u32x4 ones = {0x3f803f80u, 0x3f803f80u, 0x3f803f80u, 0x3f803f80u};
        f32x4 d1 = z4, d2 = z4, d3 = z4;
#pragma unroll
        for (int s = 0; s < 4; s++) { d1 = mfma_bf(xa[s], cb[s], d1); d2 = mfma_bf(xa[s], xa[s], d2); d3 = mfma_bf(ones, cb[s], d3); }
        LAS float* ln = (LAS float*)(lds + LDS_LN) + (w & 1) * 64 + (w >> 1);
        if (q < 2) {
#pragma unroll
            for (int i = 0; i < 4; i++) ln[q * 128 + 16 * g + 4 * i] = d1[i];
            if (lane == 1) ln[384] = d3[0];
        }
        const float da = (q & 1) ? d2[1] : d2[0], db = (q & 1) ? d2[3] : d2[2];
        if ((q >> 2) == g) ln[256 + 4 * q] = (q & 2) ? db : da;
        xc[2] = ldg<u32x2>(a.x, xo); xc[3] = ldg<u32x2>(a.x, xo + 1024);
        if (tid == 0) *(LAS u32*)(lds + LDS_TAG) = epoch + 1;
        bar();
        if (w == 0) {
            __builtin_amdgcn_s_setprio(3);
            tag = epoch + 1;
            const int r = lane & 31, hf = lane >> 5;
            const LAS char* lp = lds + LDS_LN + r * 16 + hf * 512;
            const f32x4 s0 = *(const LAS f32x4*)lp, s1 = *(const LAS f32x4*)(hf ? lds + LDS_LN + 1536 : lp + 1024);
            const float va = (s0[0] + s0[1]) + (s0[2] + s0[3]), vb = (s1[0] + s1[1]) + (s1[2] + s1[3]);
            auto e0 = __builtin_amdgcn_permlane32_swap(__float_as_uint(va), __float_as_uint(va), false, false);
            auto e1 = __builtin_amdgcn_permlane32_swap(__float_as_uint(vb), __float_as_uint(vb), false, false);
            const float mean = __uint_as_float(e0[0]) * (1.0f / D_);
            const float var = fmaxf(__uint_as_float(e1[0]) * (1.0f / D_) - mean * mean, 0.0f);
            const float sd = rbf(__builtin_amdgcn_sqrtf(rbf(rbf(var) + 1e-5f)));
            const u32 ms = pkbf(mean, sd);
            const u32 so = WS_STATS + hf * 16384 + (lnb * 32 + r) * 8;
#pragma unroll
            for (int i = 0; i < 4; i++) gst(a.ws + i * 32768, so, tag, ms);
            const float rr = (__uint_as_float(e0[1]) - bflo(ms) * __uint_as_float(e1[1])) * __builtin_amdgcn_rcpf(sd);
            const u32 rb = pkbf(rr, rr);
            const u32 key = ((rb ^ (((int)rb >> 31) | 0x80000000u)) & 0xffff0000u) | (0xffffu - (lnb * 32 + r));
            u32 k1 = row_umax(key);
            auto m1 = __builtin_amdgcn_permlane16_swap(k1, k1, false, false); k1 = max(m1[0], m1[1]);
            u32 k2 = row_umax(key == k1 ? 0u : key);
            auto m2 = __builtin_amdgcn_permlane16_swap(k2, k2, false, false); k2 = max(m2[0], m2[1]);
            if (lane < 16) gst(a.ws, WS_CAND + (lane >> 1) * 1024 + lnb * 16 + (lane & 1) * 8, tag, (lane & 1) ? k2 : k1);
            __builtin_amdgcn_s_setprio(0);
        }
        STAMP(1);
    } else {
        if (tid == 0) {
            *(LAS u32*)(lds + LDS_TAG) = epoch + 1;
            gst(a.ws, WS_SEEN + blockIdx.x * 8, epoch + 1, 0);
        }
    }

    f32x4 kt[3];
    LAS char* const tw = lds + LDS_TBL + w * TBL_SZ;
    u64 sg[4];
    {
        const float ld = __uint_as_float((ldt2[ch >> 1] << ((ch & 1) ? 0 : 16)) & 0xffff0000u);
        const float t0 = ld * 1.44269504089f;
        const float te = __builtin_fmaf(ld, 1.92596303e-8f, __builtin_fmaf(ld, 1.44269504089f, -t0));
        const float ex = __builtin_amdgcn_exp2f(t0);
        const float dt = rbf(__builtin_fmaf(ex * te, 0.69314718056f, ex));
        float dtp = dt;
        PIN(dtp);
        u32 pb = p_cim;
        asm volatile("" : "+v"(pb) :: "memory");
    if constexpr (HEAVY) {
        xc[2] = ldg<u32x2>(a.x, xo + 1024 * 1024); xc[3] = ldg<u32x2>(a.x, xo + 1025 * 1024);
        xc[0] = ldg<u32x2>(a.x, xo); xc[1] = ldg<u32x2>(a.x, xo + 1024);
    } else if constexpr (ROLE != 1) {
        xc[2] = ldg<u32x2>(a.x, xo); xc[3] = ldg<u32x2>(a.x, xo + 1024);
    }
    if constexpr (ROLE == 2) {
        const u32 o = ((64 * w + 8 * g) * F_ + 32 * f + 2 * q) * 2;
#pragma unroll
        for (int s = 0; s < 2; s++)
#pragma unroll
            for (int j = 0; j < 8; j++) {
                wgu[0][s][j] = ldg<u32>((const char*)a.wg + (32 * s + j) * F_ * 2, o);
                wgu[1][s][j] = ldg<u32>((const char*)a.wu + (32 * s + j) * F_ * 2, o);
            }
        const u32 od = ((32 * f + 8 * g) * D_ + 64 * w + 2 * q) * 2;
#pragma unroll
        for (int u = 0; u < 2; u++)
#pragma unroll
            for (int j = 0; j < 8; j++) wdn[u][j] = ldg<u32>((const char*)a.wd + j * D_ * 2 + u * 64, od);
    }
        __builtin_amdgcn_sched_barrier(0);
        const float lr = (dtp * 1.44269504089f) * bflo(p_are);
        const float ai = dtp * bflo(p_aim);
        const float th = ai * 0.15915494309f;
        const float tl = __builtin_fmaf(ai, 6.4206382e-9f, __builtin_fmaf(ai, 0.15915494309f, -th));
        const float bi = bflo(p_bim);
        const f32x2 W = cmul(f32x2{bflo(p_cre), bflo(pb)}, f32x2{bflo(p_bre), bi}, f32x2{-bi, bi}) * dtp;
        const float gp = fmaxf(lr, 0.0f);
        const int en = __builtin_amdgcn_frexp_expf(fmaxf(fabsf(W.x), fabsf(W.y))) + (int)(2168.0f * gp);
        const int E = min(max((int)wave_umax((u32)(en + 201)) - 200, -100), 120);
        const float sA = __builtin_amdgcn_ldexpf(__builtin_amdgcn_exp2f(120.0f * gp), 13 - E), sB = __builtin_amdgcn_exp2f(-120.0f * gp);
        const float inv = __uint_as_float((u32)(E - 13 + 127) << 23);
        const float t1 = __builtin_amdgcn_fractf(th), e1 = __builtin_amdgcn_exp2f(lr);
        const f32x2 z1 = f32x2{__builtin_amdgcn_cosf(t1), __builtin_amdgcn_sinf(t1)} * e1;
        const float t7 = __builtin_fmaf(128.0f, tl, __builtin_amdgcn_fractf(128.0f * th)), e7 = __builtin_amdgcn_exp2f(128.0f * lr);
        const f32x2 z128 = f32x2{__builtin_amdgcn_cosf(t7), __builtin_amdgcn_sinf(t7)} * e7;
        const f32x2 z2 = csq(z1), zz4 = csq(z2), z8 = csq(zz4);
        const f32x2 n1 = {-z1.y, z1.y}, n2 = {-z2.y, z2.y}, n4 = {-zz4.y, zz4.y}, n128 = {-z128.y, z128.y};
        const f32x2 c8 = {z8.y, -z8.y};
        LAS u32* tb = (LAS u32*)tw + lane;
        f32x2 X = W * sA, B = {sB, 0.0f};
        constexpr int KX = HEAVY ? 16 : 8;
#pragma unroll
        for (int k = 0; k < 17; k++) {
            if (k < 16) { tb[(41 + k) * 68] = pkh(B); if (k < 15) B = cmul(B, z8, c8); }
            if (k < 2) {
                f32x2 c = X;
#pragma unroll
                for (int i = 0; i < 8; i++) { tb[(8 * k + i) * 68] = pkh(c); if (i < 7) c = cmul(c, z1, n1); }
            } else if (k < 4) {
                f32x2 c = X;
#pragma unroll
                for (int i = 0; i < 4; i++) { tb[(16 + 4 * (k - 2) + i) * 68] = pkh(c); if (i < 3) c = cmul(c, z2, n2); }
            } else if (k < 8) {
                tb[(24 + 2 * (k - 4)) * 68] = pkh(X); tb[(25 + 2 * (k - 4)) * 68] = pkh(cmul(X, zz4, n4));
            } else if (k <= KX) tb[(32 + k - 8) * 68] = pkh(X);
            if (k < KX) X = cmul(X, z128, n128);
        }
        STAMP(2);
        if constexpr (ROLE != 2) {
#pragma unroll
            for (int i = HEAVY ? 0 : 2; i < 4; i++) sg[i] = gld(a.ws + (i & 1) * 8 + ((HEAVY && i >= 2) ? 8192 : 0), so);
        }
        const LAS char* tf = tw + q * TBL_ROW + g * 16;
#pragma unroll
        for (int t = 0; t < 3; t++) kt[t] = z4;
#pragma unroll
        for (int s = 0; s < 4; s++) {
            const u32x4 fb = *(const LAS u32x4*)(tf + 41 * TBL_ROW + s * 64);
#pragma unroll
            for (int t = 0; t < 3; t++) kt[t] = mfma_h(*(const LAS u32x4*)(tf + 16 * t * TBL_ROW + s * 64), fb, kt[t]);
        }
#pragma unroll
        for (int t = 0; t < 3; t++) { kt[t] *= inv; *(LAS f32x4*)(tw + t * 1024 + lane * 16) = kt[t]; }
    }
    bar();
    tag = *(LAS u32*)(lds + LDS_TAG);
#pragma unroll
    for (int t = 0; t < 3; t++) kt[t] += *(const LAS f32x4*)(lds + LDS_TBL + (w ^ 4) * TBL_SZ + t * 1024 + lane * 16);
    LAS char* const P = lds + LDS_TBL + ch * TBL_SZ + P_OFF;
    const float k1024 = __uint_as_float(__builtin_amdgcn_readlane(__float_as_uint(kt[2][0]), 0));
    if (h == 0) {
        const f32x4 v = kt[0];
        const int src = (g & 1) ? lane - 16 : (q ? lane + 15 : lane - 1);
        float pv = __uint_as_float(__builtin_amdgcn_ds_bpermute(src * 4, __float_as_uint(v[3])));
        pv = lane == 0 ? 0.0f : pv;
        *(LAS u32x4*)(P + 4 * (1052 - 128 * (g >> 1) - 8 * q - 4 * (g & 1))) = u32x4{pkbf(v[3], v[2]), pkbf(v[2], v[1]), pkbf(v[1], v[0]), pkbf(v[0], pv)};
        if (lane < 12) {
            const u32 kk = pkbf(k1024, k1024);
            const u32 f7 = lane == 7 ? kk : 0u, f6 = lane == 6 ? kk : 0u;
            *(LAS u32x4*)(P + lane * 16 + (lane >= 8 ? 4096 : 0)) = u32x4{f7, f7, f7 | (f6 & 0xffff0000u), f7 | f6};
        }
    } else {
        const f32x4 v = kt[1];
        const bool s2 = g < 2;
        const float k255 = __uint_as_float(__builtin_amdgcn_readlane(__float_as_uint(kt[0][3]), 63));
        const float nw = dppf<0x130>(v[0], v[0]);
        const float nr = dppf<0x12f>(v[0], q == 0 ? v[2] : v[0]);
        const float nA = s2 ? nw : nr;
        const float nB = dppf<0x130>(k1024, q == 0 ? v[0] : v[2]);
        const float x2 = s2 ? v[1] : v[0], x3 = s2 ? v[2] : v[1], x6 = s2 ? v[3] : nA;
        const u32 aa = pkbf(v[0], v[0]), cc = pkbf(x3, x3);
        LAS char* PA = P + 4 * (1048 - 8 * q - (s2 ? 256 + 128 * g : 256 * g));
        *(LAS u32x4*)PA = u32x4{pkbf(nA, x6), pkbf(x6, x3), cc, cc};
        *(LAS u32x4*)(PA + 16) = u32x4{pkbf(x3, x2), pkbf(x2, v[0]), aa, pkbf(v[0], lane == 0 ? k255 : v[0])};
        if (!s2) {
            const u32 a2 = pkbf(v[2], v[2]), b2 = pkbf(v[3], v[3]);
            *(LAS u32x4*)(PA - 512) = u32x4{pkbf(nB, nB), pkbf(nB, v[3]), b2, b2};
            *(LAS u32x4*)(PA - 496) = u32x4{pkbf(v[3], v[2]), a2, a2, a2};
        }
    }
    u32x4 KT[4];
    if constexpr (HEAVY) {
        LAS char* C = tw + C_OFF;
        if (lane < 4) *(LAS u32x4*)(C + 252 + 4 + lane * 16) = u32x4{0, 0, 0, 0};
        if (lane == 0) *(LAS u32*)(C + 252) = 0;
        if ((q & 1) == h) {
            LAS char* cp = C + 4 * (64 - h - 32 * g - (q >> 1));
#pragma unroll
            for (int i = 0; i < 4; i++) {
                const float vv = (i == 0 && lane == 0) ? 0.0f : kt[2][i];
                const u16 b = (u16)pkbf(vv, vv);
                *(volatile LAS u16*)(cp - 32 * i) = b; *(volatile LAS u16*)(cp - 32 * i - 2) = b;
            }
        }
        const LAS u32* cr = (const LAS u32*)C + 15 - q + 4 * g;
        KT[3] = u32x4{cr[0], cr[2], cr[16], cr[18]};
        KT[2] = u32x4{cr[16], cr[18], cr[32], cr[34]};
        KT[1] = u32x4{cr[32], cr[34], cr[48], cr[50]};
        KT[0] = u32x4{cr[48], cr[50], 0u, 0u};
    }
    bar();
    u32x4 A[17];
    {
        const LAS u32* pr = (const LAS u32*)P + 15 - q + 8 * g;
        if (HEAVY && h == 0) A[16] = u32x4{pr[0], pr[2], pr[4], pr[6]};
        pr += 64 - 32 * h;
#pragma unroll
        for (int m = 0; m < 16; m++) A[m] = u32x4{pr[64 * (15 - m)], pr[64 * (15 - m) + 2], pr[64 * (15 - m) + 4], pr[64 * (15 - m) + 6]};
    }
    STAMP(3);

    if constexpr (ROLE == 2) {
        const u32 co = WS_CAND + xcd * 1024 + lane * 8;
        u64 c0 = gld(a.ws, co), c1 = gld(a.ws + 512, co);
        u32x4 fg[2][2][2], fd[2][2];
#pragma unroll
        for (int p = 0; p < 2; p++) {
            const u32 sel = p ? 0x07060302u : 0x05040100u;
#pragma unroll
            for (int s = 0; s < 2; s++) {
#pragma unroll
                for (int d = 0; d < 4; d++) {
                    fg[0][s][p][d] = __builtin_amdgcn_perm(wgu[0][s][2 * d + 1], wgu[0][s][2 * d], sel);
                    fg[1][s][p][d] = __builtin_amdgcn_perm(wgu[1][s][2 * d + 1], wgu[1][s][2 * d], sel);
                    fd[s][p][d] = __builtin_amdgcn_perm(wdn[s][2 * d + 1], wdn[s][2 * d], sel);
                }
                PIN(fg[0][s][p]); PIN(fg[1][s][p]); PIN(fd[s][p]);
            }
        }
        u32x4 an[2];
        an[0] = an[1] = u32x4{0x3f803f80u, 0x3f803f80u, 0x3f803f80u, 0x3f803f80u};
        PIN(an[0]); PIN(an[1]);
#pragma clang loop unroll(disable)
        for (u32 spin = 0; spin < SPIN_CAP && !allok((u32)(c0 >> 32) == tag && (u32)(c1 >> 32) == tag); spin++) {
            __builtin_amdgcn_s_sleep(2);
            c0 = gld(a.ws, co); c1 = gld(a.ws + 512, co);
        }
        STAMP(4);
        const u32 ka = max((u32)c0, (u32)c1), kb = min((u32)c0, (u32)c1);
        const u32 k1 = wave_umax(ka), k2 = wave_umax(ka == k1 ? kb : ka);
        const u32 kk = (lane & 1) ? k2 : k1;
        const u32 row = ~kk & 2047u;
        if (q < 2) {
            const u32 o = row * 1024 + w * 128 + g * 16;
            an[0] = ldg<u32x4>(a.x, o); an[1] = ldg<u32x4>(a.x, o + 64);
        }
        const u32 sro = WS_STATS + xcd * 16384 + row * 8;
        u64 sr = gld(a.ws, sro);
        sg[2] = gld(a.ws, so); sg[3] = gld(a.ws + 8, so);
        if (jj == 8 && tid < 2) {
            const float sv = sigm(__uint_as_float((kk ^ ((~(int)kk >> 31) | 0x80000000u)) & 0xffff0000u));
            gst(a.ws, WS_SEL + xcd * 128 + lane * 8, tag, row | (pkbf(sv, sv) << 16));
        }
        f32x4 c[2][2];
#pragma unroll
        for (int mt = 0; mt < 2; mt++)
#pragma unroll
            for (int p = 0; p < 2; p++) c[mt][p] = mfma_bf(an[1], fg[mt][1][p], mfma_bf(an[0], fg[mt][0][p], z4));
#pragma clang loop unroll(disable)
        for (u32 spin = 0; spin < SPIN_CAP && !allok((u32)(sr >> 32) == tag); spin++) sr = gld(a.ws, sro);
        const u32 s0 = __builtin_amdgcn_readlane((u32)sr, 0), s1 = __builtin_amdgcn_readlane((u32)sr, 1);
        LAS float* gu = (LAS float*)(lds + LDS_GU);
        if (g == 0) {
#pragma unroll
            for (int mt = 0; mt < 2; mt++) {
                *(LAS f32x2*)(gu + w * 128 + mt * 64 + 2 * q) = f32x2{c[mt][0][0], c[mt][1][0]} - bflo(s0) * f32x2{c[mt][0][2], c[mt][1][2]};
                *(LAS f32x2*)(gu + w * 128 + mt * 64 + 32 + 2 * q) = f32x2{c[mt][0][1], c[mt][1][1]} - bflo(s1) * f32x2{c[mt][0][2], c[mt][1][2]};
            }
        }
        const float rs = __builtin_amdgcn_rcpf(bfhi(lane < 32 ? s0 : s1));
        bar();
        f32x2 gs = {0.0f, 0.0f};
#pragma unroll
        for (int i = 0; i < 8; i++) gs += f32x2{gu[i * 128 + lane], gu[i * 128 + 64 + lane]};
        const u32 gb = pkbf(gs.x * rs, gs.y * rs);
        const float ga = bflo(gb);
        const float hv = rbf(ga * sigm(ga)) * bfhi(gb);
        LAS u16* hb = (LAS u16*)(lds + LDS_H + w * 128);
        hb[lane] = (u16)pkbf(hv, hv);
        const u32x4 ah = *(const LAS u32x4*)(hb + (q & 1) * 32 + 8 * g);
        const f32x4 c00 = mfma_bf(ah, fd[0][0], z4), c01 = mfma_bf(ah, fd[0][1], z4), c10 = mfma_bf(ah, fd[1][0], z4), c11 = mfma_bf(ah, fd[1][1], z4);
        const float u0 = (g & 1) ? c01[0] : c00[0], u1 = (g & 1) ? c11[0] : c10[0], v0 = (g & 1) ? c01[1] : c00[1], v1 = (g & 1) ? c11[1] : c10[1];
        const u32 pp = WS_PART + (((16 * w + 8 * (g >> 1) + (q >> 1)) * NFFN + f) * 4 + 2 * (q & 1) + (g & 1)) * 8;
        gst(a.ws, pp, tag, __float_as_uint((g & 2) ? u1 : u0));
        gst(a.ws + 128 * NFFN * 32, pp, tag, __float_as_uint((g & 2) ? v1 : v0));
        STAMP(5);
    }

    {
        bool ok = (u32)(sg[2] >> 32) == tag && (u32)(sg[3] >> 32) == tag;
        if constexpr (HEAVY) ok = ok && (u32)(sg[0] >> 32) == tag && (u32)(sg[1] >> 32) == tag;
        if (!allok(ok)) {
#pragma clang loop unroll(disable)
            for (u32 spin = 0; spin < SPIN_CAP; spin++) {
                ok = true;
#pragma unroll
                for (int i = HEAVY ? 0 : 2; i < 4; i++) {
                    sg[i] = gld(a.ws + (i & 1) * 8 + ((HEAVY && i >= 2) ? 8192 : 0), so); ok = ok && (u32)(sg[i] >> 32) == tag;
                }
                if (allok(ok)) break;
            }
        }
        asm volatile("" :: "v"(warm));
    }
    STAMP(6);
    f32x2 xf[4], zn[4];
#pragma unroll
    for (int p = HEAVY ? 0 : 1; p < 2; p++) {
        const u32 sa = (u32)sg[2 * p], sb = (u32)sg[2 * p + 1];
        const f32x2 rs = {__builtin_amdgcn_rcpf(bfhi(sa)), __builtin_amdgcn_rcpf(bfhi(sb))};
        const f32x2 nb = f32x2{bflo(sa), bflo(sb)} * rs;
#pragma unroll
        for (int c = 0; c < 4; c++) {
            const u32 w0 = xc[2 * p][c >> 1], w1 = xc[2 * p + 1][c >> 1];
            const f32x2 xv = (c & 1) ? f32x2{bfhi(w0), bfhi(w1)} : f32x2{bflo(w0), bflo(w1)};
            const f32x2 z = __builtin_elementwise_fma(xv, rs, -nb);
            if (p == 1) { xf[c] = xv; zn[c] = z; }
            *(LAS u32*)(lds + c * U_CH + 512 + 4 * tr + ((HEAVY && p) ? 2048 : 0)) = pkbf(z.x, z.y);
        }
    }
    bar();
    STAMP(7);

    f32x4 acc[4];
    f32x2 gt[4];
    u64 selv, prtv, seen = 0;
    if (HEAVY && G == 0) seen = gld(a.ws, WS_SEEN + (tid & 255) * 8);
    {
        const LAS char* ub = lds + ch * U_CH + 480 + 32 * q + 16 * g - 64 * h + (HEAVY ? 2048 : 0);
        constexpr int NB = HEAVY ? 28 : 16, PF = 6;
        u32x4 B[NB];
        if constexpr (HEAVY) {
            const LAS char* uw = lds + ch * U_CH + 464 + 32 * q + 16 * g + 16 * h;
            u32x4 Wd[4];
#pragma unroll
            for (int t = 0; t < 4; t++) Wd[t] = *(const LAS u32x4*)(uw + 512 * t);
#pragma unroll
            for (int k = 0; k < PF; k++) B[k] = *(const LAS u32x4*)(ub + 128 * (12 - k));
            __builtin_amdgcn_sched_barrier(0);
            f32x4 dw[4];
#pragma unroll
            for (int t = 0; t < 4; t++) dw[t] = mfma_bf(Wd[t], band, z4);
            acc[3] = mfma_bf(A[0], B[0], z4);
            acc[3] = mfma_bf(A[1], B[1], acc[3]);
            B[PF] = *(const LAS u32x4*)(ub + 128 * (12 - PF));
            B[PF + 1] = *(const LAS u32x4*)(ub + 128 * (11 - PF));
            const u32x4 S0 = {pkbf(dw[0][0], dw[0][1]), pkbf(dw[0][2], dw[0][3]), pkbf(dw[1][0], dw[1][1]), pkbf(dw[1][2], dw[1][3])};
            const u32x4 S1 = {pkbf(dw[2][0], dw[2][1]), pkbf(dw[2][2], dw[2][3]), pkbf(dw[3][0], dw[3][1]), pkbf(dw[3][2], dw[3][3])};
#pragma unroll
            for (int i = 0; i < 3; i++) acc[i] = mfma_bf(S0, KT[i], z4);
            acc[3] = mfma_bf(S0, KT[3], acc[3]);
            acc[2] = mfma_bf(S1, KT[0], acc[2]); acc[3] = mfma_bf(S1, KT[1], acc[3]);
        } else {
#pragma unroll
            for (int k = 0; k < PF; k++) B[k] = *(const LAS u32x4*)(ub + 128 * (12 - k));
        }
        __builtin_amdgcn_sched_barrier(0);
#pragma unroll
        for (int k = HEAVY ? 2 : 0; k < NB; k++) {
            if (k + PF < NB) B[k + PF] = *(const LAS u32x4*)(ub + 128 * (12 - k - PF));
#pragma unroll
            for (int i = 0; i < 4; i++) {
                const int mp = 4 * i - 12 + k;
                if (mp >= 0 && mp < 16) acc[i] = mfma_bf(A[mp], B[k], (!HEAVY && mp == 0) ? z4 : acc[i]);
            }
            if (k >= 8 && k < 16 && (k & 1) == 0) {
                const int c = (k - 8) >> 1;
                const f32x2 e = zn[c] * -1.44269504089f;
                gt[c] = f32x2{__builtin_amdgcn_exp2f(e.x), __builtin_amdgcn_exp2f(e.y)} + 1.0f;
                PIN(gt[c]);
            }
            if (k >= 8 && k < 16 && (k & 1) == 1) {
                const int c = (k - 8) >> 1;
                gt[c] = zn[c] * f32x2{__builtin_amdgcn_rcpf(gt[c].x), __builtin_amdgcn_rcpf(gt[c].y)};
                PIN(gt[c]);
            }
            if (k == (HEAVY ? 14 : 4)) {
                selv = gld(a.ws, selo); prtv = gld(a.ws, prto);
            }
            __builtin_amdgcn_sched_barrier(0);
        }
        if (HEAVY && h == 0) {
#pragma unroll
            for (int i = 0; i < 4; i++) acc[i] = mfma_bf(A[16], i ? B[28 - 4 * i] : *(const LAS u32x4*)(ub - 128 * 16), acc[i]);
        }
    }
    STAMP(8);
#pragma unroll
    for (int i = 0; i < 4; i++) *(LAS f32x4*)(lds + ya + i * Y_TILE) = acc[i];
    bar();
    f32x2 sv[4], o[4];
    {
        const LAS char* yp = lds + yb;
#pragma unroll
        for (int c = 0; c < 4; c++) {
            sv[c] = __builtin_elementwise_fma(zn[c], f32x2{1.0f, 1.0f} * ((c & 1) ? bfhi(dsk[c >> 1]) : bflo(dsk[c >> 1])), *(const LAS f32x2*)(yp + c * Y_CH)) + *(const LAS f32x2*)(yp + (4 + c) * Y_CH);
            o[c] = __builtin_elementwise_fma(sv[c], gt[c], xf[c]);
        }
    }
    const u32 oo = xo + (HEAVY ? 1024 * 1024 : 0);
    stg(a.out, oo, u32x2{pkbf(o[0].x, o[1].x), pkbf(o[2].x, o[3].x)});
    stg(a.out, oo + 1024, u32x2{pkbf(o[0].y, o[1].y), pkbf(o[2].y, o[3].y)});
    STAMP(9);
    if (!allok((u32)(selv >> 32) == tag)) {
#pragma clang loop unroll(disable)
        for (u32 spin = 0; spin < SPIN_CAP; spin++) { selv = gld(a.ws, selo); if (allok((u32)(selv >> 32) == tag)) break; __builtin_amdgcn_s_sleep(2); }
    }
    const u32 s0 = __builtin_amdgcn_readlane((u32)selv, 0), s1 = __builtin_amdgcn_readlane((u32)selv, 1);
    const int i0 = s0 & 2047, i1 = s1 & 2047;
    const bool own0 = (i0 >= 1024) == HEAVY, own1 = (i1 >= 1024) == HEAVY;
    if (own0 || own1) {
        if (h ? own1 : own0) {
            if (!allok((u32)(prtv >> 32) == tag)) {
#pragma clang loop unroll(disable)
                for (u32 spin = 0; spin < SPIN_CAP; spin++) { prtv = gld(a.ws, prto); if (allok((u32)(prtv >> 32) == tag)) break; __builtin_amdgcn_s_sleep(2); }
            }
            const float y = wave_fsum(lane < NFFN ? __uint_as_float((u32)prtv) : 0.0f);
            if (lane == 0) *(LAS float*)(lds + LDS_UPD + w * 4) = rbf(rbf(y) * bfhi(h ? s1 : s0));
        }
        bar();
        const int row0 = 2 * tr + (HEAVY ? 1024 : 0);
#pragma unroll
        for (int e = 0; e < 2; e++) {
            const int row = row0 + e;
            if (row == i0 || row == i1) {
                const f32x4 up = *(const LAS f32x4*)(lds + LDS_UPD + (row == i0 ? 0 : 16));
                float oe[4];
#pragma unroll
                for (int c = 0; c < 4; c++) {
                    const float z = rbf(rbf(zn[c][e]) + up[c]);
                    oe[c] = __builtin_fmaf(sv[c][e], z * sigm(z), xf[c][e]);
                }
                stg(a.out, oo + e * 1024, u32x2{pkbf(oe[0], oe[1]), pkbf(oe[2], oe[3])});
            }
        }
    }
    if (HEAVY && G == 0) {
        const bool ln = ((tid >> 3) & 31) < 8;
#pragma clang loop unroll(disable)
        for (u32 spin = 0; spin < SPIN_CAP && !allok(ln || (u32)(seen >> 32) == tag); spin++) seen = gld(a.ws, WS_SEEN + (tid & 255) * 8);
        bar();
        if (tid == 0) __hip_atomic_store((gu32*)(a.ws + WS_EPOCH), tag, RLX_AGENT);
    }
    STAMPS_OUT;
}

__global__ void __launch_bounds__(512, 2) s4_mod_fused(Args a) {
    extern __shared__ __attribute__((aligned(16))) char smem[];
    LAS char* lds = (LAS char*)smem;
    const int xcd = blockIdx.x & 7, j = blockIdx.x >> 3;
    const int jj = j & 15;
    if (j >= 16) body<0>(a, lds, xcd, jj);
    else if (jj < 8) body<1>(a, lds, xcd, jj);
    else if (jj < 15) body<2>(a, lds, xcd, jj);
    else body<3>(a, lds, xcd, jj);
}
#define ROLE_OF(b) (((b) >> 3) >= 16 ? 0 : ((((b) >> 3) & 15) >= 8 && (((b) >> 3) & 15) < 15 ? 2 : ((((b) >> 3) & 15) == 15 ? 3 : 1)))

extern "C" void kernel_launch(void* const* d_in, const int* in_sizes, int n_in, void* d_out, int out_size, void* d_ws,
                              size_t ws_size, hipStream_t stream) {
    Args a;
    a.x = (const u16*)d_in[0]; a.are = (const u16*)d_in[1]; a.aim = (const u16*)d_in[2]; a.bre = (const u16*)d_in[3];
    a.bim = (const u16*)d_in[4]; a.cre = (const u16*)d_in[5]; a.cim = (const u16*)d_in[6]; a.logdt = (const u16*)d_in[7];
    a.dskip = (const u16*)d_in[8]; a.wr = (const u16*)d_in[10]; a.wg = (const u16*)d_in[11]; a.wu = (const u16*)d_in[12];
    a.wd = (const u16*)d_in[13]; a.out = (u16*)d_out; a.ws = (char*)d_ws;
    (void)hipFuncSetAttribute((const void*)s4_mod_fused, hipFuncAttributeMaxDynamicSharedMemorySize, LDS_TOTAL);
    hipLaunchKernelGGL(s4_mod_fused, dim3(256), dim3(512), LDS_TOTAL, stream, a);
}
```

```cpp
#define STAMPS
#include <hip/hip_runtime.h>
#include <stdint.h>

typedef unsigned short u16;
typedef unsigned int u32;
typedef unsigned long long u64;
typedef float f32x2 __attribute__((ext_vector_type(2)));
typedef float f32x4 __attribute__((ext_vector_type(4)));
typedef u32 u32x2 __attribute__((ext_vector_type(2)));
typedef u32 u32x4 __attribute__((ext_vector_type(4)));
typedef __bf16 bf16x2 __attribute__((ext_vector_type(2)));
typedef __bf16 bf16x8 __attribute__((ext_vector_type(8)));
typedef _Float16 f16x2 __attribute__((ext_vector_type(2)));
typedef _Float16 f16x8 __attribute__((ext_vector_type(8)));
#define GAS __attribute__((address_space(1)))
#define LAS __attribute__((address_space(3)))
#define CAS __attribute__((address_space(4)))
typedef GAS u64 gu64;
typedef GAS u32 gu32;
#define RLX_AGENT __ATOMIC_RELAXED, __HIP_MEMORY_SCOPE_AGENT

constexpr int D_ = 512, F_ = 1792;
constexpr int NFFN = 56;
constexpr u32 WS_EPOCH = 0, WS_SEEN = 1024, WS_SEL = 4096, WS_CAND = 8192, WS_STATS = 32768, WS_PART = 262144;
constexpr u32 WS_DBG = 1u << 21;
constexpr int U_CH = (256 + 2048) * 2;
constexpr int LDS_LN = 4 * U_CH;
constexpr int LDS_GU = LDS_LN + 2048;
constexpr int LDS_H = LDS_GU + 4096;
constexpr int LDS_UPD = LDS_H + 1024;
constexpr int LDS_TAG = LDS_UPD + 32;
constexpr int LDS_TBL = LDS_TAG + 32;
constexpr int TBL_ROW = 272, TBL_SZ = 57 * TBL_ROW;
constexpr int LDS_TOTAL = LDS_TBL + 8 * TBL_SZ;
constexpr int C_OFF = 3072 + 288;
constexpr int P_OFF = 4096;
constexpr int Y_TILE = 1280, Y_CH = 4 * Y_TILE;
constexpr u32 SPIN_CAP = 1u << 16;

struct Args {
    const u16 *x, *are, *aim, *bre, *bim, *cre, *cim, *logdt, *dskip, *wr, *wg, *wu, *wd;
    u16* out;
    char* ws;
};

template <typename T> __device__ __forceinline__ T ldg(const void* base, u32 off) { return *(const GAS T*)((const GAS char*)base + off); }
template <typename T> __device__ __forceinline__ void stg(void* base, u32 off, T v) { *(GAS T*)((GAS char*)base + off) = v; }
__device__ __forceinline__ u64 gld(const char* base, u32 off) { return __hip_atomic_load((gu64*)((GAS char*)base + off), RLX_AGENT); }
__device__ __forceinline__ void gst(char* base, u32 off, u32 tag, u32 v) { __hip_atomic_store((gu64*)((GAS char*)base + off), ((u64)tag << 32) | v, RLX_AGENT); }
__device__ __forceinline__ float bflo(u32 v) { return __uint_as_float(v << 16); }
__device__ __forceinline__ float bfhi(u32 v) { return __uint_as_float(v & 0xffff0000u); }
__device__ __forceinline__ u32 pkbf(float a, float b) { f32x2 v = {a, b}; return __builtin_bit_cast(u32, __builtin_convertvector(v, bf16x2)); }
__device__ __forceinline__ u32 pkh(f32x2 v) { return __builtin_bit_cast(u32, __builtin_convertvector(v, f16x2)); }
__device__ __forceinline__ float rbf(float a) { return bflo(pkbf(a, a)); }
__device__ __forceinline__ f32x2 cmul(f32x2 a, f32x2 z, f32x2 zn) {
    return __builtin_elementwise_fma(a.yx, zn, a * z.xx);
}
__device__ __forceinline__ f32x2 csq(f32x2 a) { return cmul(a, a, f32x2{-a.y, a.y}); }
__device__ __forceinline__ void bar() { asm volatile("s_waitcnt lgkmcnt(0)\n\ts_barrier" ::: "memory"); }
#define PIN(x) asm volatile("" : "+v"(x))
__device__ __forceinline__ f32x4 mfma_bf(u32x4 a, u32x4 b, f32x4 c) {
    return __builtin_amdgcn_mfma_f32_16x16x32_bf16(__builtin_bit_cast(bf16x8, a), __builtin_bit_cast(bf16x8, b), c, 0, 0, 0);
}
__device__ __forceinline__ f32x4 mfma_h(u32x4 a, u32x4 b, f32x4 c) {
    return __builtin_amdgcn_mfma_f32_16x16x32_f16(__builtin_bit_cast(f16x8, a), __builtin_bit_cast(f16x8, b), c, 0, 0, 0);
}
template <int CTRL> __device__ __forceinline__ float dppf(float old, float src) {
    return __uint_as_float(__builtin_amdgcn_update_dpp(__float_as_uint(old), __float_as_uint(src), CTRL, 0xf, 0xf, false));
}
template <int CTRL, int RM = 0xf> __device__ __forceinline__ u32 dpp0(u32 src) { return __builtin_amdgcn_update_dpp(0u, src, CTRL, RM, 0xf, false); }
__device__ __forceinline__ u32 row_umax(u32 v) {
    v = max(v, dpp0<0x128>(v)); v = max(v, dpp0<0x124>(v)); v = max(v, dpp0<0x122>(v)); v = max(v, dpp0<0x121>(v));
    return v;
}
__device__ __forceinline__ u32 wave_umax(u32 v) {
    v = row_umax(v); v = max(v, dpp0<0x142, 0xa>(v)); v = max(v, dpp0<0x143, 0xc>(v));
    return __builtin_amdgcn_readlane(v, 63);
}
__device__ __forceinline__ float wave_fsum(float f) {
    f += __uint_as_float(dpp0<0x128>(__float_as_uint(f))); f += __uint_as_float(dpp0<0x124>(__float_as_uint(f)));
    f += __uint_as_float(dpp0<0x122>(__float_as_uint(f))); f += __uint_as_float(dpp0<0x121>(__float_as_uint(f)));
    f += __uint_as_float(dpp0<0x142, 0xa>(__float_as_uint(f))); f += __uint_as_float(dpp0<0x143, 0xc>(__float_as_uint(f)));
    return __uint_as_float(__builtin_amdgcn_readlane(__float_as_uint(f), 63));
}
__device__ __forceinline__ float sigm(float z) { return __builtin_amdgcn_rcpf(1.0f + __builtin_amdgcn_exp2f(z * -1.44269504089f)); }
__device__ __forceinline__ bool allok(bool ok) { return __builtin_amdgcn_ballot_w64(!ok) == 0; }

__device__ __forceinline__ u64 rt() { u64 t; asm volatile("s_memrealtime %0\n\ts_waitcnt lgkmcnt(0)" : "=s"(t) :: "memory"); return t; }
__device__ __forceinline__ u64 ct() { u64 t; asm volatile("s_memtime %0\n\ts_waitcnt lgkmcnt(0)" : "=s"(t) :: "memory"); return t; }
#define STAMP(i) do { st_[i] = rt(); } while (0)
#define STAMPS_DECL u64 st_[14] = {0}; const u64 c0_ = ct()
#define STAMPS_OUT do { asm volatile("s_waitcnt vmcnt(0)" ::: "memory"); STAMP(10); const u64 c1_ = ct(); if (lane == 0) { u64* o_ = (u64*)(a.ws + WS_DBG) + (blockIdx.x * 8 + w) * 16; for (int i_ = 0; i_ < 14; i_++) o_[i_] = st_[i_]; o_[14] = c0_; o_[15] = c1_; } } while (0)

template <int ROLE>
__device__ __forceinline__ void body(const Args& a, LAS char* lds, const int xcd, const int jj) {
    constexpr bool HEAVY = ROLE == 0;
    const int tid = threadIdx.x, lane = tid & 63;
    const int w = __builtin_amdgcn_readfirstlane(tid >> 6);
    const int q = lane & 15, g = lane >> 4;
    const int ch = w & 3, h = w >> 2;
    const int G = xcd * 16 + jj;
    const int tr = (tid + 64 * (jj & 7)) & 511;
    const f32x4 z4 = {0, 0, 0, 0};
    STAMPS_DECL;
    STAMP(0);

    u32x4 xa[4], cb[4];
    const int lnb = jj * 8 + xcd;
    if constexpr (ROLE == 1) {
        const u32 xo = (lnb * 32 + (w & 1) * 16 + q) * 1024 + (w >> 1) * 256 + g * 16;
#pragma unroll
        for (int s = 0; s < 4; s++) xa[s] = ldg<u32x4>(a.x, xo + 64 * s);
        const u32 o1 = q == 0 ? 0x3f803f80u : 0u;
#pragma unroll
        for (int s = 0; s < 4; s++) cb[s] = u32x4{o1, o1, o1, o1};
        if (q == 1) {
#pragma unroll
            for (int s = 0; s < 4; s++) cb[s] = ldg<u32x4>(a.wr, (w >> 1) * 256 + g * 16 + 64 * s);
        }
    }
    STAMP(11);
    const u32 po = ((4 * G + ch) * 128 + 64 * h + lane) * 2;
    u32x2 ldt2 = *(const CAS u32x2*)((const CAS char*)a.logdt + G * 8);
    u32x2 dsk = *(const CAS u32x2*)((const CAS char*)a.dskip + G * 8);
    const u32 p_are = ldg<u16>(a.are, po), p_aim = ldg<u16>(a.aim, po), p_bre = ldg<u16>(a.bre, po), p_bim = ldg<u16>(a.bim, po);
    const u32 p_cre = ldg<u16>(a.cre, po), p_cim = ldg<u16>(a.cim, po);
    u32 epoch = *(const CAS u32*)(a.ws + WS_EPOCH);
    const u32 xo = tr * 2048 + G * 8;
    u32 warm = 0;
    if (HEAVY) warm = ldg<u32>(a.x, (jj * 128 + w * 16 + q) * 1024 + G * 8);
    if (tid < 128) *(LAS u32x4*)(lds + (tid >> 5) * U_CH + (tid & 31) * 16) = u32x4{0, 0, 0, 0};
    u32x4 band;
    if constexpr (HEAVY) {
        const u32 bits = ((h ? 0x7fu : 0x1ffu) << (q + 4 + h)) >> (8 * g);
#pragma unroll
        for (int d = 0; d < 4; d++) band[d] = __umul24(((bits >> (2 * d)) & 1u) | ((bits << (15 - 2 * d)) & 0x10000u), 0x3f80u);
        PIN(band);
    }
    u32 ya = LDS_TBL + w * Y_CH + g * 320 + q * 16;
    u32 yb = LDS_TBL + (tr >> 7) * Y_TILE + ((tr >> 1) & 3) * 320 + ((tr >> 3) & 15) * 16 + (tr & 1) * 8;
    u32 selo = WS_SEL + xcd * 128 + (lane & 1) * 8;
    u32 prto = WS_PART + ((h * 128 + G) * NFFN + min(lane, NFFN - 1)) * 32 + ch * 8;
    u32 so = WS_STATS + xcd * 16384 + tr * 16;
    PIN(ya); PIN(yb); PIN(selo); PIN(prto); PIN(so);
    asm volatile("" : "+s"(epoch), "+s"(ldt2), "+s"(dsk));
    u32 tag;
    u32x2 xc[4];
    u32 wgu[2][2][8], wdn[2][8];
    const int f = xcd * 7 + jj - 8;

    if constexpr (ROLE == 1) {
        const u32x4 ones = {0x3f803f80u, 0x3f803f80u, 0x3f803f80u, 0x3f803f80u};
        f32x4 d1 = z4, d2 = z4, d3 = z4;
#pragma unroll
        for (int s = 0; s < 4; s++) { d1 = mfma_bf(xa[s], cb[s], d1); d2 = mfma_bf(xa[s], xa[s], d2); d3 = mfma_bf(ones, cb[s], d3); }
        PIN(d1); PIN(d2); PIN(d3);
        STAMP(12);
        LAS float* ln = (LAS float*)(lds + LDS_LN) + (w & 1) * 64 + (w >> 1);
        if (q < 2) {
#pragma unroll
            for (int i = 0; i < 4; i++) ln[q * 128 + 16 * g + 4 * i] = d1[i];
            if (lane == 1) ln[384] = d3[0];
        }
        const float da = (q & 1) ? d2[1] : d2[0], db = (q & 1) ? d2[3] : d2[2];
        if ((q >> 2) == g) ln[256 + 4 * q] = (q & 2) ? db : da;
        xc[2] = ldg<u32x2>(a.x, xo); xc[3] = ldg<u32x2>(a.x, xo + 1024);
        if (tid == 0) *(LAS u32*)(lds + LDS_TAG) = epoch + 1;
        STAMP(13);
        bar();
        if (w == 0) {
            __builtin_amdgcn_s_setprio(3);
            tag = epoch + 1;
            const int r = lane & 31, hf = lane >> 5;
            const LAS char* lp = lds + LDS_LN + r * 16 + hf * 512;
            const f32x4 s0 = *(const LAS f32x4*)lp, s1 = *(const LAS f32x4*)(hf ? lds + LDS_LN + 1536 : lp + 1024);
            const float va = (s0[0] + s0[1]) + (s0[2] + s0[3]), vb = (s1[0] + s1[1]) + (s1[2] + s1[3]);
            auto e0 = __builtin_amdgcn_permlane32_swap(__float_as_uint(va), __float_as_uint(va), false, false);
            auto e1 = __builtin_amdgcn_permlane32_swap(__float_as_uint(vb), __float_as_uint(vb), false, false);
            const float mean = __uint_as_float(e0[0]) * (1.0f / D_);
            const float var = fmaxf(__uint_as_float(e1[0]) * (1.0f / D_) - mean * mean, 0.0f);
            const float sd = rbf(__builtin_amdgcn_sqrtf(rbf(rbf(var) + 1e-5f)));
            const u32 ms = pkbf(mean, sd);
            const u32 so = WS_STATS + hf * 16384 + (lnb * 32 + r) * 8;
#pragma unroll
            for (int i = 0; i < 4; i++) gst(a.ws + i * 32768, so, tag, ms);
            const float rr = (__uint_as_float(e0[1]) - bflo(ms) * __uint_as_float(e1[1])) * __builtin_amdgcn_rcpf(sd);
            const u32 rb = pkbf(rr, rr);
            const u32 key = ((rb ^ (((int)rb >> 31) | 0x80000000u)) & 0xffff0000u) | (0xffffu - (lnb * 32 + r));
            u32 k1 = row_umax(key);
            auto m1 = __builtin_amdgcn_permlane16_swap(k1, k1, false, false); k1 = max(m1[0], m1[1]);
            u32 k2 = row_umax(key == k1 ? 0u : key);
            auto m2 = __builtin_amdgcn_permlane16_swap(k2, k2, false, false); k2 = max(m2[0], m2[1]);
            if (lane < 16) gst(a.ws, WS_CAND + (lane >> 1) * 1024 + lnb * 16 + (lane & 1) * 8, tag, (lane & 1) ? k2 : k1);
            __builtin_amdgcn_s_setprio(0);
        }
        STAMP(1);
    } else {
        if (tid == 0) {
            *(LAS u32*)(lds + LDS_TAG) = epoch + 1;
            gst(a.ws, WS_SEEN + blockIdx.x * 8, epoch + 1, 0);
        }
    }

    f32x4 kt[3];
    LAS char* const tw = lds + LDS_TBL + w * TBL_SZ;
    u64 sg[4];
    {
        const float ld = __uint_as_float((ldt2[ch >> 1] << ((ch & 1) ? 0 : 16)) & 0xffff0000u);
        const float t0 = ld * 1.44269504089f;
        const float te = __builtin_fmaf(ld, 1.92596303e-8f, __builtin_fmaf(ld, 1.44269504089f, -t0));
        const float ex = __builtin_amdgcn_exp2f(t0);
        const float dt = rbf(__builtin_fmaf(ex * te, 0.69314718056f, ex));
        float dtp = dt;
        PIN(dtp);
        u32 pb = p_cim;
        asm volatile("" : "+v"(pb) :: "memory");
        if (ROLE != 1) STAMP(12);
    if constexpr (HEAVY) {
        xc[2] = ldg<u32x2>(a.x, xo + 1024 * 1024); xc[3] = ldg<u32x2>(a.x, xo + 1025 * 1024);
        xc[0] = ldg<u32x2>(a.x, xo); xc[1] = ldg<u32x2>(a.x, xo + 1024);
    } else if constexpr (ROLE != 1) {
        xc[2] = ldg<u32x2>(a.x, xo); xc[3] = ldg<u32x2>(a.x, xo + 1024);
    }
    if constexpr (ROLE == 2) {
        const u32 o = ((64 * w + 8 * g) * F_ + 32 * f + 2 * q) * 2;
#pragma unroll
        for (int s = 0; s < 2; s++)
#pragma unroll
            for (int j = 0; j < 8; j++) {
                wgu[0][s][j] = ldg<u32>((const char*)a.wg + (32 * s + j) * F_ * 2, o);
                wgu[1][s][j] = ldg<u32>((const char*)a.wu + (32 * s + j) * F_ * 2, o);
            }
        const u32 od = ((32 * f + 8 * g) * D_ + 64 * w + 2 * q) * 2;
#pragma unroll
        for (int u = 0; u < 2; u++)
#pragma unroll
            for (int j = 0; j < 8; j++) wdn[u][j] = ldg<u32>((const char*)a.wd + j * D_ * 2 + u * 64, od);
    }
        if (ROLE != 1) STAMP(13);
        __builtin_amdgcn_sched_barrier(0);
        const float lr = (dtp * 1.44269504089f) * bflo(p_are);
        const float ai = dtp * bflo(p_aim);
        const float th = ai * 0.15915494309f;
        const float tl = __builtin_fmaf(ai, 6.4206382e-9f, __builtin_fmaf(ai, 0.15915494309f, -th));
        const float bi = bflo(p_bim);
        const f32x2 W = cmul(f32x2{bflo(p_cre), bflo(pb)}, f32x2{bflo(p_bre), bi}, f32x2{-bi, bi}) * dtp;
        const float gp = fmaxf(lr, 0.0f);
        const int en = __builtin_amdgcn_frexp_expf(fmaxf(fabsf(W.x), fabsf(W.y))) + (int)(2168.0f * gp);
        const int E = min(max((int)wave_umax((u32)(en + 201)) - 200, -100), 120);
        const float sA = __builtin_amdgcn_ldexpf(__builtin_amdgcn_exp2f(120.0f * gp), 13 - E), sB = __builtin_amdgcn_exp2f(-120.0f * gp);
        const float inv = __uint_as_float((u32)(E - 13 + 127) << 23);
        const float t1 = __builtin_amdgcn_fractf(th), e1 = __builtin_amdgcn_exp2f(lr);
        const f32x2 z1 = f32x2{__builtin_amdgcn_cosf(t1), __builtin_amdgcn_sinf(t1)} * e1;
        const float t7 = __builtin_fmaf(128.0f, tl, __builtin_amdgcn_fractf(128.0f * th)), e7 = __builtin_amdgcn_exp2f(128.0f * lr);
        const f32x2 z128 = f32x2{__builtin_amdgcn_cosf(t7), __builtin_amdgcn_sinf(t7)} * e7;
        const f32x2 z2 = csq(z1), zz4 = csq(z2), z8 = csq(zz4);
        const f32x2 n1 = {-z1.y, z1.y}, n2 = {-z2.y, z2.y}, n4 = {-zz4.y, zz4.y}, n128 = {-z128.y, z128.y};
        const f32x2 c8 = {z8.y, -z8.y};
        LAS u32* tb = (LAS u32*)tw + lane;
        f32x2 X = W * sA, B = {sB, 0.0f};
        constexpr int KX = HEAVY ? 16 : 8;
#pragma unroll
        for (int k = 0; k < 17; k++) {
            if (k < 16) { tb[(41 + k) * 68] = pkh(B); if (k < 15) B = cmul(B, z8, c8); }
            if (k < 2) {
                f32x2 c = X;
#pragma unroll
                for (int i = 0; i < 8; i++) { tb[(8 * k + i) * 68] = pkh(c); if (i < 7) c = cmul(c, z1, n1); }
            } else if (k < 4) {
                f32x2 c = X;
#pragma unroll
                for (int i = 0; i < 4; i++) { tb[(16 + 4 * (k - 2) + i) * 68] = pkh(c); if (i < 3) c = cmul(c, z2, n2); }
            } else if (k < 8) {
                tb[(24 + 2 * (k - 4)) * 68] = pkh(X); tb[(25 + 2 * (k - 4)) * 68] = pkh(cmul(X, zz4, n4));
            } else if (k <= KX) tb[(32 + k - 8) * 68] = pkh(X);
            if (k < KX) X = cmul(X, z128, n128);
        }
        STAMP(2);
        if constexpr (ROLE != 2) {
#pragma unroll
            for (int i = HEAVY ? 0 : 2; i < 4; i++) sg[i] = gld(a.ws + (i & 1) * 8 + ((HEAVY && i >= 2) ? 8192 : 0), so);
        }
        const LAS char* tf = tw + q * TBL_ROW + g * 16;
#pragma unroll
        for (int t = 0; t < 3; t++) kt[t] = z4;
#pragma unroll
        for (int s = 0; s < 4; s++) {
            const u32x4 fb = *(const LAS u32x4*)(tf + 41 * TBL_ROW + s * 64);
#pragma unroll
            for (int t = 0; t < 3; t++) kt[t] = mfma_h(*(const LAS u32x4*)(tf + 16 * t * TBL_ROW + s * 64), fb, kt[t]);
        }
#pragma unroll
        for (int t = 0; t < 3; t++) { kt[t] *= inv; *(LAS f32x4*)(tw + t * 1024 + lane * 16) = kt[t]; }
    }
    bar();
    tag = *(LAS u32*)(lds + LDS_TAG);
#pragma unroll
    for (int t = 0; t < 3; t++) kt[t] += *(const LAS f32x4*)(lds + LDS_TBL + (w ^ 4) * TBL_SZ + t * 1024 + lane * 16);
    LAS char* const P = lds + LDS_TBL + ch * TBL_SZ + P_OFF;
    const float k1024 = __uint_as_float(__builtin_amdgcn_readlane(__float_as_uint(kt[2][0]), 0));
    if (h == 0) {
        const f32x4 v = kt[0];
        const int src = (g & 1) ? lane - 16 : (q ? lane + 15 : lane - 1);
        float pv = __uint_as_float(__builtin_amdgcn_ds_bpermute(src * 4, __float_as_uint(v[3])));
        pv = lane == 0 ? 0.0f : pv;
        *(LAS u32x4*)(P + 4 * (1052 - 128 * (g >> 1) - 8 * q - 4 * (g & 1))) = u32x4{pkbf(v[3], v[2]), pkbf(v[2], v[1]), pkbf(v[1], v[0]), pkbf(v[0], pv)};
        if (lane < 12) {
            const u32 kk = pkbf(k1024, k1024);
            const u32 f7 = lane == 7 ? kk : 0u, f6 = lane == 6 ? kk : 0u;
            *(LAS u32x4*)(P + lane * 16 + (lane >= 8 ? 4096 : 0)) = u32x4{f7, f7, f7 | (f6 & 0xffff0000u), f7 | f6};
        }
    } else {
        const f32x4 v = kt[1];
        const bool s2 = g < 2;
        const float k255 = __uint_as_float(__builtin_amdgcn_readlane(__float_as_uint(kt[0][3]), 63));
        const float nw = dppf<0x130>(v[0], v[0]);
        const float nr = dppf<0x12f>(v[0], q == 0 ? v[2] : v[0]);
        const float nA = s2 ? nw : nr;
        const float nB = dppf<0x130>(k1024, q == 0 ? v[0] : v[2]);
        const float x2 = s2 ? v[1] : v[0], x3 = s2 ? v[2] : v[1], x6 = s2 ? v[3] : nA;
        const u32 aa = pkbf(v[0], v[0]), cc = pkbf(x3, x3);
        LAS char* PA = P + 4 * (1048 - 8 * q - (s2 ? 256 + 128 * g : 256 * g));
        *(LAS u32x4*)PA = u32x4{pkbf(nA, x6), pkbf(x6, x3), cc, cc};
        *(LAS u32x4*)(PA + 16) = u32x4{pkbf(x3, x2), pkbf(x2, v[0]), aa, pkbf(v[0], lane == 0 ? k255 : v[0])};
        if (!s2) {
            const u32 a2 = pkbf(v[2], v[2]), b2 = pkbf(v[3], v[3]);
            *(LAS u32x4*)(PA - 512) = u32x4{pkbf(nB, nB), pkbf(nB, v[3]), b2, b2};
            *(LAS u32x4*)(PA - 496) = u32x4{pkbf(v[3], v[2]), a2, a2, a2};
        }
    }
    u32x4 KT[4];
    if constexpr (HEAVY) {
        LAS char* C = tw + C_OFF;
        if (lane < 4) *(LAS u32x4*)(C + 252 + 4 + lane * 16) = u32x4{0, 0, 0, 0};
        if (lane == 0) *(LAS u32*)(C + 252) = 0;
        if ((q & 1) == h) {
            LAS char* cp = C + 4 * (64 - h - 32 * g - (q >> 1));
#pragma unroll
            for (int i = 0; i < 4; i++) {
                const float vv = (i == 0 && lane == 0) ? 0.0f : kt[2][i];
                const u16 b = (u16)pkbf(vv, vv);
                *(volatile LAS u16*)(cp - 32 * i) = b; *(volatile LAS u16*)(cp - 32 * i - 2) = b;
            }
        }
        const LAS u32* cr = (const LAS u32*)C + 15 - q + 4 * g;
        KT[3] = u32x4{cr[0], cr[2], cr[16], cr[18]};
        KT[2] = u32x4{cr[16], cr[18], cr[32], cr[34]};
        KT[1] = u32x4{cr[32], cr[34], cr[48], cr[50]};
        KT[0] = u32x4{cr[48], cr[50], 0u, 0u};
    }
    bar();
    u32x4 A[17];
    {
        const LAS u32* pr = (const LAS u32*)P + 15 - q + 8 * g;
        if (HEAVY && h == 0) A[16] = u32x4{pr[0], pr[2], pr[4], pr[6]};
        pr += 64 - 32 * h;
#pragma unroll
        for (int m = 0; m < 16; m++) A[m] = u32x4{pr[64 * (15 - m)], pr[64 * (15 - m) + 2], pr[64 * (15 - m) + 4], pr[64 * (15 - m) + 6]};
    }
    STAMP(3);

    if constexpr (ROLE == 2) {
        const u32 co = WS_CAND + xcd * 1024 + lane * 8;
        u64 c0 = gld(a.ws, co), c1 = gld(a.ws + 512, co);
        u32x4 fg[2][2][2], fd[2][2];
#pragma unroll
        for (int p = 0; p < 2; p++) {
            const u32 sel = p ? 0x07060302u : 0x05040100u;
#pragma unroll
            for (int s = 0; s < 2; s++) {
#pragma unroll
                for (int d = 0; d < 4; d++) {
                    fg[0][s][p][d] = __builtin_amdgcn_perm(wgu[0][s][2 * d + 1], wgu[0][s][2 * d], sel);
                    fg[1][s][p][d] = __builtin_amdgcn_perm(wgu[1][s][2 * d + 1], wgu[1][s][2 * d], sel);
                    fd[s][p][d] = __builtin_amdgcn_perm(wdn[s][2 * d + 1], wdn[s][2 * d], sel);
                }
                PIN(fg[0][s][p]); PIN(fg[1][s][p]); PIN(fd[s][p]);
            }
        }
        u32x4 an[2];
        an[0] = an[1] = u32x4{0x3f803f80u, 0x3f803f80u, 0x3f803f80u, 0x3f803f80u};
        PIN(an[0]); PIN(an[1]);
#pragma clang loop unroll(disable)
        for (u32 spin = 0; spin < SPIN_CAP && !allok((u32)(c0 >> 32) == tag && (u32)(c1 >> 32) == tag); spin++) {
            __builtin_amdgcn_s_sleep(2);
            c0 = gld(a.ws, co); c1 = gld(a.ws + 512, co);
        }
        STAMP(4);
        const u32 ka = max((u32)c0, (u32)c1), kb = min((u32)c0, (u32)c1);
        const u32 k1 = wave_umax(ka), k2 = wave_umax(ka == k1 ? kb : ka);
        const u32 kk = (lane & 1) ? k2 : k1;
        const u32 row = ~kk & 2047u;
        if (q < 2) {
            const u32 o = row * 1024 + w * 128 + g * 16;
            an[0] = ldg<u32x4>(a.x, o); an[1] = ldg<u32x4>(a.x, o + 64);
        }
        const u32 sro = WS_STATS + xcd * 16384 + row * 8;
        u64 sr = gld(a.ws, sro);
        sg[2] = gld(a.ws, so); sg[3] = gld(a.ws + 8, so);
        if (jj == 8 && tid < 2) {
            const float sv = sigm(__uint_as_float((kk ^ ((~(int)kk >> 31) | 0x80000000u)) & 0xffff0000u));
            gst(a.ws, WS_SEL + xcd * 128 + lane * 8, tag, row | (pkbf(sv, sv) << 16));
        }
        f32x4 c[2][2];
#pragma unroll
        for (int mt = 0; mt < 2; mt++)
#pragma unroll
            for (int p = 0; p < 2; p++) c[mt][p] = mfma_bf(an[1], fg[mt][1][p], mfma_bf(an[0], fg[mt][0][p], z4));
#pragma clang loop unroll(disable)
        for (u32 spin = 0; spin < SPIN_CAP && !allok((u32)(sr >> 32) == tag); spin++) sr = gld(a.ws, sro);
        const u32 s0 = __builtin_amdgcn_readlane((u32)sr, 0), s1 = __builtin_amdgcn_readlane((u32)sr, 1);
        LAS float* gu = (LAS float*)(lds + LDS_GU);
        if (g == 0) {
#pragma unroll
            for (int mt = 0; mt < 2; mt++) {
                *(LAS f32x2*)(gu + w * 128 + mt * 64 + 2 * q) = f32x2{c[mt][0][0], c[mt][1][0]} - bflo(s0) * f32x2{c[mt][0][2], c[mt][1][2]};
                *(LAS f32x2*)(gu + w * 128 + mt * 64 + 32 + 2 * q) = f32x2{c[mt][0][1], c[mt][1][1]} - bflo(s1) * f32x2{c[mt][0][2], c[mt][1][2]};
            }
        }
        const float rs = __builtin_amdgcn_rcpf(bfhi(lane < 32 ? s0 : s1));
        bar();
        f32x2 gs = {0.0f, 0.0f};
#pragma unroll
        for (int i = 0; i < 8; i++) gs += f32x2{gu[i * 128 + lane], gu[i * 128 + 64 + lane]};
        const u32 gb = pkbf(gs.x * rs, gs.y * rs);
        const float ga = bflo(gb);
        const float hv = rbf(ga * sigm(ga)) * bfhi(gb);
        LAS u16* hb = (LAS u16*)(lds + LDS_H + w * 128);
        hb[lane] = (u16)pkbf(hv, hv);
        const u32x4 ah = *(const LAS u32x4*)(hb + (q & 1) * 32 + 8 * g);
        const f32x4 c00 = mfma_bf(ah, fd[0][0], z4), c01 = mfma_bf(ah, fd[0][1], z4), c10 = mfma_bf(ah, fd[1][0], z4), c11 = mfma_bf(ah, fd[1][1], z4);
        const float u0 = (g & 1) ? c01[0] : c00[0], u1 = (g & 1) ? c11[0] : c10[0], v0 = (g & 1) ? c01[1] : c00[1], v1 = (g & 1) ? c11[1] : c10[1];
        const u32 pp = WS_PART + (((16 * w + 8 * (g >> 1) + (q >> 1)) * NFFN + f) * 4 + 2 * (q & 1) + (g & 1)) * 8;
        gst(a.ws, pp, tag, __float_as_uint((g & 2) ? u1 : u0));
        gst(a.ws + 128 * NFFN * 32, pp, tag, __float_as_uint((g & 2) ? v1 : v0));
        STAMP(5);
    }

    {
        bool ok = (u32)(sg[2] >> 32) == tag && (u32)(sg[3] >> 32) == tag;
        if constexpr (HEAVY) ok = ok && (u32)(sg[0] >> 32) == tag && (u32)(sg[1] >> 32) == tag;
        if (!allok(ok)) {
#pragma clang loop unroll(disable)
            for (u32 spin = 0; spin < SPIN_CAP; spin++) {
                ok = true;
#pragma unroll
                for (int i = HEAVY ? 0 : 2; i < 4; i++) {
                    sg[i] = gld(a.ws + (i & 1) * 8 + ((HEAVY && i >= 2) ? 8192 : 0), so); ok = ok && (u32)(sg[i] >> 32) == tag;
                }
                if (allok(ok)) break;
            }
        }
        asm volatile("" :: "v"(warm));
    }
    STAMP(6);
    f32x2 xf[4], zn[4];
#pragma unroll
    for (int p = HEAVY ? 0 : 1; p < 2; p++) {
        const u32 sa = (u32)sg[2 * p], sb = (u32)sg[2 * p + 1];
        const f32x2 rs = {__builtin_amdgcn_rcpf(bfhi(sa)), __builtin_amdgcn_rcpf(bfhi(sb))};
        const f32x2 nb = f32x2{bflo(sa), bflo(sb)} * rs;
#pragma unroll
        for (int c = 0; c < 4; c++) {
            const u32 w0 = xc[2 * p][c >> 1], w1 = xc[2 * p + 1][c >> 1];
            const f32x2 xv = (c & 1) ? f32x2{bfhi(w0), bfhi(w1)} : f32x2{bflo(w0), bflo(w1)};
            const f32x2 z = __builtin_elementwise_fma(xv, rs, -nb);
            if (p == 1) { xf[c] = xv; zn[c] = z; }
            *(LAS u32*)(lds + c * U_CH + 512 + 4 * tr + ((HEAVY && p) ? 2048 : 0)) = pkbf(z.x, z.y);
        }
    }
    bar();
    STAMP(7);

    f32x4 acc[4];
    f32x2 gt[4];
    u64 selv, prtv, seen = 0;
    if (HEAVY && G == 0) seen = gld(a.ws, WS_SEEN + (tid & 255) * 8);
    {
        const LAS char* ub = lds + ch * U_CH + 480 + 32 * q + 16 * g - 64 * h + (HEAVY ? 2048 : 0);
        constexpr int NB = HEAVY ? 28 : 16, PF = 6;
        u32x4 B[NB];
        if constexpr (HEAVY) {
            const LAS char* uw = lds + ch * U_CH + 464 + 32 * q + 16 * g + 16 * h;
            u32x4 Wd[4];
#pragma unroll
            for (int t = 0; t < 4; t++) Wd[t] = *(const LAS u32x4*)(uw + 512 * t);
#pragma unroll
            for (int k = 0; k < PF; k++) B[k] = *(const LAS u32x4*)(ub + 128 * (12 - k));
            __builtin_amdgcn_sched_barrier(0);
            f32x4 dw[4];
#pragma unroll
            for (int t = 0; t < 4; t++) dw[t] = mfma_bf(Wd[t], band, z4);
            acc[3] = mfma_bf(A[0], B[0], z4);
            acc[3] = mfma_bf(A[1], B[1], acc[3]);
            B[PF] = *(const LAS u32x4*)(ub + 128 * (12 - PF));
            B[PF + 1] = *(const LAS u32x4*)(ub + 128 * (11 - PF));
            const u32x4 S0 = {pkbf(dw[0][0], dw[0][1]), pkbf(dw[0][2], dw[0][3]), pkbf(dw[1][0], dw[1][1]), pkbf(dw[1][2], dw[1][3])};
            const u32x4 S1 = {pkbf(dw[2][0], dw[2][1]), pkbf(dw[2][2], dw[2][3]), pkbf(dw[3][0], dw[3][1]), pkbf(dw[3][2], dw[3][3])};
#pragma unroll
            for (int i = 0; i < 3; i++) acc[i] = mfma_bf(S0, KT[i], z4);
            acc[3] = mfma_bf(S0, KT[3], acc[3]);
            acc[2] = mfma_bf(S1, KT[0], acc[2]); acc[3] = mfma_bf(S1, KT[1], acc[3]);
        } else {
#pragma unroll
            for (int k = 0; k < PF; k++) B[k] = *(const LAS u32x4*)(ub + 128 * (12 - k));
        }
        __builtin_amdgcn_sched_barrier(0);
#pragma unroll
        for (int k = HEAVY ? 2 : 0; k < NB; k++) {
            if (k + PF < NB) B[k + PF] = *(const LAS u32x4*)(ub + 128 * (12 - k - PF));
#pragma unroll
            for (int i = 0; i < 4; i++) {
                const int mp = 4 * i - 12 + k;
                if (mp >= 0 && mp < 16) acc[i] = mfma_bf(A[mp], B[k], (!HEAVY && mp == 0) ? z4 : acc[i]);
            }
            if (k >= 8 && k < 16 && (k & 1) == 0) {
                const int c = (k - 8) >> 1;
                const f32x2 e = zn[c] * -1.44269504089f;
                gt[c] = f32x2{__builtin_amdgcn_exp2f(e.x), __builtin_amdgcn_exp2f(e.y)} + 1.0f;
                PIN(gt[c]);
            }
            if (k >= 8 && k < 16 && (k & 1) == 1) {
                const int c = (k - 8) >> 1;
                gt[c] = zn[c] * f32x2{__builtin_amdgcn_rcpf(gt[c].x), __builtin_amdgcn_rcpf(gt[c].y)};
                PIN(gt[c]);
            }
            if (k == (HEAVY ? 14 : 4)) {
                selv = gld(a.ws, selo); prtv = gld(a.ws, prto);
            }
            __builtin_amdgcn_sched_barrier(0);
        }
        if (HEAVY && h == 0) {
#pragma unroll
            for (int i = 0; i < 4; i++) acc[i] = mfma_bf(A[16], i ? B[28 - 4 * i] : *(const LAS u32x4*)(ub - 128 * 16), acc[i]);
        }
    }
    STAMP(8);
#pragma unroll
    for (int i = 0; i < 4; i++) *(LAS f32x4*)(lds + ya + i * Y_TILE) = acc[i];
    bar();
    f32x2 sv[4], o[4];
    {
        const LAS char* yp = lds + yb;
#pragma unroll
        for (int c = 0; c < 4; c++) {
            sv[c] = __builtin_elementwise_fma(zn[c], f32x2{1.0f, 1.0f} * ((c & 1) ? bfhi(dsk[c >> 1]) : bflo(dsk[c >> 1])), *(const LAS f32x2*)(yp + c * Y_CH)) + *(const LAS f32x2*)(yp + (4 + c) * Y_CH);
            o[c] = __builtin_elementwise_fma(sv[c], gt[c], xf[c]);
        }
    }
    const u32 oo = xo + (HEAVY ? 1024 * 1024 : 0);
    stg(a.out, oo, u32x2{pkbf(o[0].x, o[1].x), pkbf(o[2].x, o[3].x)});
    stg(a.out, oo + 1024, u32x2{pkbf(o[0].y, o[1].y), pkbf(o[2].y, o[3].y)});
    STAMP(9);
    if (!allok((u32)(selv >> 32) == tag)) {
#pragma clang loop unroll(disable)
        for (u32 spin = 0; spin < SPIN_CAP; spin++) { selv = gld(a.ws, selo); if (allok((u32)(selv >> 32) == tag)) break; __builtin_amdgcn_s_sleep(2); }
    }
    const u32 s0 = __builtin_amdgcn_readlane((u32)selv, 0), s1 = __builtin_amdgcn_readlane((u32)selv, 1);
    const int i0 = s0 & 2047, i1 = s1 & 2047;
    const bool own0 = (i0 >= 1024) == HEAVY, own1 = (i1 >= 1024) == HEAVY;
    if (own0 || own1) {
        if (h ? own1 : own0) {
            if (!allok((u32)(prtv >> 32) == tag)) {
#pragma clang loop unroll(disable)
                for (u32 spin = 0; spin < SPIN_CAP; spin++) { prtv = gld(a.ws, prto); if (allok((u32)(prtv >> 32) == tag)) break; __builtin_amdgcn_s_sleep(2); }
            }
            const float y = wave_fsum(lane < NFFN ? __uint_as_float((u32)prtv) : 0.0f);
            if (lane == 0) *(LAS float*)(lds + LDS_UPD + w * 4) = rbf(rbf(y) * bfhi(h ? s1 : s0));
        }
        bar();
        const int row0 = 2 * tr + (HEAVY ? 1024 : 0);
#pragma unroll
        for (int e = 0; e < 2; e++) {
            const int row = row0 + e;
            if (row == i0 || row == i1) {
                const f32x4 up = *(const LAS f32x4*)(lds + LDS_UPD + (row == i0 ? 0 : 16));
                float oe[4];
#pragma unroll
                for (int c = 0; c < 4; c++) {
                    const float z = rbf(rbf(zn[c][e]) + up[c]);
                    oe[c] = __builtin_fmaf(sv[c][e], z * sigm(z), xf[c][e]);
                }
                stg(a.out, oo + e * 1024, u32x2{pkbf(oe[0], oe[1]), pkbf(oe[2], oe[3])});
            }
        }
    }
    if (HEAVY && G == 0) {
        const bool ln = ((tid >> 3) & 31) < 8;
#pragma clang loop unroll(disable)
        for (u32 spin = 0; spin < SPIN_CAP && !allok(ln || (u32)(seen >> 32) == tag); spin++) seen = gld(a.ws, WS_SEEN + (tid & 255) * 8);
        bar();
        if (tid == 0) __hip_atomic_store((gu32*)(a.ws + WS_EPOCH), tag, RLX_AGENT);
    }
    STAMPS_OUT;
}

__global__ void __launch_bounds__(512, 2) s4_mod_fused(Args a) {
    extern __shared__ __attribute__((aligned(16))) char smem[];
    LAS char* lds = (LAS char*)smem;
    const int xcd = blockIdx.x & 7, j = blockIdx.x >> 3;
    const int jj = j & 15;
    if (j >= 16) body<0>(a, lds, xcd, jj);
    else if (jj < 8) body<1>(a, lds, xcd, jj);
    else if (jj < 15) body<2>(a, lds, xcd, jj);
    else body<3>(a, lds, xcd, jj);
}
#define ROLE_OF(b) (((b) >> 3) >= 16 ? 0 : ((((b) >> 3) & 15) >= 8 && (((b) >> 3) & 15) < 15 ? 2 : ((((b) >> 3) & 15) == 15 ? 3 : 1)))

#include <cstdio>
#include <vector>
#include <algorithm>
static void dbg_dump(void* d_ws, hipStream_t stream, void* const* d_in, void* d_out) {
    hipStreamCaptureStatus st; (void)hipStreamIsCapturing(stream, &st);
    if (st != hipStreamCaptureStatusNone) return;
    (void)hipStreamSynchronize(stream);
    const int NST = 16;
    std::vector<unsigned long long> h(256 * 8 * NST);
    (void)hipMemcpy(h.data(), (char*)d_ws + WS_DBG, h.size() * 8, hipMemcpyDeviceToHost);
    unsigned long long t0 = ~0ull; for (int i = 0; i < 2048; i++) if (h[i * NST]) t0 = std::min(t0, h[i * NST]);
    fprintf(stderr, "DBG ptrs x=%p are=%p wg=%p out=%p ws=%p clock %.3f\n", d_in[0], d_in[1], d_in[11], d_out, d_ws, (double)(h[15] - h[14]) / (double)(h[10] - h[0]) / 10.0);
    for (int role = 0; role < 3; role++) { fprintf(stderr, "DBG role %d:", role);
        for (int k = 0; k < 14; k++) { std::vector<double> v;
            for (int b = 0; b < 256; b++) { if (ROLE_OF(b) != role) continue; for (int w = 0; w < 8; w++) { unsigned long long t = h[(b * 8 + w) * NST + k]; if (t) v.push_back((t - t0) / 100.0); } }
            if (v.empty()) continue; std::sort(v.begin(), v.end()); fprintf(stderr, " [%d]%.2f/%.2f", k, v[v.size() / 2], v.back()); }
        fprintf(stderr, "\n"); }
}
extern "C" void kernel_launch(void* const* d_in, const int* in_sizes, int n_in, void* d_out, int out_size, void* d_ws,
                              size_t ws_size, hipStream_t stream) {
    Args a;
    a.x = (const u16*)d_in[0]; a.are = (const u16*)d_in[1]; a.aim = (const u16*)d_in[2]; a.bre = (const u16*)d_in[3];
    a.bim = (const u16*)d_in[4]; a.cre = (const u16*)d_in[5]; a.cim = (const u16*)d_in[6]; a.logdt = (const u16*)d_in[7];
    a.dskip = (const u16*)d_in[8]; a.wr = (const u16*)d_in[10]; a.wg = (const u16*)d_in[11]; a.wu = (const u16*)d_in[12];
    a.wd = (const u16*)d_in[13]; a.out = (u16*)d_out; a.ws = (char*)d_ws;
    (void)hipFuncSetAttribute((const void*)s4_mod_fused, hipFuncAttributeMaxDynamicSharedMemorySize, LDS_TOTAL);
    hipLaunchKernelGGL(s4_mod_fused, dim3(256), dim3(512), LDS_TOTAL, stream, a);
    dbg_dump(d_ws, stream, d_in, d_out);
}
```

```cpp
#include <hip/hip_runtime.h>
#include <stdint.h>

typedef unsigned short u16;
typedef unsigned int u32;
typedef unsigned long long u64;
typedef float f32x2 __attribute__((ext_vector_type(2)));
typedef float f32x4 __attribute__((ext_vector_type(4)));
typedef u32 u32x2 __attribute__((ext_vector_type(2)));
typedef u32 u32x3 __attribute__((ext_vector_type(3)));
typedef u32 u32x4 __attribute__((ext_vector_type(4)));
typedef __bf16 bf16x2 __attribute__((ext_vector_type(2)));
typedef __bf16 bf16x8 __attribute__((ext_vector_type(8)));
typedef _Float16 f16x2 __attribute__((ext_vector_type(2)));
typedef _Float16 f16x8 __attribute__((ext_vector_type(8)));
#define GAS __attribute__((address_space(1)))
#define LAS __attribute__((address_space(3)))
#define CAS __attribute__((address_space(4)))
typedef GAS u64 gu64;
typedef GAS u32 gu32;
#define RLX_AGENT __ATOMIC_RELAXED, __HIP_MEMORY_SCOPE_AGENT

constexpr int D_ = 512, F_ = 1792;
constexpr int NFFN = 56;
constexpr u32 WS_EPOCH = 0, WS_SEL = 1024, WS_SEEN = 2048, WS_STATS = 16384, WS_CAND = WS_STATS + 8 * 16384, WS_PART = WS_CAND + 8 * 4096;
constexpr u32 WS_END = WS_PART + 2 * 128 * NFFN * 32;
constexpr u32 WS_DBG = 1u << 21;
constexpr int LDS_LN = 0;
constexpr int LDS_H = 1024;
constexpr int LDS_UPD = LDS_H + 512;
constexpr int LDS_JUNK = LDS_UPD + 64;
constexpr int LDS_TBL = 2048;
constexpr int TBL_ROW = 1040, TBL_H = 57 * TBL_ROW;
constexpr int LDS_Y = LDS_TBL + TBL_H, Y_TILE = 1280, Y_SZ = 4 * Y_TILE;
constexpr int LDS_U = LDS_TBL + 2 * TBL_H;
constexpr int U_CH = (256 + 2048) * 2;
constexpr int LDS_GU = LDS_U + 4 * U_CH;
constexpr int LDS_P = LDS_GU + 2048 + 512, P_SZ = 5344;
constexpr int C_OFF = 4416 + 64 + 112, C_SZ = 432;
constexpr int LDS_TOTAL = LDS_P + 4 * P_SZ;
constexpr int LDS_FLAG = LDS_JUNK + 128;

struct LaneTab { u32 v[128][8]; };
constexpr LaneTab make_tab() {
    LaneTab t{};
    for (int h = 0; h < 2; h++)
        for (int l = 0; l < 64; l++) {
            const int q = l & 15, g = l >> 4;
            u32* o = t.v[h * 64 + l];
            for (int d = 0; d < 4; d++) {
                const int k0 = 8 * g + 2 * d - q - 8, k1 = k0 + 1;
                o[d] = ((k0 >= h - 4 && k0 <= 4 - h) ? 0x3f80u : 0u) | ((k1 >= h - 4 && k1 <= 4 - h) ? 0x3f800000u : 0u);
            }
            o[4] = 4 * (h ? 1080 - 8 * q - (g < 2 ? 256 + 128 * g : 256 * g) : 1084 - 128 * (g >> 1) - 8 * q - 4 * (g & 1));
            o[5] = h ? (g < 2 ? 4432 : o[4] - 512)
                     : (l < 16 ? l * 16 : (l < 20 ? 4096 + l * 16 : (l < 28 ? C_OFF + ((l - 20) >> 2) * C_SZ + 256 + (l & 3) * 16 : 4432)));
            o[6] = (g < 2 || l == 32) ? C_OFF + (q & 1) * C_SZ + 4 * (64 - (q & 1) - 32 * g - (q >> 1)) : C_OFF - 4;
            o[7] = 0;
        }
    return t;
}
static __device__ const LaneTab lane_tab = make_tab();
#define GATEWARM 0
#define ROWNT 1
#define NPRE 17
constexpr u32 SPIN_CAP = 1u << 16;
constexpr int CODE_BACK = 0, CODE_LINES = 140;

struct Args {
    const u16 *x, *are, *aim, *bre, *bim, *cre, *cim, *logdt, *dskip, *wr, *wg, *wu, *wd;
    u16* out;
    char* ws;
};

template <typename T> __device__ __forceinline__ T ldg(const void* base, u32 off) { return *(const GAS T*)((const GAS char*)base + off); }
template <typename T> __device__ __forceinline__ T ldn(const void* base, u32 off) { return *(const GAS T*)((const GAS char*)base + off); }
template <typename T> __device__ __forceinline__ void stg(void* base, u32 off, T v) { *(GAS T*)((GAS char*)base + off) = v; }
__device__ __forceinline__ u64 gld(const char* base, u32 off) { return __hip_atomic_load((gu64*)((GAS char*)base + off), RLX_AGENT); }
__device__ __forceinline__ void gst(char* base, u32 off, u32 tag, u32 v) { __hip_atomic_store((gu64*)((GAS char*)base + off), ((u64)tag << 32) | v, RLX_AGENT); }
__device__ __forceinline__ float bflo(u32 v) { return __uint_as_float(v << 16); }
__device__ __forceinline__ float bfhi(u32 v) { return __uint_as_float(v & 0xffff0000u); }
__device__ __forceinline__ u32 pkbf(float a, float b) { f32x2 v = {a, b}; return __builtin_bit_cast(u32, __builtin_convertvector(v, bf16x2)); }
__device__ __forceinline__ u32 pkh(f32x2 v) { return __builtin_bit_cast(u32, __builtin_convertvector(v, f16x2)); }
__device__ __forceinline__ float rbf(float a) { return bflo(pkbf(a, a)); }
__device__ __forceinline__ f32x2 cmul(f32x2 a, f32x2 z, f32x2 zn) {
    return __builtin_elementwise_fma(a.yx, zn, a * z.xx);
}
__device__ __forceinline__ f32x2 csq(f32x2 a) { return cmul(a, a, f32x2{-a.y, a.y}); }
__device__ __forceinline__ void rawbar() { asm volatile("s_barrier" ::: "memory"); }
__device__ __forceinline__ void bar() { asm volatile("s_waitcnt lgkmcnt(0)\n\ts_barrier" ::: "memory"); }
#define PIN(x) asm volatile("" : "+v"(x))
#define FENCE() asm volatile("" ::: "memory")
__device__ __forceinline__ f32x4 mfma_bf(u32x4 a, u32x4 b, f32x4 c) {
    return __builtin_amdgcn_mfma_f32_16x16x32_bf16(__builtin_bit_cast(bf16x8, a), __builtin_bit_cast(bf16x8, b), c, 0, 0, 0);
}
__device__ __forceinline__ f32x4 mfma_h(u32x4 a, u32x4 b, f32x4 c) {
    return __builtin_amdgcn_mfma_f32_16x16x32_f16(__builtin_bit_cast(f16x8, a), __builtin_bit_cast(f16x8, b), c, 0, 0, 0);
}
template <int CTRL> __device__ __forceinline__ float dppf(float old, float src) {
    return __uint_as_float(__builtin_amdgcn_update_dpp(__float_as_uint(old), __float_as_uint(src), CTRL, 0xf, 0xf, false));
}
template <int CTRL, int RM = 0xf> __device__ __forceinline__ u32 dpp0(u32 src) { return __builtin_amdgcn_update_dpp(0u, src, CTRL, RM, 0xf, false); }
__device__ __forceinline__ u32 row_umax(u32 v) {
    v = max(v, dpp0<0x128>(v)); v = max(v, dpp0<0x124>(v)); v = max(v, dpp0<0x122>(v)); v = max(v, dpp0<0x121>(v));
    return v;
}
__device__ __forceinline__ u32 wave_umax(u32 v) {
    v = row_umax(v); v = max(v, dpp0<0x142, 0xa>(v)); v = max(v, dpp0<0x143, 0xc>(v));
    return __builtin_amdgcn_readlane(v, 63);
}
__device__ __forceinline__ float wave_fsum(float f) {
    f += __uint_as_float(dpp0<0x128>(__float_as_uint(f))); f += __uint_as_float(dpp0<0x124>(__float_as_uint(f)));
    f += __uint_as_float(dpp0<0x122>(__float_as_uint(f))); f += __uint_as_float(dpp0<0x121>(__float_as_uint(f)));
    f += __uint_as_float(dpp0<0x142, 0xa>(__float_as_uint(f))); f += __uint_as_float(dpp0<0x143, 0xc>(__float_as_uint(f)));
    return __uint_as_float(__builtin_amdgcn_readlane(__float_as_uint(f), 63));
}
__device__ __forceinline__ float sigm(float z) { return __builtin_amdgcn_rcpf(1.0f + __builtin_amdgcn_exp2f(z * -1.44269504089f)); }
__device__ __forceinline__ u64 bad(u64 granule, u32 tag) { return __builtin_amdgcn_ballot_w64((u32)(granule >> 32) != tag); }

#define STAMP(i)
#define STAMPS_DECL
#define STAMPS_OUT

__global__ void __launch_bounds__(768, 3) s4_mod_fused(Args a) {
    extern __shared__ __attribute__((aligned(16))) char smem[];
    LAS char* const lds = (LAS char*)smem;
    const int tid = threadIdx.x, lane = tid & 63;
    const int w = __builtin_amdgcn_readfirstlane(tid >> 6);
    const int xcd = blockIdx.x & 7, j = blockIdx.x >> 3, jj = j & 15;
    const bool heavy = j >= 16, ffn = j < 7, lnr = j >= 7 && j < 15;
    const int q = lane & 15, g = lane >> 4;
    const int ch = w & 3, h = w >> 2;
    const int G = xcd * 16 + jj;
    const f32x4 z4 = {0, 0, 0, 0};
    STAMPS_DECL;
    const u64 tstart_ = __builtin_amdgcn_s_memrealtime();
    STAMP(0);
    u32 tag;
    const __amdgpu_buffer_rsrc_t rw = __builtin_amdgcn_make_buffer_rsrc((void*)a.ws, 0, WS_END, 0x00020000);
    auto bst = [&](const bool on, const u32 off, const u32 v) {
        __builtin_amdgcn_raw_buffer_store_b64(u32x2{v, tag}, rw, on ? off : 0xfffffff0u, 0, 16);
    };

    u32 p_ldt, p_are, p_aim, p_bre, p_bim, p_cre, p_cim, warm0;
    u32x3 adr;
    {
        warm0 = ldg<u32>((const char*)(__builtin_amdgcn_s_getpc() & ~0x7full), min(j * 5 + (tid & 7), CODE_LINES - 1) * 128);
        asm volatile("" :: "s"(a.x), "s"(a.are), "s"(a.aim), "s"(a.bre), "s"(a.bim), "s"(a.cre), "s"(a.cim), "s"(a.logdt), "s"(a.dskip), "s"(a.wr),
                     "s"(a.wg), "s"(a.wu), "s"(a.wd), "s"(a.out), "s"(a.ws) : "memory");
    }
    if (h < 2) {
        const u32 po = ((4 * G + ch) * 128 + 64 * h + lane) * 2;
        p_ldt = ldg<u16>(a.logdt, (4 * G + ch) * 2);
        p_are = ldn<u16>(a.are, po); p_aim = ldn<u16>(a.aim, po); p_bre = ldn<u16>(a.bre, po); p_bim = ldn<u16>(a.bim, po);
        p_cre = ldn<u16>(a.cre, po); p_cim = ldn<u16>(a.cim, po);
        adr = ldg<u32x4>(&lane_tab, (h * 64 + lane) * 32 + 16).xyz;
        *(LAS u32x4*)(lds + LDS_FLAG + (lane < 2 ? 128 + lane * 16 : w * 16)) = u32x4{0, 0, 0, 0};
    }
    int h2 = h;
    asm volatile("" : "+s"(h2) :: "memory");

    if (h2 < 2) {
        bar();
        const u32 tw = LDS_TBL + h * TBL_H + ch * 256, g16 = lane & 48, q16 = q * 16;
        u32 a_tf = __umul24(q, TBL_ROW) + g16 + tw, a_ln = __umul24(g, TBL_ROW) + q16 + tw, a_y = __umul24(g, 320) + q16 + (LDS_Y + w * Y_SZ);
        u32 a_cr = g16 - 4 * q + (LDS_P + ch * P_SZ + C_OFF + h * C_SZ + 60), a_pr = 2 * g16 - 4 * q + (LDS_P + ch * P_SZ + 188 - 128 * h);
        u32 a_u = 2 * q16 + g16 + (LDS_U + ch * U_CH + 464 + 16 * h), a_b = a_u + (16 - 80 * h - (heavy ? 0 : 2048));
        u32 prto = min(lane, NFFN - 1) * 32 + (WS_PART + (h * 128 + G) * NFFN * 32 + ch * 8), selo = (lane & 1) * 8 + (WS_SEL + xcd * 128);
        PIN(a_tf); PIN(a_ln); PIN(a_y); PIN(a_pr); PIN(a_cr); PIN(a_u); PIN(a_b); PIN(prto); PIN(selo);
        __builtin_amdgcn_sched_barrier(0);
        if (h == 0) __builtin_amdgcn_s_setprio(2); else __builtin_amdgcn_s_setprio(1);
        STAMP(11);
        PIN(p_ldt); PIN(p_are); PIN(p_aim); PIN(p_bre); PIN(p_bim); PIN(p_cre); PIN(p_cim);
        STAMP(3);
        const float ld = bflo(p_ldt);
        const float t0 = ld * 1.44269504089f;
        const float te = __builtin_fmaf(ld, 1.92596303e-8f, __builtin_fmaf(ld, 1.44269504089f, -t0));
        const float ex = __builtin_amdgcn_exp2f(t0);
        float dt = rbf(__builtin_fmaf(ex * te, 0.69314718056f, ex));
        const float lr = (dt * 1.44269504089f) * bflo(p_are);
        const float ai = dt * bflo(p_aim);
        const float th = ai * 0.15915494309f;
        const float tl = __builtin_fmaf(ai, 6.4206382e-9f, __builtin_fmaf(ai, 0.15915494309f, -th));
        const float bi = bflo(p_bim);
        const f32x2 W = cmul(f32x2{bflo(p_cre), bflo(p_cim)}, f32x2{bflo(p_bre), bi}, f32x2{-bi, bi}) * dt;
        const float gp = fmaxf(lr, 0.0f);
        const int en = __builtin_amdgcn_frexp_expf(fmaxf(fabsf(W.x), fabsf(W.y))) + (int)(2168.0f * gp);
        const int E = min(max((int)wave_umax((u32)(en + 201)) - 200, -100), 120);
        const float sA = __builtin_amdgcn_ldexpf(__builtin_amdgcn_exp2f(120.0f * gp), 13 - E);
        const float inv = __uint_as_float((u32)(E - 13 + 127) << 23);
        const float t1 = __builtin_amdgcn_fractf(th), e1 = __builtin_amdgcn_exp2f(lr);
        const f32x2 z1 = f32x2{__builtin_amdgcn_cosf(t1), __builtin_amdgcn_sinf(t1)} * e1;
        const float t7 = __builtin_fmaf(128.0f, tl, __builtin_amdgcn_fractf(128.0f * th)), e7 = __builtin_amdgcn_exp2f(128.0f * lr);
        const f32x2 z128 = f32x2{__builtin_amdgcn_cosf(t7), __builtin_amdgcn_sinf(t7)} * e7;
        const f32x2 z2 = csq(z1), zz4 = csq(z2), z8 = csq(zz4);
        f32x2 n1 = {-z1.y, z1.y}, n2 = {-z2.y, z2.y}, n4 = {-zz4.y, zz4.y}, n128 = {-z128.y, z128.y}, c8 = {z8.y, -z8.y};
        f32x2 X = W * sA, B = {__builtin_amdgcn_exp2f(-120.0f * gp), 0.0f};
        PIN(adr);
        const u32 a_e0 = adr[0] + (LDS_P + ch * P_SZ), a_e1 = adr[1] + (LDS_P + ch * P_SZ), a_cw = adr[2] + (LDS_P + ch * P_SZ);
        STAMP(12);
        {
            const u32 m0v = tw + (u32)(size_t)lds;
            asm volatile("s_mov_b32 m0, %0\n\ts_nop 0" :: "s"(m0v) : "memory");
        }
        auto put2 = [&](const int r0, const f32x2 v0, const int r1, const f32x2 v1) {
            asm volatile("ds_write_addtid_b32 %0 offset:%c2\n\tds_write_addtid_b32 %1 offset:%c3" :: "v"(pkh(v0)), "v"(pkh(v1)), "i"(r0 * TBL_ROW), "i"(r1 * TBL_ROW));
        };
        {
            f32x2 c0 = X, X1 = cmul(X, z128, n128), c1 = X1;
            X = cmul(X1, z128, n128);
            f32x2 d = X;
#pragma unroll
            for (int i = 0; i < 8; i++) {
                put2(i, c0, 8 + i, c1);
                put2(41 + i, B, 16 + i, d);
                if (i < 7) { c0 = cmul(c0, z1, n1); c1 = cmul(c1, z1, n1); }
                B = cmul(B, z8, c8);
                if (i == 3) d = X; else if (i < 7) d = cmul(d, z2, n2);
                if (i == 2 || i == 6) X = cmul(X, z128, n128);
            }
#pragma unroll
            for (int i = 0; i < 4; i++) {
                const f32x2 B1 = cmul(B, z8, c8), e = cmul(X, zz4, n4), Xn = cmul(X, z128, n128);
                put2(49 + 2 * i, B, 24 + 2 * i, X);
                if (i < 3) B = cmul(B1, z8, c8);
                put2(50 + 2 * i, B1, 25 + 2 * i, e);
                X = Xn;
            }
        }
        FENCE();
        STAMP(2);
        f32x4 ka, kb;
        float k255, k1024;
        {
            u32x4 fa[4][4];
#pragma unroll
            for (int s = 0; s < 4; s++) {
                fa[s][3] = *(const LAS u32x4*)(lds + a_tf + 41 * TBL_ROW + s * 64);
#pragma unroll
                for (int t = 0; t < 2; t++) fa[s][t] = *(const LAS u32x4*)(lds + a_tf + 16 * t * TBL_ROW + s * 64);
            }
            FENCE();
            const f32x2 X9 = cmul(X, z128, n128);
            put2(32, X, 33, X9);
            if (heavy) {
                const f32x2 z256 = csq(z128), n256 = {-z256.y, z256.y};
                f32x2 e = X, o = X9;
#pragma unroll
                for (int i = 0; i < 3; i++) { e = cmul(e, z256, n256); o = cmul(o, z256, n256); put2(34 + 2 * i, e, 35 + 2 * i, o); }
                e = cmul(e, z256, n256);
                put2(40, e, 40, e);
            }
            __builtin_amdgcn_s_setprio(0);
            FENCE();
#pragma unroll
            for (int s = 0; s < 4; s++) fa[s][2] = *(const LAS u32x4*)(lds + a_tf + 32 * TBL_ROW + s * 64);
            __builtin_amdgcn_sched_barrier(0);
            f32x4 k0, k1, k2;
#pragma unroll
            for (int s = 0; s < 4; s++) { k0 = mfma_h(fa[s][0], fa[s][3], s ? k0 : z4); k1 = mfma_h(fa[s][1], fa[s][3], s ? k1 : z4); }
#pragma unroll
            for (int s = 0; s < 4; s++) k2 = mfma_h(fa[s][2], fa[s][3], s ? k2 : z4);
            FENCE();
            const int dlt = h ? -TBL_H : TBL_H;
            LAS char* fo = lds + LDS_FLAG + w * 16;
            const LAS char* fi = lds + LDS_FLAG + (w ^ 4) * 16;
            if (h) {
                *(LAS f32x4*)(lds + a_ln) = k0 * inv; *(LAS f32x4*)(lds + a_ln + 4 * TBL_ROW) = k2 * inv;
                asm volatile("s_waitcnt lgkmcnt(0)" ::: "memory");
                *(volatile LAS u32*)fo = 1;
                STAMP(13);
                u32x4 fl; f32x4 o;
#pragma clang loop unroll(disable)
                for (u32 spin = 0; spin < SPIN_CAP; spin++) {
                    fl = *(volatile LAS u32x4*)fi; o = *(volatile LAS f32x4*)(lds + a_ln + dlt);
                    if (__builtin_amdgcn_readfirstlane(fl[0])) break;
                    __builtin_amdgcn_s_sleep(2);
                }
                ka = k1 * inv + o;
                k255 = __uint_as_float(__builtin_amdgcn_readlane(__float_as_uint(k0[3]), 63)) * inv + __uint_as_float(fl[1]);
                k1024 = __uint_as_float(__builtin_amdgcn_readlane(__float_as_uint(k2[0]), 0)) * inv + __uint_as_float(fl[2]);
            } else {
                *(LAS f32x4*)(lds + a_ln) = k1 * inv;
                const float s0 = __uint_as_float(__builtin_amdgcn_readlane(__float_as_uint(k0[3]), 63)) * inv;
                const float s1 = __uint_as_float(__builtin_amdgcn_readlane(__float_as_uint(k2[0]), 0)) * inv;
                asm volatile("s_waitcnt lgkmcnt(0)" ::: "memory");
                *(volatile LAS u32x4*)fo = u32x4{1, __float_as_uint(s0), __float_as_uint(s1), 0};
                STAMP(13);
                u32 fl; f32x4 o0, o1;
#pragma clang loop unroll(disable)
                for (u32 spin = 0; spin < SPIN_CAP; spin++) {
                    fl = *(volatile LAS u32*)fi; o0 = *(volatile LAS f32x4*)(lds + a_ln + dlt); o1 = *(volatile LAS f32x4*)(lds + a_ln + dlt + 4 * TBL_ROW);
                    if (__builtin_amdgcn_readfirstlane(fl)) break;
                    __builtin_amdgcn_s_sleep(2);
                }
                ka = k0 * inv + o0; kb = k2 * inv + o1;
                k1024 = __uint_as_float(__builtin_amdgcn_readlane(__float_as_uint(kb[0]), 0));
            }
        }
        STAMP(1);
        if (h == 0) {
            const f32x4 v = ka;
            auto sw = __builtin_amdgcn_permlane16_swap(__float_as_uint(v[3]), __float_as_uint(v[3]), false, false);
            const float pd = dppf<0x138>(0.0f, __uint_as_float(sw[1]));
            const float pv = (g & 1) ? __uint_as_float(sw[0]) : pd;
            *(LAS u32x4*)(lds + a_e0) = u32x4{pkbf(v[3], v[2]), pkbf(v[2], v[1]), pkbf(v[1], v[0]), pkbf(v[0], pv)};
            const u32 kk = pkbf(k1024, k1024);
            const u32 f7 = lane == 15 ? kk : 0u, f6 = lane == 14 ? kk : 0u;
            *(LAS u32x4*)(lds + a_e1) = u32x4{f7, f7, f7 | (f6 & 0xffff0000u), f7 | f6};
            const f32x4 u = {lane == 0 ? 0.0f : kb[0], kb[1], kb[2], kb[3]};
            const bool e = q >= 14;
            LAS u32* cw = (LAS u32*)(lds + a_cw);
            cw[0] = pkbf(u[0], dppf<0x138>(0.0f, dppf<0x138>(0.0f, e ? u[3] : u[0])));
#pragma unroll
            for (int i = 1; i < 4; i++) cw[-8 * i] = pkbf(u[i], dppf<0x122>(0.0f, e ? u[i - 1] : u[i]));
        } else {
            const f32x4 v = ka;
            const bool s2 = g < 2;
            const float nw = dppf<0x130>(v[0], v[0]);
            const float nr = dppf<0x12f>(v[0], q == 0 ? v[2] : v[0]);
            const float nA = s2 ? nw : nr;
            const float nB = dppf<0x130>(k1024, q == 0 ? v[0] : v[2]);
            const float x2 = s2 ? v[1] : v[0], x3 = s2 ? v[2] : v[1], x6 = s2 ? v[3] : nA;
            const u32 aa = pkbf(v[0], v[0]), cc = pkbf(x3, x3), a2 = pkbf(v[2], v[2]), b2 = pkbf(v[3], v[3]);
            *(LAS u32x4*)(lds + a_e0) = u32x4{pkbf(nA, x6), pkbf(x6, x3), cc, cc};
            *(LAS u32x4*)(lds + a_e0 + 16) = u32x4{pkbf(x3, x2), pkbf(x2, v[0]), aa, pkbf(v[0], lane == 0 ? k255 : v[0])};
            *(LAS u32x4*)(lds + a_e1) = u32x4{pkbf(nB, nB), pkbf(nB, v[3]), b2, b2};
            *(LAS u32x4*)(lds + a_e1 + 16) = u32x4{pkbf(v[3], v[2]), a2, a2, a2};
        }
        STAMP(4);
        u32x4 A[17];
        const LAS u32* pr = (const LAS u32*)(lds + a_pr);
        auto lda = [&](const int m) { A[m] = u32x4{pr[64 * (16 - m)], pr[64 * (16 - m) + 2], pr[64 * (16 - m) + 4], pr[64 * (16 - m) + 6]}; };
        {
            asm volatile("s_waitcnt lgkmcnt(0)" ::: "memory");
            *(volatile LAS u32*)(lds + LDS_FLAG + w * 16 + 12) = 2;
#pragma clang loop unroll(disable)
            for (u32 spin = 0; spin < SPIN_CAP; spin++)
                if (__builtin_amdgcn_readfirstlane(*(volatile LAS u32*)(lds + LDS_FLAG + (w ^ 4) * 16 + 12)) == 2) break;
            FENCE();
        }
#pragma unroll
        for (int m = 0; m < NPRE; m++) lda(m);
        asm volatile("" :: "v"(warm0) : "memory");
        STAMP(5);
        if (ffn) rawbar();
        rawbar();
        STAMP(7);

        f32x4 acc[4];
        u64 selv, prtv;
        {
            auto ldb = [&](const int k) { return *(const LAS u32x4*)(lds + a_b + 128 * (28 - k)); };
            u32x4 Bf[29];
            auto mm = [&](const int k, const int first) {
#pragma unroll
                for (int i = 3; i >= 0; i--) {
                    const int mp = 4 * i - 12 + k;
                    if (mp >= 0 && mp < 17) acc[i] = mfma_bf(A[mp], Bf[k], (first >> i) & 1 ? z4 : acc[i]);
                }
            };
            if (heavy) {
                const LAS char* uw = lds + a_u;
                const LAS u32* cr = (const LAS u32*)(lds + a_cr);
                u32x4 Wd[4], KT[4], band, S0, S1;
                f32x4 dw[4];
#pragma unroll
                for (int k = 0; k < 3; k++) { Bf[16 + k] = ldb(16 + k); Bf[k] = ldb(k); }
                __builtin_amdgcn_sched_barrier(0);
                STAMP(6);
#pragma unroll
                for (int k = 0; k < 16; k++) {
                    if (k + 3 < 13) Bf[19 + k] = ldb(19 + k);
                    if (k + 3 < 16) Bf[k + 3] = ldb(k + 3);
                    if (k == 3) band = *(const LAS u32x4*)(lds + LDS_GU + (tid & 63) * 16 + h * 1024);
                    if (k >= 4 && k < 8) Wd[k - 4] = *(const LAS u32x4*)(uw + 512 * (k - 4));
                    if (k >= 10 && k < 13) KT[13 - k] = u32x4{cr[16 * (k - 10)], cr[16 * (k - 10) + 2], cr[16 * (k - 10) + 16], cr[16 * (k - 10) + 18]};
                    if (k == 13) KT[0] = u32x4{cr[48], cr[50], 0u, 0u};
                    FENCE();
                    if (k < 13) mm(k + 16, k == 0 ? 15 : 0);
                    if (k >= 8 && k < 12) dw[k - 8] = mfma_bf(Wd[k - 8], band, z4);
                    mm(k, 0);
                    if (k == 11) S0 = u32x4{pkbf(dw[0][0], dw[0][1]), pkbf(dw[0][2], dw[0][3]), pkbf(dw[1][0], dw[1][1]), pkbf(dw[1][2], dw[1][3])};
                    if (k == 13) S1 = u32x4{pkbf(dw[2][0], dw[2][1]), pkbf(dw[2][2], dw[2][3]), pkbf(dw[3][0], dw[3][1]), pkbf(dw[3][2], dw[3][3])};
                    __builtin_amdgcn_sched_barrier(0);
                }
#pragma unroll
                for (int i = 3; i >= 0; i--) acc[i] = mfma_bf(S0, KT[i], acc[i]);
                acc[3] = mfma_bf(S1, KT[1], acc[3]); acc[2] = mfma_bf(S1, KT[0], acc[2]);
            } else {
#pragma unroll
                for (int k = 0; k < 5; k++) Bf[15 - k] = ldb(15 - k);
                __builtin_amdgcn_sched_barrier(0);
#pragma unroll
                for (int k = 15; k >= 0; k--) {
                    if (k >= 5) Bf[k - 5] = ldb(k - 5);
                    mm(k, k == 15 ? 15 : 0);
                    __builtin_amdgcn_sched_barrier(0);
                }
            }
        }
        STAMP(9);
        selv = gld(a.ws, selo); prtv = gld(a.ws, prto);
        tag = __builtin_amdgcn_readfirstlane(*(LAS u32*)(lds + LDS_UPD + 48));
#pragma unroll
        for (int i = 3; i >= 0; i--) *(LAS f32x4*)(lds + a_y + i * Y_TILE) = acc[i];
        STAMP(8);
        bar();
        if (bad(selv, tag)) {
#pragma clang loop unroll(disable)
            for (u32 spin = 0; spin < SPIN_CAP; spin++) { selv = gld(a.ws, selo); if (!bad(selv, tag)) break; __builtin_amdgcn_s_sleep(2); }
        }
        const u32 sel = h ? __builtin_amdgcn_readlane((u32)selv, 1) : __builtin_amdgcn_readlane((u32)selv, 0);
        const bool own = ((sel & 2047) >= 1024) == heavy;
        if (own) {
            if (bad(prtv, tag)) {
#pragma clang loop unroll(disable)
                for (u32 spin = 0; spin < SPIN_CAP; spin++) { prtv = gld(a.ws, prto); if (!bad(prtv, tag)) break; __builtin_amdgcn_s_sleep(1); }
            }
            const float y = wave_fsum(lane < NFFN ? __uint_as_float((u32)prtv) : 0.0f);
            *(LAS float*)(lds + LDS_UPD + w * 4) = rbf(rbf(y) * bfhi(sel));
        }
        *(LAS u32*)(lds + LDS_UPD + 32 + h * 4) = own ? sel & 2047u : 0x7fffffffu;
        bar();
    } else {
        __builtin_amdgcn_s_setprio(3);
        const int lt = (tid + 64 * jj) & 255;
        const u32 xo = lt * 4096 + G * 8;
        u32 xh = xo + (heavy ? 1024 * 1024 : 0);
        const u32 so = WS_STATS + xcd * 16384 + lt * 32, sh = so + (heavy ? 8192 : 0);
        u32 warm1 = 0, warm2 = 0, warm3 = 0;
        u32x2 xc[4], xl[4];
        u64 sg[4], sl[4], cd[4], ce[4];
        f32x2 xf[4][2], zn[4][2], gt[4][2];
        u32 epoch;
        u32x2 dsk, dskr;
        asm volatile("s_load_dword %0, %2, 0x0\n\ts_load_dwordx2 %1, %3, %4" : "=&s"(epoch), "=&s"(dskr) : "s"(a.ws), "s"(a.dskip), "s"(G * 8));
        auto first = [&]() {
            asm volatile("s_waitcnt lgkmcnt(0)\n\ts_add_u32 %0, %2, 1\n\ts_mov_b64 %1, %3" : "=&s"(tag), "=&s"(dsk) : "s"(epoch), "s"(dskr) : "scc");
        };
        u32x4 bnd;
        if (heavy) bnd = ldg<u32x4>(&lane_tab, (tid & 127) * 32);
        if (lnr) {
            const int lnu = (blockIdx.x - 56) * 4 + ch;
            u32x4 xa[8], cb[8];
#pragma unroll
            for (int s = 0; s < 8; s++) xa[s] = (ROWNT ? ldn<u32x4> : ldg<u32x4>)(a.x, (lnu * 8 + (q >> 1)) * 1024 + (q & 1) * 64 + g * 16 + 128 * s);
            rawbar();
#pragma unroll
            for (int s = 0; s < 8; s++) cb[s] = ldg<u32x4>(a.wr, (q & 2) * 32 + g * 16 + 128 * s);
            const u32x4 ones = {0x3f803f80u, 0x3f803f80u, 0x3f803f80u, 0x3f803f80u};
            const int rr = 2 * g + ((q >> 1) & 1);
            u32 o_st = WS_STATS + ((q & 1) + 2 * (q >> 2)) * 16384 + (lnu * 8 + rr) * 8, o_id = 0xffffu - (lnu * 8 + rr);
            u32 o_cd = (WS_CAND + (lane >> 1) * 4096 + lnu * 16 + (lane & 1) * 8) | (u32)(((15 - lane) >> 31) << 20);
            PIN(o_st); PIN(o_id); PIN(o_cd);
            first();
            f32x4 d0 = z4, d1 = z4, d2 = z4, d3 = z4;
#pragma unroll
            for (int s = 0; s < 8; s++) { d2 = mfma_bf(xa[s], xa[s], d2); d0 = mfma_bf(xa[s], ones, d0); }
            asm volatile("" : "+v"(d0), "+v"(d2));
            __builtin_amdgcn_sched_barrier(0);
#pragma unroll
            for (int s = 0; s < 8; s++) { d1 = mfma_bf(xa[s], cb[s], d1); d3 = mfma_bf(ones, cb[s], d3); }
            STAMP(13);
            const bool hi = q & 2;
            const float da = (q & 1) ? d2[1] : d2[0], db = (q & 1) ? d2[3] : d2[2];
            float sq = (q >> 2) == g ? (hi ? db : da) : 0.0f;
            sq += __uint_as_float(dpp0<0xb1>(__float_as_uint(sq)));
            sq += __uint_as_float(dpp0<0x124>(__float_as_uint(sq)));
            sq += __uint_as_float(dpp0<0x128>(__float_as_uint(sq)));
            const float mean = (hi ? d0[2] + d0[3] : d0[0] + d0[1]) * (1.0f / D_);
            const float var = fmaxf(sq * (1.0f / D_) - mean * mean, 0.0f);
            const float sd = rbf(__builtin_amdgcn_sqrtf(rbf(rbf(var) + 1e-5f)));
            const u32 ms = pkbf(mean, sd);
            bst(true, o_st, ms);
            const float dot = (hi ? d1[3] : d1[0]) + __uint_as_float(dpp0<0x4e>(__float_as_uint(hi ? d1[1] : d1[2])));
            const float sw = d3[0] + __uint_as_float(dpp0<0x4e>(__float_as_uint(d3[0])));
            const float r = (dot - bflo(ms) * sw) * __builtin_amdgcn_rcpf(sd);
            const u32 rb = pkbf(r, r);
            const u32 key = ((rb ^ (((int)rb >> 31) | 0x80000000u)) & 0xffff0000u) | o_id;
            const u32 ko = dpp0<0x4e>(key);
            auto x1 = __builtin_amdgcn_permlane16_swap(max(key, ko), max(key, ko), false, false);
            auto x2 = __builtin_amdgcn_permlane16_swap(min(key, ko), min(key, ko), false, false);
            const u32 a1 = max(x1[0], x1[1]), a2 = max(min(x1[0], x1[1]), max(x2[0], x2[1]));
            auto y1 = __builtin_amdgcn_permlane32_swap(a1, a1, false, false);
            auto y2 = __builtin_amdgcn_permlane32_swap(a2, a2, false, false);
            bst(true, o_cd, (lane & 1) ? max(min(y1[0], y1[1]), max(y2[0], y2[1])) : max(y1[0], y1[1]));
            STAMP(1);
            FENCE();
            *(volatile LAS u32*)(lds + LDS_FLAG + 128 + ch * 4) = 1;
#pragma clang loop unroll(disable)
            for (u32 spin = 0; spin < SPIN_CAP; spin++) {
                const u32x4 f0 = *(volatile LAS u32x4*)(lds + LDS_FLAG + 128);
                if (__builtin_amdgcn_readfirstlane(f0[0] & f0[1] & f0[2] & f0[3])) break;
            }
            FENCE();
        } else {
            warm1 = ldn<u32>(a.x, heavy ? (jj * 128 + ch * 32 + (lane & 31)) * 1024 + xcd * 128 : 0);
            rawbar();
        }
        auto second = [&]() {
            if (!lnr) { first(); bst(lane == 0, WS_SEEN + ((blockIdx.x < 56 ? blockIdx.x : blockIdx.x - 64) * 4 + ch) * 8, 0); }
            *(LAS u32*)(lds + LDS_UPD + 48) = tag;
            *(LAS u32x2*)(lds + LDS_U + ch * U_CH + lane * 8) = u32x2{0, 0};
        };
        u32 yb = LDS_Y + (lt >> 6) * Y_TILE + (lt & 3) * 320 + ((lt >> 2) & 15) * 16;
        const u32 a_st = LDS_U + 512 + 8 * lt;
        auto tags4 = [&](const u64 (&v)[4]) { return (bad(v[0], tag) | bad(v[1], tag) | bad(v[2], tag) | bad(v[3], tag)) == 0; };
        auto sweep4 = [&](u64 (&v)[4], const u32 off, const u32 stride) {
            if (!tags4(v)) {
#pragma clang loop unroll(disable)
                for (u32 spin = 0; spin < SPIN_CAP; spin++) {
#pragma unroll
                    for (int i = 0; i < 4; i++) v[i] = gld(a.ws + stride * i, off);
                    if (tags4(v)) break;
                    __builtin_amdgcn_s_sleep(4);
                }
            }
        };
        auto stage = [&](const u64 (&st)[4], const u32x2 (&r)[4], const u32 dst, const bool keep) {
            f32x2 rs[2], nb[2];
#pragma unroll
            for (int p = 0; p < 2; p++) {
                rs[p] = f32x2{__builtin_amdgcn_rcpf(bfhi((u32)st[2 * p])), __builtin_amdgcn_rcpf(bfhi((u32)st[2 * p + 1]))};
                nb[p] = f32x2{bflo((u32)st[2 * p]), bflo((u32)st[2 * p + 1])} * rs[p];
            }
#pragma unroll
            for (int c = 0; c < 4; c++) {
                u32x2 o;
#pragma unroll
                for (int p = 0; p < 2; p++) {
                    const u32 w0 = r[2 * p][c >> 1], w1 = r[2 * p + 1][c >> 1];
                    const f32x2 xv = (c & 1) ? f32x2{bfhi(w0), bfhi(w1)} : f32x2{bflo(w0), bflo(w1)};
                    const f32x2 z = __builtin_elementwise_fma(xv, rs[p], -nb[p]);
                    if (keep) { xf[c][p] = xv; zn[c][p] = z; }
                    o[p] = pkbf(z.x, z.y);
                }
                *(LAS u32x2*)(lds + dst + c * U_CH) = o;
            }
        };
        auto gates = [&]() {
#pragma unroll
            for (int c = 0; c < 4; c++)
#pragma unroll
                for (int p = 0; p < 2; p++) {
                    const f32x2 e = zn[c][p] * -1.44269504089f;
                    const f32x2 d = f32x2{__builtin_amdgcn_exp2f(e.x), __builtin_amdgcn_exp2f(e.y)} + 1.0f;
                    gt[c][p] = zn[c][p] * f32x2{__builtin_amdgcn_rcpf(d.x), __builtin_amdgcn_rcpf(d.y)};
                    PIN(gt[c][p]);
                }
        };
        if (ffn) {
            const int f = xcd * 7 + jj;
            u32 wgu[2][4][8], wdn[4][8];
            const __amdgpu_buffer_rsrc_t rg = __builtin_amdgcn_make_buffer_rsrc((void*)((f & 1) ? a.wu : a.wg), 0, D_ * F_ * 2, 0x00020000);
            const __amdgpu_buffer_rsrc_t ru = __builtin_amdgcn_make_buffer_rsrc((void*)((f & 1) ? a.wg : a.wu), 0, D_ * F_ * 2, 0x00020000);
            const __amdgpu_buffer_rsrc_t rd = __builtin_amdgcn_make_buffer_rsrc((void*)a.wd, 0, D_ * F_ * 2, 0x00020000);
            const u32 og = ((128 * ch + 8 * g) * F_ + 32 * f + 2 * q) * 2, od = ((32 * f + 8 * g) * D_ + 128 * ch + 2 * q) * 2;
#pragma unroll
            for (int m = 0; m < 2; m++)
#pragma unroll
                for (int s = 0; s < 4; s++)
#pragma unroll
                    for (int i = 0; i < 8; i++) wgu[m][s][i] = __builtin_amdgcn_raw_buffer_load_b32(m ? ru : rg, og, (32 * s + i) * F_ * 2, 0);
#pragma unroll
            for (int i = 0; i < 8; i++)
#pragma unroll
                for (int u = 0; u < 4; u++) wdn[u][i] = __builtin_amdgcn_raw_buffer_load_b32(rd, od + ((i & 3) * D_ * 2 + u * 64), (i & 4) * D_ * 2, 0);
            second();
            STAMP(12);
            const u32 co = WS_CAND + xcd * 4096 + lane * 8;
#pragma unroll
            for (int i = 0; i < 4; i++) { cd[i] = gld(a.ws + 512 * i, co); ce[i] = gld(a.ws + 512 * i + 2048, co); }
            u32x4 fg[2][4][2], fd[4][2];
#pragma unroll
            for (int m = 0; m < 3; m++)
#pragma unroll
                for (int s = 0; s < 4; s++)
#pragma unroll
                    for (int p = 0; p < 2; p++) {
                        u32x4 o;
#pragma unroll
                        for (int d = 0; d < 4; d++)
                            o[d] = __builtin_amdgcn_perm(m < 2 ? wgu[m & 1][s][2 * d + 1] : wdn[s][2 * d + 1], m < 2 ? wgu[m & 1][s][2 * d] : wdn[s][2 * d], p ? 0x07060302u : 0x05040100u);
                        PIN(o);
                        if (m < 2) fg[m & 1][s][p] = o; else fd[s][p] = o;
                    }
            u32x4 an[4];
            const u32 o1 = q < 2 ? 0u : 0x3f803f80u;
            const __amdgpu_buffer_rsrc_t rx = __builtin_amdgcn_make_buffer_rsrc((void*)a.x, 0, 2048 * 1024, 0x00020000);
            asm volatile("" :: "v"(warm0), "v"(warm1));
            sweep4(cd, co, 512);
            sweep4(ce, co + 2048, 512);
            STAMP(4);
            u32 ka = max((u32)cd[0], (u32)ce[0]), kb = min((u32)cd[0], (u32)ce[0]);
#pragma unroll
            for (int i = 1; i < 4; i++) {
                const u32 ha = max((u32)cd[i], (u32)ce[i]), hb = min((u32)cd[i], (u32)ce[i]);
                kb = max(min(ka, ha), max(kb, hb)); ka = max(ka, ha);
            }
            const u32 k1 = wave_umax(ka), k2 = wave_umax(ka == k1 ? kb : ka);
            const u32 kk = (lane & 1) ? k2 : k1;
            const u32 row = ~kk & 2047u;
            const u32 xr = q < 2 ? row * 1024 + ch * 256 + g * 16 : 0xfffff000u;
#pragma unroll
            for (int s = 0; s < 4; s++) an[s] = __builtin_amdgcn_raw_buffer_load_b128(rx, xr + 64 * s, 0, 0);
            const u32 sro = WS_STATS + xcd * 16384 + row * 8;
            u64 sr = gld(a.ws, sro);
            STAMP(11);
            if (jj == 0 && ch == 0) {
                const float sv = sigm(__uint_as_float((kk ^ ((~(int)kk >> 31) | 0x80000000u)) & 0xffff0000u));
                bst(lane < 2, WS_SEL + xcd * 128 + lane * 8, row | (pkbf(sv, sv) << 16));
            }
#pragma unroll
            for (int i = 0; i < 4; i++) xc[i] = ldg<u32x2>(a.x, xh + 1024 * i);
#pragma unroll
            for (int i = 0; i < 4; i++) sg[i] = gld(a.ws + 8 * i, sh);
            f32x4 c[2][2];
#pragma unroll
            for (int s = 0; s < 4; s++)
#pragma unroll
                for (int mt = 0; mt < 2; mt++)
#pragma unroll
                    for (int p = 0; p < 2; p++) c[mt][p] = mfma_bf(an[s] | o1, fg[mt][s][p], s ? c[mt][p] : z4);
            if (bad(sr, tag)) {
#pragma clang loop unroll(disable)
                for (u32 spin = 0; spin < SPIN_CAP; spin++) { sr = gld(a.ws, sro); if (!bad(sr, tag)) break; }
            }
            PIN(c[0][0]); PIN(c[1][1]);
            STAMP(2);
            const u32 s0 = __builtin_amdgcn_readlane((u32)sr, 0), s1 = __builtin_amdgcn_readlane((u32)sr, 1);
            LAS float* gu = (LAS float*)(lds + LDS_GU);
            LAS float* gw = gu + (g == 0 ? ch * 128 + 2 * q : 512);
#pragma unroll
            for (int mt = 0; mt < 2; mt++) {
                *(LAS f32x2*)(gw + (mt ^ (f & 1)) * 64) = f32x2{c[mt][0][0], c[mt][1][0]} - bflo(s0) * f32x2{c[mt][0][2], c[mt][1][2]};
                *(LAS f32x2*)(gw + (mt ^ (f & 1)) * 64 + 32) = f32x2{c[mt][0][1], c[mt][1][1]} - bflo(s1) * f32x2{c[mt][0][2], c[mt][1][2]};
            }
            const float rs = __builtin_amdgcn_rcpf(bfhi(lane < 32 ? s0 : s1));
            bar();
            f32x2 gs = {0.0f, 0.0f};
#pragma unroll
            for (int i = 0; i < 4; i++) gs += f32x2{gu[i * 128 + lane], gu[i * 128 + 64 + lane]};
            const u32 gb = pkbf(gs.x * rs, gs.y * rs);
            const float ga = bflo(gb);
            const float hv = rbf(ga * sigm(ga)) * bfhi(gb);
            LAS u16* hb = (LAS u16*)(lds + LDS_H + ch * 128);
            hb[lane] = (u16)pkbf(hv, hv);
            FENCE();
            const u32x4 ah = *(const LAS u32x4*)(hb + (q & 1) * 32 + 8 * g);
            f32x4 y0 = z4, y1 = z4;
#pragma unroll
            for (int u = 0; u < 4; u++) {
                const f32x4 d0 = mfma_bf(ah, fd[u][0], z4), d1 = mfma_bf(ah, fd[u][1], z4);
                y0[0] = g == u ? d0[0] : y0[0]; y0[1] = g == u ? d0[1] : y0[1]; y1[0] = g == u ? d1[0] : y1[0]; y1[1] = g == u ? d1[1] : y1[1];
            }
            STAMP(3);
            asm volatile("" : "+v"(sg[0]), "+v"(sg[1]), "+v"(sg[2]), "+v"(sg[3]));
            const u32 pp = WS_PART + (((32 * ch + 8 * g + (q >> 1)) * NFFN + f) * 4 + 2 * (q & 1)) * 8;
            bst(true, pp, __float_as_uint(y0[0])); bst(true, pp + 8, __float_as_uint(y1[0]));
            bst(true, pp + 128 * NFFN * 32, __float_as_uint(y0[1])); bst(true, pp + 128 * NFFN * 32 + 8, __float_as_uint(y1[1]));
            STAMP(5);
            sweep4(sg, sh, 8);
            u32 t2 = tid;
            PIN(t2);
            stage(sg, xc, LDS_U + 512 + 8 * ((t2 + 64 * jj) & 255), true);
        } else {
            if (!heavy) {
                u32 s1 = ((j - 7) * 4 + ch) * 15, s2 = ((j - 7) * 4 + ch) * 50;
                asm volatile("" : "+s"(s1), "+s"(s2));
                warm3 = ldn<u32>((xcd & 1) ? a.wg : a.wu, __umul24(min(s1 + min(lane, 15), 511u), F_ * 2) + (xcd * 7 + ((xcd & 1) ? 0 : 6)) * 64);
                warm2 = ldn<u32>(a.wd, min(s2 + min(lane, 50), 1791u) * 128 + xcd * 224 * 1024);
            }
#pragma unroll
            for (int i = 0; i < 4; i++) xc[i] = ldg<u32x2>(a.x, xh + 1024 * i);
            if (heavy) {
#pragma unroll
                for (int i = 0; i < 4; i++) xl[i] = ldg<u32x2>(a.x, xo + 1024 * i);
                *(LAS u32x4*)(lds + LDS_GU + (tid & 127) * 16) = bnd;
            }
            second();
            STAMP(12);
#pragma unroll
            for (int i = 0; i < 4; i++) sg[i] = gld(a.ws + 8 * i, sh);
            if (heavy) {
#pragma unroll
                for (int i = 0; i < 4; i++) sl[i] = gld(a.ws + 8 * i, so);
            }
            sweep4(sg, sh, 8);
            asm volatile("" :: "v"(warm0), "v"(warm1), "v"(warm2), "v"(warm3));
            STAMP(4);
            stage(sg, xc, a_st + (heavy ? 2048 : 0), true);
            if (heavy) {
                sweep4(sl, so, 8);
                stage(sl, xl, a_st, false);
                if (blockIdx.x == 128) {
#pragma clang loop unroll(disable)
                    for (u32 spin = 0; spin < SPIN_CAP; spin++)
                        if (!(bad(gld(a.ws + WS_SEEN, (tid & 255) * 8), tag) | bad(gld(a.ws + WS_SEEN + 2048, (tid & 255) * 8), tag) | bad(gld(a.ws + WS_SEEN + 4096, (tid & 255) * 8), tag))) break;
                }
            }
        }
        STAMP(6);
        bar();
        if (blockIdx.x == 128 && ch == 0) __builtin_amdgcn_raw_buffer_store_b32(tag, rw, lane == 0 ? WS_EPOCH : 0xfffffff0u, 0, 16);
        PIN(yb);
        __builtin_amdgcn_s_setprio(0);
        gates();
#pragma unroll
        for (int c = 0; c < 4; c++) {
            const float dk = (c & 1) ? bfhi(dsk[c >> 1]) : bflo(dsk[c >> 1]);
#pragma unroll
            for (int p = 0; p < 2; p++) { xf[c][p] = __builtin_elementwise_fma(zn[c][p] * dk, gt[c][p], xf[c][p]); PIN(xf[c][p]); }
        }
        __builtin_amdgcn_s_setprio(3);
        STAMP(7);
        rawbar();
        STAMP(8);
        f32x2 yv[4][2];
        u32x2 o[4];
#pragma unroll
        for (int c = 0; c < 4; c++) {
            const f32x4 y = *(const LAS f32x4*)(lds + yb + c * Y_SZ) + *(const LAS f32x4*)(lds + yb + (c + 4) * Y_SZ);
            yv[c][0] = y.lo; yv[c][1] = y.hi;
        }
#pragma unroll
        for (int p = 0; p < 2; p++) {
            f32x2 t[4];
#pragma unroll
            for (int c = 0; c < 4; c++) t[c] = __builtin_elementwise_fma(yv[c][p], gt[c][p], xf[c][p]);
            o[2 * p] = u32x2{pkbf(t[0].x, t[1].x), pkbf(t[2].x, t[3].x)};
            o[2 * p + 1] = u32x2{pkbf(t[0].y, t[1].y), pkbf(t[2].y, t[3].y)};
        }
#pragma unroll
        for (int i = 0; i < 4; i++) stg(a.out, xh + 1024 * i, o[i]);
        STAMP(9);
        rawbar();
        const u32x2 rows = *(const LAS u32x2*)(lds + LDS_UPD + 32);
#pragma clang loop unroll(disable)
        for (int t = 0; t < 2; t++) {
            const u32 e = (t ? rows.y : rows.x) - (xh >> 10);
            if (e < 4) {
                const f32x4 up = *(const LAS f32x4*)(lds + LDS_UPD + t * 16);
                float oe[4];
#pragma unroll
                for (int c = 0; c < 4; c++) {
                    auto pick = [&](const f32x2 v0, const f32x2 v1) { const float s0 = (e & 2) ? v1.x : v0.x, s1 = (e & 2) ? v1.y : v0.y; return (e & 1) ? s1 : s0; };
                    const float z = pick(zn[c][0], zn[c][1]), g0 = pick(gt[c][0], gt[c][1]), dk = (c & 1) ? bfhi(dsk[c >> 1]) : bflo(dsk[c >> 1]);
                    const float zz = rbf(rbf(z) + up[c]), g1 = zz * sigm(zz);
                    oe[c] = __builtin_fmaf(z * dk, g1 - g0, __builtin_fmaf(pick(yv[c][0], yv[c][1]), g1, pick(xf[c][0], xf[c][1])));
                }
                stg(a.out, xh + e * 1024, u32x2{pkbf(oe[0], oe[1]), pkbf(oe[2], oe[3])});
            }
        }
    }
    while (__builtin_amdgcn_s_memrealtime() - tstart_ < 1000ull) __builtin_amdgcn_s_sleep(1);
    STAMPS_OUT;
}
#define ROLE_OF(b) (((b) >> 3) >= 16 ? 0 : (((b) >> 3) < 7 ? 2 : 1))
#define NWAVES 12

extern "C" void kernel_launch(void* const* d_in, const int* in_sizes, int n_in, void* d_out, int out_size, void* d_ws,
                              size_t ws_size, hipStream_t stream) {
    Args a;
    a.x = (const u16*)d_in[0]; a.are = (const u16*)d_in[1]; a.aim = (const u16*)d_in[2]; a.bre = (const u16*)d_in[3];
    a.bim = (const u16*)d_in[4]; a.cre = (const u16*)d_in[5]; a.cim = (const u16*)d_in[6]; a.logdt = (const u16*)d_in[7];
    a.dskip = (const u16*)d_in[8]; a.wr = (const u16*)d_in[10]; a.wg = (const u16*)d_in[11]; a.wu = (const u16*)d_in[12];
    a.wd = (const u16*)d_in[13]; a.out = (u16*)d_out; a.ws = (char*)d_ws;
    (void)hipFuncSetAttribute((const void*)s4_mod_fused, hipFuncAttributeMaxDynamicSharedMemorySize, LDS_TOTAL);
    hipLaunchKernelGGL(s4_mod_fused, dim3(256), dim3(768), LDS_TOTAL, stream, a);
}
```

```cpp
#include <hip/hip_runtime.h>
#include <stdint.h>

typedef unsigned short u16;
typedef unsigned int u32;
typedef unsigned long long u64;
typedef float f32x2 __attribute__((ext_vector_type(2)));
typedef float f32x4 __attribute__((ext_vector_type(4)));
typedef u32 u32x2 __attribute__((ext_vector_type(2)));
typedef u32 u32x3 __attribute__((ext_vector_type(3)));
typedef u32 u32x4 __attribute__((ext_vector_type(4)));
typedef __bf16 bf16x2 __attribute__((ext_vector_type(2)));
typedef __bf16 bf16x8 __attribute__((ext_vector_type(8)));
typedef _Float16 f16x2 __attribute__((ext_vector_type(2)));
typedef _Float16 f16x8 __attribute__((ext_vector_type(8)));
#define GAS __attribute__((address_space(1)))
#define LAS __attribute__((address_space(3)))
#define CAS __attribute__((address_space(4)))
typedef GAS u64 gu64;
typedef GAS u32 gu32;
#define RLX_AGENT __ATOMIC_RELAXED, __HIP_MEMORY_SCOPE_AGENT

constexpr int D_ = 512, F_ = 1792;
constexpr int NFFN = 56;
constexpr u32 WS_EPOCH = 0, WS_SEL = 1024, WS_SEEN = 2048, WS_STATS = 16384, WS_CAND = WS_STATS + 8 * 16384, WS_PART = WS_CAND + 8 * 4096;
constexpr u32 WS_END = WS_PART + 2 * 128 * NFFN * 32;
constexpr u32 WS_DBG = 1u << 21;
constexpr int LDS_LN = 0;
constexpr int LDS_H = 1024;
constexpr int LDS_UPD = LDS_H + 512;
constexpr int LDS_JUNK = LDS_UPD + 64;
constexpr int LDS_TBL = 2048;
constexpr int TBL_ROW = 1040, TBL_H = 57 * TBL_ROW;
constexpr int LDS_Y = LDS_TBL + TBL_H, Y_TILE = 1280, Y_SZ = 4 * Y_TILE;
constexpr int LDS_U = LDS_TBL + 2 * TBL_H;
constexpr int U_CH = (256 + 2048) * 2;
constexpr int LDS_GU = LDS_U + 4 * U_CH;
constexpr int LDS_P = LDS_GU + 2048 + 512, P_SZ = 5344;
constexpr int C_OFF = 4416 + 64 + 112, C_SZ = 432;
constexpr int LDS_TOTAL = LDS_P + 4 * P_SZ;
constexpr int LDS_FLAG = LDS_JUNK + 128;

struct LaneTab { u32 v[128][8]; };
constexpr LaneTab make_tab() {
    LaneTab t{};
    for (int h = 0; h < 2; h++)
        for (int l = 0; l < 64; l++) {
            const int q = l & 15, g = l >> 4;
            u32* o = t.v[h * 64 + l];
            for (int d = 0; d < 4; d++) {
                const int k0 = 8 * g + 2 * d - q - 8, k1 = k0 + 1;
                o[d] = ((k0 >= h - 4 && k0 <= 4 - h) ? 0x3f80u : 0u) | ((k1 >= h - 4 && k1 <= 4 - h) ? 0x3f800000u : 0u);
            }
            o[4] = 4 * (h ? 1080 - 8 * q - (g < 2 ? 256 + 128 * g : 256 * g) : 1084 - 128 * (g >> 1) - 8 * q - 4 * (g & 1));
            o[5] = h ? (g < 2 ? 4432 : o[4] - 512)
                     : (l < 16 ? l * 16 : (l < 20 ? 4096 + l * 16 : (l < 28 ? C_OFF + ((l - 20) >> 2) * C_SZ + 256 + (l & 3) * 16 : 4432)));
            o[6] = (g < 2 || l == 32) ? C_OFF + (q & 1) * C_SZ + 4 * (64 - (q & 1) - 32 * g - (q >> 1)) : C_OFF - 4;
            o[7] = 0;
        }
    return t;
}
static __device__ const LaneTab lane_tab = make_tab();
#define GATEWARM 0
#define ROWNT 1
#define NPRE 17
constexpr u32 SPIN_CAP = 1u << 16;
constexpr int CODE_BACK = 0, CODE_LINES = 140;

struct Args {
    const u16 *x, *are, *aim, *bre, *bim, *cre, *cim, *logdt, *dskip, *wr, *wg, *wu, *wd;
    u16* out;
    char* ws;
};

template <typename T> __device__ __forceinline__ T ldg(const void* base, u32 off) { return *(const GAS T*)((const GAS char*)base + off); }
template <typename T> __device__ __forceinline__ T ldn(const void* base, u32 off) { return *(const GAS T*)((const GAS char*)base + off); }
template <typename T> __device__ __forceinline__ void stg(void* base, u32 off, T v) { *(GAS T*)((GAS char*)base + off) = v; }
__device__ __forceinline__ u64 gld(const char* base, u32 off) { return __hip_atomic_load((gu64*)((GAS char*)base + off), RLX_AGENT); }
__device__ __forceinline__ void gst(char* base, u32 off, u32 tag, u32 v) { __hip_atomic_store((gu64*)((GAS char*)base + off), ((u64)tag << 32) | v, RLX_AGENT); }
__device__ __forceinline__ float bflo(u32 v) { return __uint_as_float(v << 16); }
__device__ __forceinline__ float bfhi(u32 v) { return __uint_as_float(v & 0xffff0000u); }
__device__ __forceinline__ u32 pkbf(float a, float b) { f32x2 v = {a, b}; return __builtin_bit_cast(u32, __builtin_convertvector(v, bf16x2)); }
__device__ __forceinline__ u32 pkh(f32x2 v) { return __builtin_bit_cast(u32, __builtin_convertvector(v, f16x2)); }
__device__ __forceinline__ float rbf(float a) { return bflo(pkbf(a, a)); }
__device__ __forceinline__ f32x2 cmul(f32x2 a, f32x2 z, f32x2 zn) {
    return __builtin_elementwise_fma(a.yx, zn, a * z.xx);
}
__device__ __forceinline__ f32x2 csq(f32x2 a) { return cmul(a, a, f32x2{-a.y, a.y}); }
__device__ __forceinline__ void rawbar() { asm volatile("s_barrier" ::: "memory"); }
__device__ __forceinline__ void bar() { asm volatile("s_waitcnt lgkmcnt(0)\n\ts_barrier" ::: "memory"); }
#define PIN(x) asm volatile("" : "+v"(x))
#define FENCE() asm volatile("" ::: "memory")
__device__ __forceinline__ f32x4 mfma_bf(u32x4 a, u32x4 b, f32x4 c) {
    return __builtin_amdgcn_mfma_f32_16x16x32_bf16(__builtin_bit_cast(bf16x8, a), __builtin_bit_cast(bf16x8, b), c, 0, 0, 0);
}
__device__ __forceinline__ f32x4 mfma_h(u32x4 a, u32x4 b, f32x4 c) {
    return __builtin_amdgcn_mfma_f32_16x16x32_f16(__builtin_bit_cast(f16x8, a), __builtin_bit_cast(f16x8, b), c, 0, 0, 0);
}
template <int CTRL> __device__ __forceinline__ float dppf(float old, float src) {
    return __uint_as_float(__builtin_amdgcn_update_dpp(__float_as_uint(old), __float_as_uint(src), CTRL, 0xf, 0xf, false));
}
template <int CTRL, int RM = 0xf> __device__ __forceinline__ u32 dpp0(u32 src) { return __builtin_amdgcn_update_dpp(0u, src, CTRL, RM, 0xf, false); }
__device__ __forceinline__ u32 row_umax(u32 v) {
    v = max(v, dpp0<0x128>(v)); v = max(v, dpp0<0x124>(v)); v = max(v, dpp0<0x122>(v)); v = max(v, dpp0<0x121>(v));
    return v;
}
__device__ __forceinline__ u32 wave_umax(u32 v) {
    v = row_umax(v); v = max(v, dpp0<0x142, 0xa>(v)); v = max(v, dpp0<0x143, 0xc>(v));
    return __builtin_amdgcn_readlane(v, 63);
}
__device__ __forceinline__ float wave_fsum(float f) {
    f += __uint_as_float(dpp0<0x128>(__float_as_uint(f))); f += __uint_as_float(dpp0<0x124>(__float_as_uint(f)));
    f += __uint_as_float(dpp0<0x122>(__float_as_uint(f))); f += __uint_as_float(dpp0<0x121>(__float_as_uint(f)));
    f += __uint_as_float(dpp0<0x142, 0xa>(__float_as_uint(f))); f += __uint_as_float(dpp0<0x143, 0xc>(__float_as_uint(f)));
    return __uint_as_float(__builtin_amdgcn_readlane(__float_as_uint(f), 63));
}
__device__ __forceinline__ float sigm(float z) { return __builtin_amdgcn_rcpf(1.0f + __builtin_amdgcn_exp2f(z * -1.44269504089f)); }
__device__ __forceinline__ u64 bad(u64 granule, u32 tag) { return __builtin_amdgcn_ballot_w64((u32)(granule >> 32) != tag); }

#define STAMP(i)
#define STAMPS_DECL
#define STAMPS_OUT

__global__ void __launch_bounds__(768, 3) s4_mod_fused(Args a) {
    extern __shared__ __attribute__((aligned(16))) char smem[];
    LAS char* const lds = (LAS char*)smem;
    const int tid = threadIdx.x, lane = tid & 63;
    const int w = __builtin_amdgcn_readfirstlane(tid >> 6);
    const int xcd = blockIdx.x & 7, j = blockIdx.x >> 3, jj = j & 15;
    const bool heavy = j >= 16, ffn = j < 7, lnr = j >= 7 && j < 15;
    const int q = lane & 15, g = lane >> 4;
    const int ch = w & 3, h = w >> 2;
    const int G = xcd * 16 + jj;
    const f32x4 z4 = {0, 0, 0, 0};
    STAMPS_DECL;
    STAMP(0);
    u32 tag;
    const __amdgpu_buffer_rsrc_t rw = __builtin_amdgcn_make_buffer_rsrc((void*)a.ws, 0, WS_END, 0x00020000);
    auto bst = [&](const bool on, const u32 off, const u32 v) {
        __builtin_amdgcn_raw_buffer_store_b64(u32x2{v, tag}, rw, on ? off : 0xfffffff0u, 0, 16);
    };

    u32 p_ldt, p_are, p_aim, p_bre, p_bim, p_cre, p_cim, warm0;
    u32x3 adr;
    {
        warm0 = ldg<u32>((const char*)(__builtin_amdgcn_s_getpc() & ~0x7full), min(j * 5 + (tid & 7), CODE_LINES - 1) * 128);
        asm volatile("" :: "s"(a.x), "s"(a.are), "s"(a.aim), "s"(a.bre), "s"(a.bim), "s"(a.cre), "s"(a.cim), "s"(a.logdt), "s"(a.dskip), "s"(a.wr),
                     "s"(a.wg), "s"(a.wu), "s"(a.wd), "s"(a.out), "s"(a.ws) : "memory");
    }
    if (h < 2) {
        const u32 po = ((4 * G + ch) * 128 + 64 * h + lane) * 2;
        p_ldt = ldg<u16>(a.logdt, (4 * G + ch) * 2);
        p_are = ldn<u16>(a.are, po); p_aim = ldn<u16>(a.aim, po); p_bre = ldn<u16>(a.bre, po); p_bim = ldn<u16>(a.bim, po);
        p_cre = ldn<u16>(a.cre, po); p_cim = ldn<u16>(a.cim, po);
        adr = ldg<u32x4>(&lane_tab, (h * 64 + lane) * 32 + 16).xyz;
        *(LAS u32x4*)(lds + LDS_FLAG + (lane < 2 ? 128 + lane * 16 : w * 16)) = u32x4{0, 0, 0, 0};
    }
    int h2 = h;
    asm volatile("" : "+s"(h2) :: "memory");

    if (h2 < 2) {
        bar();
        const u32 tw = LDS_TBL + h * TBL_H + ch * 256, g16 = lane & 48, q16 = q * 16;
        u32 a_tf = __umul24(q, TBL_ROW) + g16 + tw, a_ln = __umul24(g, TBL_ROW) + q16 + tw, a_y = __umul24(g, 320) + q16 + (LDS_Y + w * Y_SZ);
        u32 a_cr = g16 - 4 * q + (LDS_P + ch * P_SZ + C_OFF + h * C_SZ + 60), a_pr = 2 * g16 - 4 * q + (LDS_P + ch * P_SZ + 188 - 128 * h);
        u32 a_u = 2 * q16 + g16 + (LDS_U + ch * U_CH + 464 + 16 * h), a_b = a_u + (16 - 80 * h - (heavy ? 0 : 2048));
        u32 prto = min(lane, NFFN - 1) * 32 + (WS_PART + (h * 128 + G) * NFFN * 32 + ch * 8), selo = (lane & 1) * 8 + (WS_SEL + xcd * 128);
        PIN(a_tf); PIN(a_ln); PIN(a_y); PIN(a_pr); PIN(a_cr); PIN(a_u); PIN(a_b); PIN(prto); PIN(selo);
        __builtin_amdgcn_sched_barrier(0);
        if (h == 0) __builtin_amdgcn_s_setprio(2); else __builtin_amdgcn_s_setprio(1);
        STAMP(11);
        PIN(p_ldt); PIN(p_are); PIN(p_aim); PIN(p_bre); PIN(p_bim); PIN(p_cre); PIN(p_cim);
        STAMP(3);
        const float ld = bflo(p_ldt);
        const float t0 = ld * 1.44269504089f;
        const float te = __builtin_fmaf(ld, 1.92596303e-8f, __builtin_fmaf(ld, 1.44269504089f, -t0));
        const float ex = __builtin_amdgcn_exp2f(t0);
        float dt = rbf(__builtin_fmaf(ex * te, 0.69314718056f, ex));
        const float lr = (dt * 1.44269504089f) * bflo(p_are);
        const float ai = dt * bflo(p_aim);
        const float th = ai * 0.15915494309f;
        const float tl = __builtin_fmaf(ai, 6.4206382e-9f, __builtin_fmaf(ai, 0.15915494309f, -th));
        const float bi = bflo(p_bim);
        const f32x2 W = cmul(f32x2{bflo(p_cre), bflo(p_cim)}, f32x2{bflo(p_bre), bi}, f32x2{-bi, bi}) * dt;
        const float gp = fmaxf(lr, 0.0f);
        const int en = __builtin_amdgcn_frexp_expf(fmaxf(fabsf(W.x), fabsf(W.y))) + (int)(2168.0f * gp);
        const int E = min(max((int)wave_umax((u32)(en + 201)) - 200, -100), 120);
        const float sA = __builtin_amdgcn_ldexpf(__builtin_amdgcn_exp2f(120.0f * gp), 13 - E);
        const float inv = __uint_as_float((u32)(E - 13 + 127) << 23);
        const float t1 = __builtin_amdgcn_fractf(th), e1 = __builtin_amdgcn_exp2f(lr);
        const f32x2 z1 = f32x2{__builtin_amdgcn_cosf(t1), __builtin_amdgcn_sinf(t1)} * e1;
        const float t7 = __builtin_fmaf(128.0f, tl, __builtin_amdgcn_fractf(128.0f * th)), e7 = __builtin_amdgcn_exp2f(128.0f * lr);
        const f32x2 z128 = f32x2{__builtin_amdgcn_cosf(t7), __builtin_amdgcn_sinf(t7)} * e7;
        const f32x2 z2 = csq(z1), zz4 = csq(z2), z8 = csq(zz4);
        f32x2 n1 = {-z1.y, z1.y}, n2 = {-z2.y, z2.y}, n4 = {-zz4.y, zz4.y}, n128 = {-z128.y, z128.y}, c8 = {z8.y, -z8.y};
        f32x2 X = W * sA, B = {__builtin_amdgcn_exp2f(-120.0f * gp), 0.0f};
        PIN(adr);
        const u32 a_e0 = adr[0] + (LDS_P + ch * P_SZ), a_e1 = adr[1] + (LDS_P + ch * P_SZ), a_cw = adr[2] + (LDS_P + ch * P_SZ);
        STAMP(12);
        {
            const u32 m0v = tw + (u32)(size_t)lds;
            asm volatile("s_mov_b32 m0, %0\n\ts_nop 0" :: "s"(m0v) : "memory");
        }
        auto put2 = [&](const int r0, const f32x2 v0, const int r1, const f32x2 v1) {
            asm volatile("ds_write_addtid_b32 %0 offset:%c2\n\tds_write_addtid_b32 %1 offset:%c3" :: "v"(pkh(v0)), "v"(pkh(v1)), "i"(r0 * TBL_ROW), "i"(r1 * TBL_ROW));
        };
        {
            f32x2 c0 = X, X1 = cmul(X, z128, n128), c1 = X1;
            X = cmul(X1, z128, n128);
            f32x2 d = X;
#pragma unroll
            for (int i = 0; i < 8; i++) {
                put2(i, c0, 8 + i, c1);
                put2(41 + i, B, 16 + i, d);
                if (i < 7) { c0 = cmul(c0, z1, n1); c1 = cmul(c1, z1, n1); }
                B = cmul(B, z8, c8);
                if (i == 3) d = X; else if (i < 7) d = cmul(d, z2, n2);
                if (i == 2 || i == 6) X = cmul(X, z128, n128);
            }
#pragma unroll
            for (int i = 0; i < 4; i++) {
                const f32x2 B1 = cmul(B, z8, c8), e = cmul(X, zz4, n4), Xn = cmul(X, z128, n128);
                put2(49 + 2 * i, B, 24 + 2 * i, X);
                if (i < 3) B = cmul(B1, z8, c8);
                put2(50 + 2 * i, B1, 25 + 2 * i, e);
                X = Xn;
            }
        }
        FENCE();
        STAMP(2);
        f32x4 ka, kb;
        float k255, k1024;
        {
            u32x4 fa[4][4];
#pragma unroll
            for (int s = 0; s < 4; s++) {
                fa[s][3] = *(const LAS u32x4*)(lds + a_tf + 41 * TBL_ROW + s * 64);
#pragma unroll
                for (int t = 0; t < 2; t++) fa[s][t] = *(const LAS u32x4*)(lds + a_tf + 16 * t * TBL_ROW + s * 64);
            }
            FENCE();
            const f32x2 X9 = cmul(X, z128, n128);
            put2(32, X, 33, X9);
            if (heavy) {
                const f32x2 z256 = csq(z128), n256 = {-z256.y, z256.y};
                f32x2 e = X, o = X9;
#pragma unroll
                for (int i = 0; i < 3; i++) { e = cmul(e, z256, n256); o = cmul(o, z256, n256); put2(34 + 2 * i, e, 35 + 2 * i, o); }
                e = cmul(e, z256, n256);
                put2(40, e, 40, e);
            }
            __builtin_amdgcn_s_setprio(0);
            FENCE();
#pragma unroll
            for (int s = 0; s < 4; s++) fa[s][2] = *(const LAS u32x4*)(lds + a_tf + 32 * TBL_ROW + s * 64);
            __builtin_amdgcn_sched_barrier(0);
            f32x4 k0, k1, k2;
#pragma unroll
            for (int s = 0; s < 4; s++) { k0 = mfma_h(fa[s][0], fa[s][3], s ? k0 : z4); k1 = mfma_h(fa[s][1], fa[s][3], s ? k1 : z4); }
#pragma unroll
            for (int s = 0; s < 4; s++) k2 = mfma_h(fa[s][2], fa[s][3], s ? k2 : z4);
            FENCE();
            const int dlt = h ? -TBL_H : TBL_H;
            LAS char* fo = lds + LDS_FLAG + w * 16;
            const LAS char* fi = lds + LDS_FLAG + (w ^ 4) * 16;
            if (h) {
                *(LAS f32x4*)(lds + a_ln) = k0 * inv; *(LAS f32x4*)(lds + a_ln + 4 * TBL_ROW) = k2 * inv;
                asm volatile("s_waitcnt lgkmcnt(0)" ::: "memory");
                *(volatile LAS u32*)fo = 1;
                STAMP(13);
                u32x4 fl; f32x4 o;
#pragma clang loop unroll(disable)
                for (u32 spin = 0; spin < SPIN_CAP; spin++) {
                    fl = *(volatile LAS u32x4*)fi; o = *(volatile LAS f32x4*)(lds + a_ln + dlt);
                    if (__builtin_amdgcn_readfirstlane(fl[0])) break;
                    __builtin_amdgcn_s_sleep(2);
                }
                ka = k1 * inv + o;
                k255 = __uint_as_float(__builtin_amdgcn_readlane(__float_as_uint(k0[3]), 63)) * inv + __uint_as_float(fl[1]);
                k1024 = __uint_as_float(__builtin_amdgcn_readlane(__float_as_uint(k2[0]), 0)) * inv + __uint_as_float(fl[2]);
            } else {
                *(LAS f32x4*)(lds + a_ln) = k1 * inv;
                const float s0 = __uint_as_float(__builtin_amdgcn_readlane(__float_as_uint(k0[3]), 63)) * inv;
                const float s1 = __uint_as_float(__builtin_amdgcn_readlane(__float_as_uint(k2[0]), 0)) * inv;
                asm volatile("s_waitcnt lgkmcnt(0)" ::: "memory");
                *(volatile LAS u32x4*)fo = u32x4{1, __float_as_uint(s0), __float_as_uint(s1), 0};
                STAMP(13);
                u32 fl; f32x4 o0, o1;
#pragma clang loop unroll(disable)
                for (u32 spin = 0; spin < SPIN_CAP; spin++) {
                    fl = *(volatile LAS u32*)fi; o0 = *(volatile LAS f32x4*)(lds + a_ln + dlt); o1 = *(volatile LAS f32x4*)(lds + a_ln + dlt + 4 * TBL_ROW);
                    if (__builtin_amdgcn_readfirstlane(fl)) break;
                    __builtin_amdgcn_s_sleep(2);
                }
                ka = k0 * inv + o0; kb = k2 * inv + o1;
                k1024 = __uint_as_float(__builtin_amdgcn_readlane(__float_as_uint(kb[0]), 0));
            }
        }
        STAMP(1);
        if (h == 0) {
            const f32x4 v = ka;
            auto sw = __builtin_amdgcn_permlane16_swap(__float_as_uint(v[3]), __float_as_uint(v[3]), false, false);
            const float pd = dppf<0x138>(0.0f, __uint_as_float(sw[1]));
            const float pv = (g & 1) ? __uint_as_float(sw[0]) : pd;
            *(LAS u32x4*)(lds + a_e0) = u32x4{pkbf(v[3], v[2]), pkbf(v[2], v[1]), pkbf(v[1], v[0]), pkbf(v[0], pv)};
            const u32 kk = pkbf(k1024, k1024);
            const u32 f7 = lane == 15 ? kk : 0u, f6 = lane == 14 ? kk : 0u;
            *(LAS u32x4*)(lds + a_e1) = u32x4{f7, f7, f7 | (f6 & 0xffff0000u), f7 | f6};
            const f32x4 u = {lane == 0 ? 0.0f : kb[0], kb[1], kb[2], kb[3]};
            const bool e = q >= 14;
            LAS u32* cw = (LAS u32*)(lds + a_cw);
            cw[0] = pkbf(u[0], dppf<0x138>(0.0f, dppf<0x138>(0.0f, e ? u[3] : u[0])));
#pragma unroll
            for (int i = 1; i < 4; i++) cw[-8 * i] = pkbf(u[i], dppf<0x122>(0.0f, e ? u[i - 1] : u[i]));
        } else {
            const f32x4 v = ka;
            const bool s2 = g < 2;
            const float nw = dppf<0x130>(v[0], v[0]);
            const float nr = dppf<0x12f>(v[0], q == 0 ? v[2] : v[0]);
            const float nA = s2 ? nw : nr;
            const float nB = dppf<0x130>(k1024, q == 0 ? v[0] : v[2]);
            const float x2 = s2 ? v[1] : v[0], x3 = s2 ? v[2] : v[1], x6 = s2 ? v[3] : nA;
            const u32 aa = pkbf(v[0], v[0]), cc = pkbf(x3, x3), a2 = pkbf(v[2], v[2]), b2 = pkbf(v[3], v[3]);
            *(LAS u32x4*)(lds + a_e0) = u32x4{pkbf(nA, x6), pkbf(x6, x3), cc, cc};
            *(LAS u32x4*)(lds + a_e0 + 16) = u32x4{pkbf(x3, x2), pkbf(x2, v[0]), aa, pkbf(v[0], lane == 0 ? k255 : v[0])};
            *(LAS u32x4*)(lds + a_e1) = u32x4{pkbf(nB, nB), pkbf(nB, v[3]), b2, b2};
            *(LAS u32x4*)(lds + a_e1 + 16) = u32x4{pkbf(v[3], v[2]), a2, a2, a2};
        }
        STAMP(4);
        u32x4 A[17];
        const LAS u32* pr = (const LAS u32*)(lds + a_pr);
        auto lda = [&](const int m) { A[m] = u32x4{pr[64 * (16 - m)], pr[64 * (16 - m) + 2], pr[64 * (16 - m) + 4], pr[64 * (16 - m) + 6]}; };
        {
            asm volatile("s_waitcnt lgkmcnt(0)" ::: "memory");
            *(volatile LAS u32*)(lds + LDS_FLAG + w * 16 + 12) = 2;
#pragma clang loop unroll(disable)
            for (u32 spin = 0; spin < SPIN_CAP; spin++)
                if (__builtin_amdgcn_readfirstlane(*(volatile LAS u32*)(lds + LDS_FLAG + (w ^ 4) * 16 + 12)) == 2) break;
            FENCE();
        }
#pragma unroll
        for (int m = 0; m < NPRE; m++) lda(m);
        asm volatile("" :: "v"(warm0) : "memory");
        STAMP(5);
        if (ffn) rawbar();
        rawbar();
        STAMP(7);

        f32x4 acc[4];
        u64 selv, prtv;
        {
            auto ldb = [&](const int k) { return *(const LAS u32x4*)(lds + a_b + 128 * (28 - k)); };
            u32x4 Bf[29];
            auto mm = [&](const int k, const int first) {
#pragma unroll
                for (int i = 3; i >= 0; i--) {
                    const int mp = 4 * i - 12 + k;
                    if (mp >= 0 && mp < 17) acc[i] = mfma_bf(A[mp], Bf[k], (first >> i) & 1 ? z4 : acc[i]);
                }
            };
            if (heavy) {
                const LAS char* uw = lds + a_u;
                const LAS u32* cr = (const LAS u32*)(lds + a_cr);
                u32x4 Wd[4], KT[4], band, S0, S1;
                f32x4 dw[4];
#pragma unroll
                for (int k = 0; k < 3; k++) { Bf[16 + k] = ldb(16 + k); Bf[k] = ldb(k); }
                __builtin_amdgcn_sched_barrier(0);
                STAMP(6);
#pragma unroll
                for (int k = 0; k < 16; k++) {
                    if (k + 3 < 13) Bf[19 + k] = ldb(19 + k);
                    if (k + 3 < 16) Bf[k + 3] = ldb(k + 3);
                    if (k == 3) band = *(const LAS u32x4*)(lds + LDS_GU + (tid & 63) * 16 + h * 1024);
                    if (k >= 4 && k < 8) Wd[k - 4] = *(const LAS u32x4*)(uw + 512 * (k - 4));
                    if (k >= 10 && k < 13) KT[13 - k] = u32x4{cr[16 * (k - 10)], cr[16 * (k - 10) + 2], cr[16 * (k - 10) + 16], cr[16 * (k - 10) + 18]};
                    if (k == 13) KT[0] = u32x4{cr[48], cr[50], 0u, 0u};
                    FENCE();
                    if (k < 13) mm(k + 16, k == 0 ? 15 : 0);
                    if (k >= 8 && k < 12) dw[k - 8] = mfma_bf(Wd[k - 8], band, z4);
                    mm(k, 0);
                    if (k == 11) S0 = u32x4{pkbf(dw[0][0], dw[0][1]), pkbf(dw[0][2], dw[0][3]), pkbf(dw[1][0], dw[1][1]), pkbf(dw[1][2], dw[1][3])};
                    if (k == 13) S1 = u32x4{pkbf(dw[2][0], dw[2][1]), pkbf(dw[2][2], dw[2][3]), pkbf(dw[3][0], dw[3][1]), pkbf(dw[3][2], dw[3][3])};
                    __builtin_amdgcn_sched_barrier(0);
                }
#pragma unroll
                for (int i = 3; i >= 0; i--) acc[i] = mfma_bf(S0, KT[i], acc[i]);
                acc[3] = mfma_bf(S1, KT[1], acc[3]); acc[2] = mfma_bf(S1, KT[0], acc[2]);
            } else {
#pragma unroll
                for (int k = 0; k < 5; k++) Bf[15 - k] = ldb(15 - k);
                __builtin_amdgcn_sched_barrier(0);
#pragma unroll
                for (int k = 15; k >= 0; k--) {
                    if (k >= 5) Bf[k - 5] = ldb(k - 5);
                    mm(k, k == 15 ? 15 : 0);
                    __builtin_amdgcn_sched_barrier(0);
                }
            }
        }
        STAMP(9);
        selv = gld(a.ws, selo); prtv = gld(a.ws, prto);
        tag = __builtin_amdgcn_readfirstlane(*(LAS u32*)(lds + LDS_UPD + 48));
#pragma unroll
        for (int i = 3; i >= 0; i--) *(LAS f32x4*)(lds + a_y + i * Y_TILE) = acc[i];
        STAMP(8);
        bar();
        if (bad(selv, tag)) {
#pragma clang loop unroll(disable)
            for (u32 spin = 0; spin < SPIN_CAP; spin++) { selv = gld(a.ws, selo); if (!bad(selv, tag)) break; __builtin_amdgcn_s_sleep(2); }
        }
        const u32 sel = h ? __builtin_amdgcn_readlane((u32)selv, 1) : __builtin_amdgcn_readlane((u32)selv, 0);
        const bool own = ((sel & 2047) >= 1024) == heavy;
        if (own) {
            if (bad(prtv, tag)) {
#pragma clang loop unroll(disable)
                for (u32 spin = 0; spin < SPIN_CAP; spin++) { prtv = gld(a.ws, prto); if (!bad(prtv, tag)) break; __builtin_amdgcn_s_sleep(1); }
            }
            const float y = wave_fsum(lane < NFFN ? __uint_as_float((u32)prtv) : 0.0f);
            *(LAS float*)(lds + LDS_UPD + w * 4) = rbf(rbf(y) * bfhi(sel));
        }
        *(LAS u32*)(lds + LDS_UPD + 32 + h * 4) = own ? sel & 2047u : 0x7fffffffu;
        bar();
    } else {
        __builtin_amdgcn_s_setprio(3);
        const int lt = (tid + 64 * jj) & 255;
        const u32 xo = lt * 4096 + G * 8;
        u32 xh = xo + (heavy ? 1024 * 1024 : 0);
        const u32 so = WS_STATS + xcd * 16384 + lt * 32, sh = so + (heavy ? 8192 : 0);
        u32 warm1 = 0, warm2 = 0, warm3 = 0;
        u32x2 xc[4], xl[4];
        u64 sg[4], sl[4], cd[4], ce[4];
        f32x2 xf[4][2], zn[4][2], gt[4][2];
        u32 epoch;
        u32x2 dsk, dskr;
        asm volatile("s_load_dword %0, %2, 0x0\n\ts_load_dwordx2 %1, %3, %4" : "=&s"(epoch), "=&s"(dskr) : "s"(a.ws), "s"(a.dskip), "s"(G * 8));
        auto first = [&]() {
            asm volatile("s_waitcnt lgkmcnt(0)\n\ts_add_u32 %0, %2, 1\n\ts_mov_b64 %1, %3" : "=&s"(tag), "=&s"(dsk) : "s"(epoch), "s"(dskr) : "scc");
        };
        u32x4 bnd;
        if (heavy) bnd = ldg<u32x4>(&lane_tab, (tid & 127) * 32);
        if (lnr) {
            const int lnu = (blockIdx.x - 56) * 4 + ch;
            u32x4 xa[8], cb[8];
#pragma unroll
            for (int s = 0; s < 8; s++) xa[s] = (ROWNT ? ldn<u32x4> : ldg<u32x4>)(a.x, (lnu * 8 + (q >> 1)) * 1024 + (q & 1) * 64 + g * 16 + 128 * s);
            rawbar();
#pragma unroll
            for (int s = 0; s < 8; s++) cb[s] = ldg<u32x4>(a.wr, (q & 2) * 32 + g * 16 + 128 * s);
            const u32x4 ones = {0x3f803f80u, 0x3f803f80u, 0x3f803f80u, 0x3f803f80u};
            const int rr = 2 * g + ((q >> 1) & 1);
            u32 o_st = WS_STATS + ((q & 1) + 2 * (q >> 2)) * 16384 + (lnu * 8 + rr) * 8, o_id = 0xffffu - (lnu * 8 + rr);
            u32 o_cd = (WS_CAND + (lane >> 1) * 4096 + lnu * 16 + (lane & 1) * 8) | (u32)(((15 - lane) >> 31) << 20);
            PIN(o_st); PIN(o_id); PIN(o_cd);
            first();
            f32x4 d0 = z4, d1 = z4, d2 = z4, d3 = z4;
#pragma unroll
            for (int s = 0; s < 8; s++) { d2 = mfma_bf(xa[s], xa[s], d2); d0 = mfma_bf(xa[s], ones, d0); }
            asm volatile("" : "+v"(d0), "+v"(d2));
            __builtin_amdgcn_sched_barrier(0);
#pragma unroll
            for (int s = 0; s < 8; s++) { d1 = mfma_bf(xa[s], cb[s], d1); d3 = mfma_bf(ones, cb[s], d3); }
            STAMP(13);
            const bool hi = q & 2;
            const float da = (q & 1) ? d2[1] : d2[0], db = (q & 1) ? d2[3] : d2[2];
            float sq = (q >> 2) == g ? (hi ? db : da) : 0.0f;
            sq += __uint_as_float(dpp0<0xb1>(__float_as_uint(sq)));
            sq += __uint_as_float(dpp0<0x124>(__float_as_uint(sq)));
            sq += __uint_as_float(dpp0<0x128>(__float_as_uint(sq)));
            const float mean = (hi ? d0[2] + d0[3] : d0[0] + d0[1]) * (1.0f / D_);
            const float var = fmaxf(sq * (1.0f / D_) - mean * mean, 0.0f);
            const float sd = rbf(__builtin_amdgcn_sqrtf(rbf(rbf(var) + 1e-5f)));
            const u32 ms = pkbf(mean, sd);
            bst(true, o_st, ms);
            const float dot = (hi ? d1[3] : d1[0]) + __uint_as_float(dpp0<0x4e>(__float_as_uint(hi ? d1[1] : d1[2])));
            const float sw = d3[0] + __uint_as_float(dpp0<0x4e>(__float_as_uint(d3[0])));
            const float r = (dot - bflo(ms) * sw) * __builtin_amdgcn_rcpf(sd);
            const u32 rb = pkbf(r, r);
            const u32 key = ((rb ^ (((int)rb >> 31) | 0x80000000u)) & 0xffff0000u) | o_id;
            const u32 ko = dpp0<0x4e>(key);
            auto x1 = __builtin_amdgcn_permlane16_swap(max(key, ko), max(key, ko), false, false);
            auto x2 = __builtin_amdgcn_permlane16_swap(min(key, ko), min(key, ko), false, false);
            const u32 a1 = max(x1[0], x1[1]), a2 = max(min(x1[0], x1[1]), max(x2[0], x2[1]));
            auto y1 = __builtin_amdgcn_permlane32_swap(a1, a1, false, false);
            auto y2 = __builtin_amdgcn_permlane32_swap(a2, a2, false, false);
            bst(true, o_cd, (lane & 1) ? max(min(y1[0], y1[1]), max(y2[0], y2[1])) : max(y1[0], y1[1]));
            STAMP(1);
            FENCE();
            *(volatile LAS u32*)(lds + LDS_FLAG + 128 + ch * 4) = 1;
#pragma clang loop unroll(disable)
            for (u32 spin = 0; spin < SPIN_CAP; spin++) {
                const u32x4 f0 = *(volatile LAS u32x4*)(lds + LDS_FLAG + 128);
                if (__builtin_amdgcn_readfirstlane(f0[0] & f0[1] & f0[2] & f0[3])) break;
            }
            FENCE();
        } else {
            warm1 = ldn<u32>(a.x, heavy ? (jj * 128 + ch * 32 + (lane & 31)) * 1024 + xcd * 128 : 0);
            rawbar();
        }
        auto second = [&]() {
            if (!lnr) { first(); bst(lane == 0, WS_SEEN + ((blockIdx.x < 56 ? blockIdx.x : blockIdx.x - 64) * 4 + ch) * 8, 0); }
            *(LAS u32*)(lds + LDS_UPD + 48) = tag;
            *(LAS u32x2*)(lds + LDS_U + ch * U_CH + lane * 8) = u32x2{0, 0};
        };
        u32 yb = LDS_Y + (lt >> 6) * Y_TILE + (lt & 3) * 320 + ((lt >> 2) & 15) * 16;
        const u32 a_st = LDS_U + 512 + 8 * lt;
        auto tags4 = [&](const u64 (&v)[4]) { return (bad(v[0], tag) | bad(v[1], tag) | bad(v[2], tag) | bad(v[3], tag)) == 0; };
        auto sweep4 = [&](u64 (&v)[4], const u32 off, const u32 stride) {
            if (!tags4(v)) {
#pragma clang loop unroll(disable)
                for (u32 spin = 0; spin < SPIN_CAP; spin++) {
#pragma unroll
                    for (int i = 0; i < 4; i++) v[i] = gld(a.ws + stride * i, off);
                    if (tags4(v)) break;
                    __builtin_amdgcn_s_sleep(4);
                }
            }
        };
        auto stage = [&](const u64 (&st)[4], const u32x2 (&r)[4], const u32 dst, const bool keep) {
            f32x2 rs[2], nb[2];
#pragma unroll
            for (int p = 0; p < 2; p++) {
                rs[p] = f32x2{__builtin_amdgcn_rcpf(bfhi((u32)st[2 * p])), __builtin_amdgcn_rcpf(bfhi((u32)st[2 * p + 1]))};
                nb[p] = f32x2{bflo((u32)st[2 * p]), bflo((u32)st[2 * p + 1])} * rs[p];
            }
#pragma unroll
            for (int c = 0; c < 4; c++) {
                u32x2 o;
#pragma unroll
                for (int p = 0; p < 2; p++) {
                    const u32 w0 = r[2 * p][c >> 1], w1 = r[2 * p + 1][c >> 1];
                    const f32x2 xv = (c & 1) ? f32x2{bfhi(w0), bfhi(w1)} : f32x2{bflo(w0), bflo(w1)};
                    const f32x2 z = __builtin_elementwise_fma(xv, rs[p], -nb[p]);
                    if (keep) { xf[c][p] = xv; zn[c][p] = z; }
                    o[p] = pkbf(z.x, z.y);
                }
                *(LAS u32x2*)(lds + dst + c * U_CH) = o;
            }
        };
        auto gates = [&]() {
#pragma unroll
            for (int c = 0; c < 4; c++)
#pragma unroll
                for (int p = 0; p < 2; p++) {
                    const f32x2 e = zn[c][p] * -1.44269504089f;
                    const f32x2 d = f32x2{__builtin_amdgcn_exp2f(e.x), __builtin_amdgcn_exp2f(e.y)} + 1.0f;
                    gt[c][p] = zn[c][p] * f32x2{__builtin_amdgcn_rcpf(d.x), __builtin_amdgcn_rcpf(d.y)};
                    PIN(gt[c][p]);
                }
        };
        if (ffn) {
            const int f = xcd * 7 + jj;
            u32 wgu[2][4][8], wdn[4][8];
            const __amdgpu_buffer_rsrc_t rg = __builtin_amdgcn_make_buffer_rsrc((void*)((f & 1) ? a.wu : a.wg), 0, D_ * F_ * 2, 0x00020000);
            const __amdgpu_buffer_rsrc_t ru = __builtin_amdgcn_make_buffer_rsrc((void*)((f & 1) ? a.wg : a.wu), 0, D_ * F_ * 2, 0x00020000);
            const __amdgpu_buffer_rsrc_t rd = __builtin_amdgcn_make_buffer_rsrc((void*)a.wd, 0, D_ * F_ * 2, 0x00020000);
            const u32 og = ((128 * ch + 8 * g) * F_ + 32 * f + 2 * q) * 2, od = ((32 * f + 8 * g) * D_ + 128 * ch + 2 * q) * 2;
#pragma unroll
            for (int m = 0; m < 2; m++)
#pragma unroll
                for (int s = 0; s < 4; s++)
#pragma unroll
                    for (int i = 0; i < 8; i++) wgu[m][s][i] = __builtin_amdgcn_raw_buffer_load_b32(m ? ru : rg, og, (32 * s + i) * F_ * 2, 0);
#pragma unroll
            for (int i = 0; i < 8; i++)
#pragma unroll
                for (int u = 0; u < 4; u++) wdn[u][i] = __builtin_amdgcn_raw_buffer_load_b32(rd, od + ((i & 3) * D_ * 2 + u * 64), (i & 4) * D_ * 2, 0);
            second();
            STAMP(12);
            const u32 co = WS_CAND + xcd * 4096 + lane * 8;
#pragma unroll
            for (int i = 0; i < 4; i++) { cd[i] = gld(a.ws + 512 * i, co); ce[i] = gld(a.ws + 512 * i + 2048, co); }
            u32x4 fg[2][4][2], fd[4][2];
#pragma unroll
            for (int m = 0; m < 3; m++)
#pragma unroll
                for (int s = 0; s < 4; s++)
#pragma unroll
                    for (int p = 0; p < 2; p++) {
                        u32x4 o;
#pragma unroll
                        for (int d = 0; d < 4; d++)
                            o[d] = __builtin_amdgcn_perm(m < 2 ? wgu[m & 1][s][2 * d + 1] : wdn[s][2 * d + 1], m < 2 ? wgu[m & 1][s][2 * d] : wdn[s][2 * d], p ? 0x07060302u : 0x05040100u);
                        PIN(o);
                        if (m < 2) fg[m & 1][s][p] = o; else fd[s][p] = o;
                    }
            u32x4 an[4];
            const u32 o1 = q < 2 ? 0u : 0x3f803f80u;
            const __amdgpu_buffer_rsrc_t rx = __builtin_amdgcn_make_buffer_rsrc((void*)a.x, 0, 2048 * 1024, 0x00020000);
            asm volatile("" :: "v"(warm0), "v"(warm1));
            sweep4(cd, co, 512);
            sweep4(ce, co + 2048, 512);
            STAMP(4);
            u32 ka = max((u32)cd[0], (u32)ce[0]), kb = min((u32)cd[0], (u32)ce[0]);
#pragma unroll
            for (int i = 1; i < 4; i++) {
                const u32 ha = max((u32)cd[i], (u32)ce[i]), hb = min((u32)cd[i], (u32)ce[i]);
                kb = max(min(ka, ha), max(kb, hb)); ka = max(ka, ha);
            }
            const u32 k1 = wave_umax(ka), k2 = wave_umax(ka == k1 ? kb : ka);
            const u32 kk = (lane & 1) ? k2 : k1;
            const u32 row = ~kk & 2047u;
            const u32 xr = q < 2 ? row * 1024 + ch * 256 + g * 16 : 0xfffff000u;
#pragma unroll
            for (int s = 0; s < 4; s++) an[s] = __builtin_amdgcn_raw_buffer_load_b128(rx, xr + 64 * s, 0, 0);
            const u32 sro = WS_STATS + xcd * 16384 + row * 8;
            u64 sr = gld(a.ws, sro);
            STAMP(11);
            if (jj == 0 && ch == 0) {
                const float sv = sigm(__uint_as_float((kk ^ ((~(int)kk >> 31) | 0x80000000u)) & 0xffff0000u));
                bst(lane < 2, WS_SEL + xcd * 128 + lane * 8, row | (pkbf(sv, sv) << 16));
            }
#pragma unroll
            for (int i = 0; i < 4; i++) xc[i] = ldg<u32x2>(a.x, xh + 1024 * i);
#pragma unroll
            for (int i = 0; i < 4; i++) sg[i] = gld(a.ws + 8 * i, sh);
            f32x4 c[2][2];
#pragma unroll
            for (int s = 0; s < 4; s++)
#pragma unroll
                for (int mt = 0; mt < 2; mt++)
#pragma unroll
                    for (int p = 0; p < 2; p++) c[mt][p] = mfma_bf(an[s] | o1, fg[mt][s][p], s ? c[mt][p] : z4);
            if (bad(sr, tag)) {
#pragma clang loop unroll(disable)
                for (u32 spin = 0; spin < SPIN_CAP; spin++) { sr = gld(a.ws, sro); if (!bad(sr, tag)) break; }
            }
            PIN(c[0][0]); PIN(c[1][1]);
            STAMP(2);
            const u32 s0 = __builtin_amdgcn_readlane((u32)sr, 0), s1 = __builtin_amdgcn_readlane((u32)sr, 1);
            LAS float* gu = (LAS float*)(lds + LDS_GU);
            LAS float* gw = gu + (g == 0 ? ch * 128 + 2 * q : 512);
#pragma unroll
            for (int mt = 0; mt < 2; mt++) {
                *(LAS f32x2*)(gw + (mt ^ (f & 1)) * 64) = f32x2{c[mt][0][0], c[mt][1][0]} - bflo(s0) * f32x2{c[mt][0][2], c[mt][1][2]};
                *(LAS f32x2*)(gw + (mt ^ (f & 1)) * 64 + 32) = f32x2{c[mt][0][1], c[mt][1][1]} - bflo(s1) * f32x2{c[mt][0][2], c[mt][1][2]};
            }
            const float rs = __builtin_amdgcn_rcpf(bfhi(lane < 32 ? s0 : s1));
            bar();
            f32x2 gs = {0.0f, 0.0f};
#pragma unroll
            for (int i = 0; i < 4; i++) gs += f32x2{gu[i * 128 + lane], gu[i * 128 + 64 + lane]};
            const u32 gb = pkbf(gs.x * rs, gs.y * rs);
            const float ga = bflo(gb);
            const float hv = rbf(ga * sigm(ga)) * bfhi(gb);
            LAS u16* hb = (LAS u16*)(lds + LDS_H + ch * 128);
            hb[lane] = (u16)pkbf(hv, hv);
            FENCE();
            const u32x4 ah = *(const LAS u32x4*)(hb + (q & 1) * 32 + 8 * g);
            f32x4 y0 = z4, y1 = z4;
#pragma unroll
            for (int u = 0; u < 4; u++) {
                const f32x4 d0 = mfma_bf(ah, fd[u][0], z4), d1 = mfma_bf(ah, fd[u][1], z4);
                y0[0] = g == u ? d0[0] : y0[0]; y0[1] = g == u ? d0[1] : y0[1]; y1[0] = g == u ? d1[0] : y1[0]; y1[1] = g == u ? d1[1] : y1[1];
            }
            STAMP(3);
            asm volatile("" : "+v"(sg[0]), "+v"(sg[1]), "+v"(sg[2]), "+v"(sg[3]));
            const u32 pp = WS_PART + (((32 * ch + 8 * g + (q >> 1)) * NFFN + f) * 4 + 2 * (q & 1)) * 8;
            bst(true, pp, __float_as_uint(y0[0])); bst(true, pp + 8, __float_as_uint(y1[0]));
            bst(true, pp + 128 * NFFN * 32, __float_as_uint(y0[1])); bst(true, pp + 128 * NFFN * 32 + 8, __float_as_uint(y1[1]));
            STAMP(5);
            sweep4(sg, sh, 8);
            u32 t2 = tid;
            PIN(t2);
            stage(sg, xc, LDS_U + 512 + 8 * ((t2 + 64 * jj) & 255), true);
        } else {
            if (!heavy) {
                u32 s1 = ((j - 7) * 4 + ch) * 15, s2 = ((j - 7) * 4 + ch) * 50;
                asm volatile("" : "+s"(s1), "+s"(s2));
                warm3 = ldn<u32>((xcd & 1) ? a.wg : a.wu, __umul24(min(s1 + min(lane, 15), 511u), F_ * 2) + (xcd * 7 + ((xcd & 1) ? 0 : 6)) * 64);
                warm2 = ldn<u32>(a.wd, min(s2 + min(lane, 50), 1791u) * 128 + xcd * 224 * 1024);
            }
#pragma unroll
            for (int i = 0; i < 4; i++) xc[i] = ldg<u32x2>(a.x, xh + 1024 * i);
            if (heavy) {
#pragma unroll
                for (int i = 0; i < 4; i++) xl[i] = ldg<u32x2>(a.x, xo + 1024 * i);
                *(LAS u32x4*)(lds + LDS_GU + (tid & 127) * 16) = bnd;
            }
            second();
            STAMP(12);
#pragma unroll
            for (int i = 0; i < 4; i++) sg[i] = gld(a.ws + 8 * i, sh);
            if (heavy) {
#pragma unroll
                for (int i = 0; i < 4; i++) sl[i] = gld(a.ws + 8 * i, so);
            }
            sweep4(sg, sh, 8);
            asm volatile("" :: "v"(warm0), "v"(warm1), "v"(warm2), "v"(warm3));
            STAMP(4);
            stage(sg, xc, a_st + (heavy ? 2048 : 0), true);
            if (heavy) {
                sweep4(sl, so, 8);
                stage(sl, xl, a_st, false);
                if (blockIdx.x == 128) {
#pragma clang loop unroll(disable)
                    for (u32 spin = 0; spin < SPIN_CAP; spin++)
                        if (!(bad(gld(a.ws + WS_SEEN, (tid & 255) * 8), tag) | bad(gld(a.ws + WS_SEEN + 2048, (tid & 255) * 8), tag) | bad(gld(a.ws + WS_SEEN + 4096, (tid & 255) * 8), tag))) break;
                }
            }
        }
        STAMP(6);
        bar();
        if (blockIdx.x == 128 && ch == 0) __builtin_amdgcn_raw_buffer_store_b32(tag, rw, lane == 0 ? WS_EPOCH : 0xfffffff0u, 0, 16);
        PIN(yb);
        __builtin_amdgcn_s_setprio(0);
        gates();
#pragma unroll
        for (int c = 0; c < 4; c++) {
            const float dk = (c & 1) ? bfhi(dsk[c >> 1]) : bflo(dsk[c >> 1]);
#pragma unroll
            for (int p = 0; p < 2; p++) { xf[c][p] = __builtin_elementwise_fma(zn[c][p] * dk, gt[c][p], xf[c][p]); PIN(xf[c][p]); }
        }
        __builtin_amdgcn_s_setprio(3);
        STAMP(7);
        rawbar();
        STAMP(8);
        f32x2 yv[4][2];
        u32x2 o[4];
#pragma unroll
        for (int c = 0; c < 4; c++) {
            const f32x4 y = *(const LAS f32x4*)(lds + yb + c * Y_SZ) + *(const LAS f32x4*)(lds + yb + (c + 4) * Y_SZ);
            yv[c][0] = y.lo; yv[c][1] = y.hi;
        }
#pragma unroll
        for (int p = 0; p < 2; p++) {
            f32x2 t[4];
#pragma unroll
            for (int c = 0; c < 4; c++) t[c] = __builtin_elementwise_fma(yv[c][p], gt[c][p], xf[c][p]);
            o[2 * p] = u32x2{pkbf(t[0].x, t[1].x), pkbf(t[2].x, t[3].x)};
            o[2 * p + 1] = u32x2{pkbf(t[0].y, t[1].y), pkbf(t[2].y, t[3].y)};
        }
#pragma unroll
        for (int i = 0; i < 4; i++) stg(a.out, xh + 1024 * i, o[i]);
        STAMP(9);
        rawbar();
        const u32x2 rows = *(const LAS u32x2*)(lds + LDS_UPD + 32);
#pragma clang loop unroll(disable)
        for (int t = 0; t < 2; t++) {
            const u32 e = (t ? rows.y : rows.x) - (xh >> 10);
            if (e < 4) {
                const f32x4 up = *(const LAS f32x4*)(lds + LDS_UPD + t * 16);
                float oe[4];
#pragma unroll
                for (int c = 0; c < 4; c++) {
                    auto pick = [&](const f32x2 v0, const f32x2 v1) { const float s0 = (e & 2) ? v1.x : v0.x, s1 = (e & 2) ? v1.y : v0.y; return (e & 1) ? s1 : s0; };
                    const float z = pick(zn[c][0], zn[c][1]), g0 = pick(gt[c][0], gt[c][1]), dk = (c & 1) ? bfhi(dsk[c >> 1]) : bflo(dsk[c >> 1]);
                    const float zz = rbf(rbf(z) + up[c]), g1 = zz * sigm(zz);
                    oe[c] = __builtin_fmaf(z * dk, g1 - g0, __builtin_fmaf(pick(yv[c][0], yv[c][1]), g1, pick(xf[c][0], xf[c][1])));
                }
                stg(a.out, xh + e * 1024, u32x2{pkbf(oe[0], oe[1]), pkbf(oe[2], oe[3])});
            }
        }
    }
    STAMPS_OUT;
}
#define ROLE_OF(b) (((b) >> 3) >= 16 ? 0 : (((b) >> 3) < 7 ? 2 : 1))
#define NWAVES 12

__global__ void __launch_bounds__(768) k_probe(char* p) {
    const u64 t0 = __builtin_amdgcn_s_memrealtime();
    asm volatile("v_mov_b32 v160, 0" ::: "v160");
    while (__builtin_amdgcn_s_memrealtime() - t0 < 1000ull) __builtin_amdgcn_s_sleep(1);
    if (p == nullptr) __builtin_trap();
}
extern "C" void kernel_launch(void* const* d_in, const int* in_sizes, int n_in, void* d_out, int out_size, void* d_ws,
                              size_t ws_size, hipStream_t stream) {
    Args a;
    a.x = (const u16*)d_in[0]; a.are = (const u16*)d_in[1]; a.aim = (const u16*)d_in[2]; a.bre = (const u16*)d_in[3];
    a.bim = (const u16*)d_in[4]; a.cre = (const u16*)d_in[5]; a.cim = (const u16*)d_in[6]; a.logdt = (const u16*)d_in[7];
    a.dskip = (const u16*)d_in[8]; a.wr = (const u16*)d_in[10]; a.wg = (const u16*)d_in[11]; a.wu = (const u16*)d_in[12];
    a.wd = (const u16*)d_in[13]; a.out = (u16*)d_out; a.ws = (char*)d_ws;
    (void)hipFuncSetAttribute((const void*)s4_mod_fused, hipFuncAttributeMaxDynamicSharedMemorySize, LDS_TOTAL);
    (void)hipFuncSetAttribute((const void*)k_probe, hipFuncAttributeMaxDynamicSharedMemorySize, 160 * 1024);
    hipLaunchKernelGGL(k_probe, dim3(256), dim3(768), 163840, stream, a.ws);
    hipLaunchKernelGGL(s4_mod_fused, dim3(256), dim3(768), LDS_TOTAL, stream, a);
}
```

```cpp
#include <hip/hip_runtime.h>
#include <stdint.h>

typedef unsigned short u16;
typedef unsigned int u32;
typedef unsigned long long u64;
typedef float f32x2 __attribute__((ext_vector_type(2)));
typedef float f32x4 __attribute__((ext_vector_type(4)));
typedef u32 u32x2 __attribute__((ext_vector_type(2)));
typedef u32 u32x3 __attribute__((ext_vector_type(3)));
typedef u32 u32x4 __attribute__((ext_vector_type(4)));
typedef __bf16 bf16x2 __attribute__((ext_vector_type(2)));
typedef __bf16 bf16x8 __attribute__((ext_vector_type(8)));
typedef _Float16 f16x2 __attribute__((ext_vector_type(2)));
typedef _Float16 f16x8 __attribute__((ext_vector_type(8)));
#define GAS __attribute__((address_space(1)))
#define LAS __attribute__((address_space(3)))
#define CAS __attribute__((address_space(4)))
typedef GAS u64 gu64;
typedef GAS u32 gu32;
#define RLX_AGENT __ATOMIC_RELAXED, __HIP_MEMORY_SCOPE_AGENT

constexpr int D_ = 512, F_ = 1792;
constexpr int NFFN = 56;
constexpr u32 WS_EPOCH = 0, WS_SEL = 1024, WS_SEEN = 2048, WS_STATS = 16384, WS_CAND = WS_STATS + 8 * 16384, WS_PART = WS_CAND + 8 * 4096;
constexpr u32 WS_END = WS_PART + 2 * 128 * NFFN * 32;
constexpr u32 WS_DBG = 1u << 21;
constexpr int LDS_LN = 0;
constexpr int LDS_H = 1024;
constexpr int LDS_UPD = LDS_H + 512;
constexpr int LDS_JUNK = LDS_UPD + 64;
constexpr int LDS_TBL = 2048;
constexpr int TBL_ROW = 1040, TBL_H = 57 * TBL_ROW;
constexpr int LDS_Y = LDS_TBL + TBL_H, Y_TILE = 1280, Y_SZ = 4 * Y_TILE;
constexpr int LDS_U = LDS_TBL + 2 * TBL_H;
constexpr int U_CH = (256 + 2048) * 2;
constexpr int LDS_GU = LDS_U + 4 * U_CH;
constexpr int LDS_P = LDS_GU + 2048 + 512, P_SZ = 5344;
constexpr int C_OFF = 4416 + 64 + 112, C_SZ = 432;
constexpr int LDS_TOTAL = LDS_P + 4 * P_SZ;
constexpr int LDS_FLAG = LDS_JUNK + 128;

struct LaneTab { u32 v[128][8]; };
constexpr LaneTab make_tab() {
    LaneTab t{};
    for (int h = 0; h < 2; h++)
        for (int l = 0; l < 64; l++) {
            const int q = l & 15, g = l >> 4;
            u32* o = t.v[h * 64 + l];
            for (int d = 0; d < 4; d++) {
                const int k0 = 8 * g + 2 * d - q - 8, k1 = k0 + 1;
                o[d] = ((k0 >= h - 4 && k0 <= 4 - h) ? 0x3f80u : 0u) | ((k1 >= h - 4 && k1 <= 4 - h) ? 0x3f800000u : 0u);
            }
            o[4] = 4 * (h ? 1080 - 8 * q - (g < 2 ? 256 + 128 * g : 256 * g) : 1084 - 128 * (g >> 1) - 8 * q - 4 * (g & 1));
            o[5] = h ? (g < 2 ? 4432 : o[4] - 512)
                     : (l < 16 ? l * 16 : (l < 20 ? 4096 + l * 16 : (l < 28 ? C_OFF + ((l - 20) >> 2) * C_SZ + 256 + (l & 3) * 16 : 4432)));
            o[6] = (g < 2 || l == 32) ? C_OFF + (q & 1) * C_SZ + 4 * (64 - (q & 1) - 32 * g - (q >> 1)) : C_OFF - 4;
            o[7] = 0;
        }
    return t;
}
static __device__ const LaneTab lane_tab = make_tab();
#define GATEWARM 0
#define ROWNT 1
#define NPRE 17
constexpr u32 SPIN_CAP = 1u << 16;
constexpr int CODE_BACK = 0, CODE_LINES = 140;

struct Args {
    const u16 *x, *are, *aim, *bre, *bim, *cre, *cim, *logdt, *dskip, *wr, *wg, *wu, *wd;
    u16* out;
    char* ws;
};

template <typename T> __device__ __forceinline__ T ldg(const void* base, u32 off) { return *(const GAS T*)((const GAS char*)base + off); }
template <typename T> __device__ __forceinline__ T ldn(const void* base, u32 off) { return *(const GAS T*)((const GAS char*)base + off); }
template <typename T> __device__ __forceinline__ void stg(void* base, u32 off, T v) { *(GAS T*)((GAS char*)base + off) = v; }
__device__ __forceinline__ u64 gld(const char* base, u32 off) { return __hip_atomic_load((gu64*)((GAS char*)base + off), RLX_AGENT); }
__device__ __forceinline__ void gst(char* base, u32 off, u32 tag, u32 v) { __hip_atomic_store((gu64*)((GAS char*)base + off), ((u64)tag << 32) | v, RLX_AGENT); }
__device__ __forceinline__ float bflo(u32 v) { return __uint_as_float(v << 16); }
__device__ __forceinline__ float bfhi(u32 v) { return __uint_as_float(v & 0xffff0000u); }
__device__ __forceinline__ u32 pkbf(float a, float b) { f32x2 v = {a, b}; return __builtin_bit_cast(u32, __builtin_convertvector(v, bf16x2)); }
__device__ __forceinline__ u32 pkh(f32x2 v) { return __builtin_bit_cast(u32, __builtin_convertvector(v, f16x2)); }
__device__ __forceinline__ float rbf(float a) { return bflo(pkbf(a, a)); }
__device__ __forceinline__ f32x2 cmul(f32x2 a, f32x2 z, f32x2 zn) {
    return __builtin_elementwise_fma(a.yx, zn, a * z.xx);
}
__device__ __forceinline__ f32x2 csq(f32x2 a) { return cmul(a, a, f32x2{-a.y, a.y}); }
__device__ __forceinline__ void rawbar() { asm volatile("s_barrier" ::: "memory"); }
__device__ __forceinline__ void bar() { asm volatile("s_waitcnt lgkmcnt(0)\n\ts_barrier" ::: "memory"); }
#define PIN(x) asm volatile("" : "+v"(x))
#define FENCE() asm volatile("" ::: "memory")
__device__ __forceinline__ f32x4 mfma_bf(u32x4 a, u32x4 b, f32x4 c) {
    return __builtin_amdgcn_mfma_f32_16x16x32_bf16(__builtin_bit_cast(bf16x8, a), __builtin_bit_cast(bf16x8, b), c, 0, 0, 0);
}
__device__ __forceinline__ f32x4 mfma_h(u32x4 a, u32x4 b, f32x4 c) {
    return __builtin_amdgcn_mfma_f32_16x16x32_f16(__builtin_bit_cast(f16x8, a), __builtin_bit_cast(f16x8, b), c, 0, 0, 0);
}
template <int CTRL> __device__ __forceinline__ float dppf(float old, float src) {
    return __uint_as_float(__builtin_amdgcn_update_dpp(__float_as_uint(old), __float_as_uint(src), CTRL, 0xf, 0xf, false));
}
template <int CTRL, int RM = 0xf> __device__ __forceinline__ u32 dpp0(u32 src) { return __builtin_amdgcn_update_dpp(0u, src, CTRL, RM, 0xf, false); }
__device__ __forceinline__ u32 row_umax(u32 v) {
    v = max(v, dpp0<0x128>(v)); v = max(v, dpp0<0x124>(v)); v = max(v, dpp0<0x122>(v)); v = max(v, dpp0<0x121>(v));
    return v;
}
__device__ __forceinline__ u32 wave_umax(u32 v) {
    v = row_umax(v); v = max(v, dpp0<0x142, 0xa>(v)); v = max(v, dpp0<0x143, 0xc>(v));
    return __builtin_amdgcn_readlane(v, 63);
}
__device__ __forceinline__ float wave_fsum(float f) {
    f += __uint_as_float(dpp0<0x128>(__float_as_uint(f))); f += __uint_as_float(dpp0<0x124>(__float_as_uint(f)));
    f += __uint_as_float(dpp0<0x122>(__float_as_uint(f))); f += __uint_as_float(dpp0<0x121>(__float_as_uint(f)));
    f += __uint_as_float(dpp0<0x142, 0xa>(__float_as_uint(f))); f += __uint_as_float(dpp0<0x143, 0xc>(__float_as_uint(f)));
    return __uint_as_float(__builtin_amdgcn_readlane(__float_as_uint(f), 63));
}
__device__ __forceinline__ float sigm(float z) { return __builtin_amdgcn_rcpf(1.0f + __builtin_amdgcn_exp2f(z * -1.44269504089f)); }
__device__ __forceinline__ u64 bad(u64 granule, u32 tag) { return __builtin_amdgcn_ballot_w64((u32)(granule >> 32) != tag); }

#define STAMP(i)
#define STAMPS_DECL
#define STAMPS_OUT

__global__ void __launch_bounds__(768, 3) s4_mod_fused(Args a) {
    extern __shared__ __attribute__((aligned(16))) char smem[];
    LAS char* const lds = (LAS char*)smem;
    const int tid = threadIdx.x, lane = tid & 63;
    const int w = __builtin_amdgcn_readfirstlane(tid >> 6);
    const int xcd = blockIdx.x & 7, j = blockIdx.x >> 3, jj = j & 15;
    const bool heavy = j >= 16, ffn = j < 7, lnr = j >= 7 && j < 15;
    const int q = lane & 15, g = lane >> 4;
    const int ch = w & 3, h = w >> 2;
    const int G = xcd * 16 + jj;
    const f32x4 z4 = {0, 0, 0, 0};
    STAMPS_DECL;
    const u64 tstart_ = __builtin_amdgcn_s_memrealtime();
    STAMP(0);
    u32 tag;
    const __amdgpu_buffer_rsrc_t rw = __builtin_amdgcn_make_buffer_rsrc((void*)a.ws, 0, WS_END, 0x00020000);
    auto bst = [&](const bool on, const u32 off, const u32 v) {
        __builtin_amdgcn_raw_buffer_store_b64(u32x2{v, tag}, rw, on ? off : 0xfffffff0u, 0, 16);
    };

    u32 p_ldt, p_are, p_aim, p_bre, p_bim, p_cre, p_cim, warm0;
    u32x3 adr;
    {
        warm0 = ldg<u32>((const char*)(__builtin_amdgcn_s_getpc() & ~0x7full), min(j * 5 + (tid & 7), CODE_LINES - 1) * 128);
        asm volatile("" :: "s"(a.x), "s"(a.are), "s"(a.aim), "s"(a.bre), "s"(a.bim), "s"(a.cre), "s"(a.cim), "s"(a.logdt), "s"(a.dskip), "s"(a.wr),
                     "s"(a.wg), "s"(a.wu), "s"(a.wd), "s"(a.out), "s"(a.ws) : "memory");
    }
    if (h < 2) {
        const u32 po = ((4 * G + ch) * 128 + 64 * h + lane) * 2;
        p_ldt = ldg<u16>(a.logdt, (4 * G + ch) * 2);
        p_are = ldn<u16>(a.are, po); p_aim = ldn<u16>(a.aim, po); p_bre = ldn<u16>(a.bre, po); p_bim = ldn<u16>(a.bim, po);
        p_cre = ldn<u16>(a.cre, po); p_cim = ldn<u16>(a.cim, po);
        adr = ldg<u32x4>(&lane_tab, (h * 64 + lane) * 32 + 16).xyz;
        *(LAS u32x4*)(lds + LDS_FLAG + (lane < 2 ? 128 + lane * 16 : w * 16)) = u32x4{0, 0, 0, 0};
    }
    int h2 = h;
    asm volatile("" : "+s"(h2) :: "memory");

    if (h2 < 2) {
        bar();
        const u32 tw = LDS_TBL + h * TBL_H + ch * 256, g16 = lane & 48, q16 = q * 16;
        u32 a_tf = __umul24(q, TBL_ROW) + g16 + tw, a_ln = __umul24(g, TBL_ROW) + q16 + tw, a_y = __umul24(g, 320) + q16 + (LDS_Y + w * Y_SZ);
        u32 a_cr = g16 - 4 * q + (LDS_P + ch * P_SZ + C_OFF + h * C_SZ + 60), a_pr = 2 * g16 - 4 * q + (LDS_P + ch * P_SZ + 188 - 128 * h);
        u32 a_u = 2 * q16 + g16 + (LDS_U + ch * U_CH + 464 + 16 * h), a_b = a_u + (16 - 80 * h - (heavy ? 0 : 2048));
        u32 prto = min(lane, NFFN - 1) * 32 + (WS_PART + (h * 128 + G) * NFFN * 32 + ch * 8), selo = (lane & 1) * 8 + (WS_SEL + xcd * 128);
        PIN(a_tf); PIN(a_ln); PIN(a_y); PIN(a_pr); PIN(a_cr); PIN(a_u); PIN(a_b); PIN(prto); PIN(selo);
        __builtin_amdgcn_sched_barrier(0);
        if (h == 0) __builtin_amdgcn_s_setprio(2); else __builtin_amdgcn_s_setprio(1);
        STAMP(11);
        PIN(p_ldt); PIN(p_are); PIN(p_aim); PIN(p_bre); PIN(p_bim); PIN(p_cre); PIN(p_cim);
        STAMP(3);
        const float ld = bflo(p_ldt);
        const float t0 = ld * 1.44269504089f;
        const float te = __builtin_fmaf(ld, 1.92596303e-8f, __builtin_fmaf(ld, 1.44269504089f, -t0));
        const float ex = __builtin_amdgcn_exp2f(t0);
        float dt = rbf(__builtin_fmaf(ex * te, 0.69314718056f, ex));
        const float lr = (dt * 1.44269504089f) * bflo(p_are);
        const float ai = dt * bflo(p_aim);
        const float th = ai * 0.15915494309f;
        const float tl = __builtin_fmaf(ai, 6.4206382e-9f, __builtin_fmaf(ai, 0.15915494309f, -th));
        const float bi = bflo(p_bim);
        const f32x2 W = cmul(f32x2{bflo(p_cre), bflo(p_cim)}, f32x2{bflo(p_bre), bi}, f32x2{-bi, bi}) * dt;
        const float gp = fmaxf(lr, 0.0f);
        const int en = __builtin_amdgcn_frexp_expf(fmaxf(fabsf(W.x), fabsf(W.y))) + (int)(2168.0f * gp);
        const int E = min(max((int)wave_umax((u32)(en + 201)) - 200, -100), 120);
        const float sA = __builtin_amdgcn_ldexpf(__builtin_amdgcn_exp2f(120.0f * gp), 13 - E);
        const float inv = __uint_as_float((u32)(E - 13 + 127) << 23);
        const float t1 = __builtin_amdgcn_fractf(th), e1 = __builtin_amdgcn_exp2f(lr);
        const f32x2 z1 = f32x2{__builtin_amdgcn_cosf(t1), __builtin_amdgcn_sinf(t1)} * e1;
        const float t7 = __builtin_fmaf(128.0f, tl, __builtin_amdgcn_fractf(128.0f * th)), e7 = __builtin_amdgcn_exp2f(128.0f * lr);
        const f32x2 z128 = f32x2{__builtin_amdgcn_cosf(t7), __builtin_amdgcn_sinf(t7)} * e7;
        const f32x2 z2 = csq(z1), zz4 = csq(z2), z8 = csq(zz4);
        f32x2 n1 = {-z1.y, z1.y}, n2 = {-z2.y, z2.y}, n4 = {-zz4.y, zz4.y}, n128 = {-z128.y, z128.y}, c8 = {z8.y, -z8.y};
        f32x2 X = W * sA, B = {__builtin_amdgcn_exp2f(-120.0f * gp), 0.0f};
        PIN(adr);
        const u32 a_e0 = adr[0] + (LDS_P + ch * P_SZ), a_e1 = adr[1] + (LDS_P + ch * P_SZ), a_cw = adr[2] + (LDS_P + ch * P_SZ);
        STAMP(12);
        {
            const u32 m0v = tw + (u32)(size_t)lds;
            asm volatile("s_mov_b32 m0, %0\n\ts_nop 0" :: "s"(m0v) : "memory");
        }
        auto put2 = [&](const int r0, const f32x2 v0, const int r1, const f32x2 v1) {
            asm volatile("ds_write_addtid_b32 %0 offset:%c2\n\tds_write_addtid_b32 %1 offset:%c3" :: "v"(pkh(v0)), "v"(pkh(v1)), "i"(r0 * TBL_ROW), "i"(r1 * TBL_ROW));
        };
        {
            f32x2 c0 = X, X1 = cmul(X, z128, n128), c1 = X1;
            X = cmul(X1, z128, n128);
            f32x2 d = X;
#pragma unroll
            for (int i = 0; i < 8; i++) {
                put2(i, c0, 8 + i, c1);
                put2(41 + i, B, 16 + i, d);
                if (i < 7) { c0 = cmul(c0, z1, n1); c1 = cmul(c1, z1, n1); }
                B = cmul(B, z8, c8);
                if (i == 3) d = X; else if (i < 7) d = cmul(d, z2, n2);
                if (i == 2 || i == 6) X = cmul(X, z128, n128);
            }
#pragma unroll
            for (int i = 0; i < 4; i++) {
                const f32x2 B1 = cmul(B, z8, c8), e = cmul(X, zz4, n4), Xn = cmul(X, z128, n128);
                put2(49 + 2 * i, B, 24 + 2 * i, X);
                if (i < 3) B = cmul(B1, z8, c8);
                put2(50 + 2 * i, B1, 25 + 2 * i, e);
                X = Xn;
            }
        }
        FENCE();
        STAMP(2);
        f32x4 ka, kb;
        float k255, k1024;
        {
            u32x4 fa[4][4];
#pragma unroll
            for (int s = 0; s < 4; s++) {
                fa[s][3] = *(const LAS u32x4*)(lds + a_tf + 41 * TBL_ROW + s * 64);
#pragma unroll
                for (int t = 0; t < 2; t++) fa[s][t] = *(const LAS u32x4*)(lds + a_tf + 16 * t * TBL_ROW + s * 64);
            }
            FENCE();
            const f32x2 X9 = cmul(X, z128, n128);
            put2(32, X, 33, X9);
            if (heavy) {
                const f32x2 z256 = csq(z128), n256 = {-z256.y, z256.y};
                f32x2 e = X, o = X9;
#pragma unroll
                for (int i = 0; i < 3; i++) { e = cmul(e, z256, n256); o = cmul(o, z256, n256); put2(34 + 2 * i, e, 35 + 2 * i, o); }
                e = cmul(e, z256, n256);
                put2(40, e, 40, e);
            }
            __builtin_amdgcn_s_setprio(0);
            FENCE();
#pragma unroll
            for (int s = 0; s < 4; s++) fa[s][2] = *(const LAS u32x4*)(lds + a_tf + 32 * TBL_ROW + s * 64);
            __builtin_amdgcn_sched_barrier(0);
            f32x4 k0, k1, k2;
#pragma unroll
            for (int s = 0; s < 4; s++) { k0 = mfma_h(fa[s][0], fa[s][3], s ? k0 : z4); k1 = mfma_h(fa[s][1], fa[s][3], s ? k1 : z4); }
#pragma unroll
            for (int s = 0; s < 4; s++) k2 = mfma_h(fa[s][2], fa[s][3], s ? k2 : z4);
            FENCE();
            const int dlt = h ? -TBL_H : TBL_H;
            LAS char* fo = lds + LDS_FLAG + w * 16;
            const LAS char* fi = lds + LDS_FLAG + (w ^ 4) * 16;
            if (h) {
                *(LAS f32x4*)(lds + a_ln) = k0 * inv; *(LAS f32x4*)(lds + a_ln + 4 * TBL_ROW) = k2 * inv;
                asm volatile("s_waitcnt lgkmcnt(0)" ::: "memory");
                *(volatile LAS u32*)fo = 1;
                STAMP(13);
                u32x4 fl; f32x4 o;
#pragma clang loop unroll(disable)
                for (u32 spin = 0; spin < SPIN_CAP; spin++) {
                    fl = *(volatile LAS u32x4*)fi; o = *(volatile LAS f32x4*)(lds + a_ln + dlt);
                    if (__builtin_amdgcn_readfirstlane(fl[0])) break;
                    __builtin_amdgcn_s_sleep(2);
                }
                ka = k1 * inv + o;
                k255 = __uint_as_float(__builtin_amdgcn_readlane(__float_as_uint(k0[3]), 63)) * inv + __uint_as_float(fl[1]);
                k1024 = __uint_as_float(__builtin_amdgcn_readlane(__float_as_uint(k2[0]), 0)) * inv + __uint_as_float(fl[2]);
            } else {
                *(LAS f32x4*)(lds + a_ln) = k1 * inv;
                const float s0 = __uint_as_float(__builtin_amdgcn_readlane(__float_as_uint(k0[3]), 63)) * inv;
                const float s1 = __uint_as_float(__builtin_amdgcn_readlane(__float_as_uint(k2[0]), 0)) * inv;
                asm volatile("s_waitcnt lgkmcnt(0)" ::: "memory");
                *(volatile LAS u32x4*)fo = u32x4{1, __float_as_uint(s0), __float_as_uint(s1), 0};
                STAMP(13);
                u32 fl; f32x4 o0, o1;
#pragma clang loop unroll(disable)
                for (u32 spin = 0; spin < SPIN_CAP; spin++) {
                    fl = *(volatile LAS u32*)fi; o0 = *(volatile LAS f32x4*)(lds + a_ln + dlt); o1 = *(volatile LAS f32x4*)(lds + a_ln + dlt + 4 * TBL_ROW);
                    if (__builtin_amdgcn_readfirstlane(fl)) break;
                    __builtin_amdgcn_s_sleep(2);
                }
                ka = k0 * inv + o0; kb = k2 * inv + o1;
                k1024 = __uint_as_float(__builtin_amdgcn_readlane(__float_as_uint(kb[0]), 0));
            }
        }
        STAMP(1);
        if (h == 0) {
            const f32x4 v = ka;
            auto sw = __builtin_amdgcn_permlane16_swap(__float_as_uint(v[3]), __float_as_uint(v[3]), false, false);
            const float pd = dppf<0x138>(0.0f, __uint_as_float(sw[1]));
            const float pv = (g & 1) ? __uint_as_float(sw[0]) : pd;
            *(LAS u32x4*)(lds + a_e0) = u32x4{pkbf(v[3], v[2]), pkbf(v[2], v[1]), pkbf(v[1], v[0]), pkbf(v[0], pv)};
            const u32 kk = pkbf(k1024, k1024);
            const u32 f7 = lane == 15 ? kk : 0u, f6 = lane == 14 ? kk : 0u;
            *(LAS u32x4*)(lds + a_e1) = u32x4{f7, f7, f7 | (f6 & 0xffff0000u), f7 | f6};
            const f32x4 u = {lane == 0 ? 0.0f : kb[0], kb[1], kb[2], kb[3]};
            const bool e = q >= 14;
            LAS u32* cw = (LAS u32*)(lds + a_cw);
            cw[0] = pkbf(u[0], dppf<0x138>(0.0f, dppf<0x138>(0.0f, e ? u[3] : u[0])));
#pragma unroll
            for (int i = 1; i < 4; i++) cw[-8 * i] = pkbf(u[i], dppf<0x122>(0.0f, e ? u[i - 1] : u[i]));
        } else {
            const f32x4 v = ka;
            const bool s2 = g < 2;
            const float nw = dppf<0x130>(v[0], v[0]);
            const float nr = dppf<0x12f>(v[0], q == 0 ? v[2] : v[0]);
            const float nA = s2 ? nw : nr;
            const float nB = dppf<0x130>(k1024, q == 0 ? v[0] : v[2]);
            const float x2 = s2 ? v[1] : v[0], x3 = s2 ? v[2] : v[1], x6 = s2 ? v[3] : nA;
            const u32 aa = pkbf(v[0], v[0]), cc = pkbf(x3, x3), a2 = pkbf(v[2], v[2]), b2 = pkbf(v[3], v[3]);
            *(LAS u32x4*)(lds + a_e0) = u32x4{pkbf(nA, x6), pkbf(x6, x3), cc, cc};
            *(LAS u32x4*)(lds + a_e0 + 16) = u32x4{pkbf(x3, x2), pkbf(x2, v[0]), aa, pkbf(v[0], lane == 0 ? k255 : v[0])};
            *(LAS u32x4*)(lds + a_e1) = u32x4{pkbf(nB, nB), pkbf(nB, v[3]), b2, b2};
            *(LAS u32x4*)(lds + a_e1 + 16) = u32x4{pkbf(v[3], v[2]), a2, a2, a2};
        }
        STAMP(4);
        u32x4 A[17];
        const LAS u32* pr = (const LAS u32*)(lds + a_pr);
        auto lda = [&](const int m) { A[m] = u32x4{pr[64 * (16 - m)], pr[64 * (16 - m) + 2], pr[64 * (16 - m) + 4], pr[64 * (16 - m) + 6]}; };
        {
            asm volatile("s_waitcnt lgkmcnt(0)" ::: "memory");
            *(volatile LAS u32*)(lds + LDS_FLAG + w * 16 + 12) = 2;
#pragma clang loop unroll(disable)
            for (u32 spin = 0; spin < SPIN_CAP; spin++)
                if (__builtin_amdgcn_readfirstlane(*(volatile LAS u32*)(lds + LDS_FLAG + (w ^ 4) * 16 + 12)) == 2) break;
            FENCE();
        }
#pragma unroll
        for (int m = 0; m < NPRE; m++) lda(m);
        asm volatile("" :: "v"(warm0) : "memory");
        STAMP(5);
        if (ffn) rawbar();
        rawbar();
        STAMP(7);

        f32x4 acc[4];
        u64 selv, prtv;
        {
            auto ldb = [&](const int k) { return *(const LAS u32x4*)(lds + a_b + 128 * (28 - k)); };
            u32x4 Bf[29];
            auto mm = [&](const int k, const int first) {
#pragma unroll
                for (int i = 3; i >= 0; i--) {
                    const int mp = 4 * i - 12 + k;
                    if (mp >= 0 && mp < 17) acc[i] = mfma_bf(A[mp], Bf[k], (first >> i) & 1 ? z4 : acc[i]);
                }
            };
            if (heavy) {
                const LAS char* uw = lds + a_u;
                const LAS u32* cr = (const LAS u32*)(lds + a_cr);
                u32x4 Wd[4], KT[4], band, S0, S1;
                f32x4 dw[4];
#pragma unroll
                for (int k = 0; k < 3; k++) { Bf[16 + k] = ldb(16 + k); Bf[k] = ldb(k); }
                __builtin_amdgcn_sched_barrier(0);
                STAMP(6);
#pragma unroll
                for (int k = 0; k < 16; k++) {
                    if (k + 3 < 13) Bf[19 + k] = ldb(19 + k);
                    if (k + 3 < 16) Bf[k + 3] = ldb(k + 3);
                    if (k == 3) band = *(const LAS u32x4*)(lds + LDS_GU + (tid & 63) * 16 + h * 1024);
                    if (k >= 4 && k < 8) Wd[k - 4] = *(const LAS u32x4*)(uw + 512 * (k - 4));
                    if (k >= 10 && k < 13) KT[13 - k] = u32x4{cr[16 * (k - 10)], cr[16 * (k - 10) + 2], cr[16 * (k - 10) + 16], cr[16 * (k - 10) + 18]};
                    if (k == 13) KT[0] = u32x4{cr[48], cr[50], 0u, 0u};
                    FENCE();
                    if (k < 13) mm(k + 16, k == 0 ? 15 : 0);
                    if (k >= 8 && k < 12) dw[k - 8] = mfma_bf(Wd[k - 8], band, z4);
                    mm(k, 0);
                    if (k == 11) S0 = u32x4{pkbf(dw[0][0], dw[0][1]), pkbf(dw[0][2], dw[0][3]), pkbf(dw[1][0], dw[1][1]), pkbf(dw[1][2], dw[1][3])};
                    if (k == 13) S1 = u32x4{pkbf(dw[2][0], dw[2][1]), pkbf(dw[2][2], dw[2][3]), pkbf(dw[3][0], dw[3][1]), pkbf(dw[3][2], dw[3][3])};
                    __builtin_amdgcn_sched_barrier(0);
                }
#pragma unroll
                for (int i = 3; i >= 0; i--) acc[i] = mfma_bf(S0, KT[i], acc[i]);
                acc[3] = mfma_bf(S1, KT[1], acc[3]); acc[2] = mfma_bf(S1, KT[0], acc[2]);
            } else {
#pragma unroll
                for (int k = 0; k < 5; k++) Bf[15 - k] = ldb(15 - k);
                __builtin_amdgcn_sched_barrier(0);
#pragma unroll
                for (int k = 15; k >= 0; k--) {
                    if (k >= 5) Bf[k - 5] = ldb(k - 5);
                    mm(k, k == 15 ? 15 : 0);
                    __builtin_amdgcn_sched_barrier(0);
                }
            }
        }
        STAMP(9);
        selv = gld(a.ws, selo); prtv = gld(a.ws, prto);
        tag = __builtin_amdgcn_readfirstlane(*(LAS u32*)(lds + LDS_UPD + 48));
#pragma unroll
        for (int i = 3; i >= 0; i--) *(LAS f32x4*)(lds + a_y + i * Y_TILE) = acc[i];
        STAMP(8);
        bar();
        if (bad(selv, tag)) {
#pragma clang loop unroll(disable)
            for (u32 spin = 0; spin < SPIN_CAP; spin++) { selv = gld(a.ws, selo); if (!bad(selv, tag)) break; __builtin_amdgcn_s_sleep(2); }
        }
        const u32 sel = h ? __builtin_amdgcn_readlane((u32)selv, 1) : __builtin_amdgcn_readlane((u32)selv, 0);
        const bool own = ((sel & 2047) >= 1024) == heavy;
        if (own) {
            if (bad(prtv, tag)) {
#pragma clang loop unroll(disable)
                for (u32 spin = 0; spin < SPIN_CAP; spin++) { prtv = gld(a.ws, prto); if (!bad(prtv, tag)) break; __builtin_amdgcn_s_sleep(1); }
            }
            const float y = wave_fsum(lane < NFFN ? __uint_as_float((u32)prtv) : 0.0f);
            *(LAS float*)(lds + LDS_UPD + w * 4) = rbf(rbf(y) * bfhi(sel));
        }
        *(LAS u32*)(lds + LDS_UPD + 32 + h * 4) = own ? sel & 2047u : 0x7fffffffu;
        bar();
    } else {
        __builtin_amdgcn_s_setprio(3);
        const int lt = (tid + 64 * jj) & 255;
        const u32 xo = lt * 4096 + G * 8;
        u32 xh = xo + (heavy ? 1024 * 1024 : 0);
        const u32 so = WS_STATS + xcd * 16384 + lt * 32, sh = so + (heavy ? 8192 : 0);
        u32 warm1 = 0, warm2 = 0, warm3 = 0;
        u32x2 xc[4], xl[4];
        u64 sg[4], sl[4], cd[4], ce[4];
        f32x2 xf[4][2], zn[4][2], gt[4][2];
        u32 epoch;
        u32x2 dsk, dskr;
        asm volatile("s_load_dword %0, %2, 0x0\n\ts_load_dwordx2 %1, %3, %4" : "=&s"(epoch), "=&s"(dskr) : "s"(a.ws), "s"(a.dskip), "s"(G * 8));
        auto first = [&]() {
            asm volatile("s_waitcnt lgkmcnt(0)\n\ts_add_u32 %0, %2, 1\n\ts_mov_b64 %1, %3" : "=&s"(tag), "=&s"(dsk) : "s"(epoch), "s"(dskr) : "scc");
        };
        u32x4 bnd;
        if (heavy) bnd = ldg<u32x4>(&lane_tab, (tid & 127) * 32);
        if (lnr) {
            const int lnu = (blockIdx.x - 56) * 4 + ch;
            u32x4 xa[8], cb[8];
#pragma unroll
            for (int s = 0; s < 8; s++) xa[s] = (ROWNT ? ldn<u32x4> : ldg<u32x4>)(a.x, (lnu * 8 + (q >> 1)) * 1024 + (q & 1) * 64 + g * 16 + 128 * s);
            rawbar();
#pragma unroll
            for (int s = 0; s < 8; s++) cb[s] = ldg<u32x4>(a.wr, (q & 2) * 32 + g * 16 + 128 * s);
            const u32x4 ones = {0x3f803f80u, 0x3f803f80u, 0x3f803f80u, 0x3f803f80u};
            const int rr = 2 * g + ((q >> 1) & 1);
            u32 o_st = WS_STATS + ((q & 1) + 2 * (q >> 2)) * 16384 + (lnu * 8 + rr) * 8, o_id = 0xffffu - (lnu * 8 + rr);
            u32 o_cd = (WS_CAND + (lane >> 1) * 4096 + lnu * 16 + (lane & 1) * 8) | (u32)(((15 - lane) >> 31) << 20);
            PIN(o_st); PIN(o_id); PIN(o_cd);
            first();
            f32x4 d0 = z4, d1 = z4, d2 = z4, d3 = z4;
#pragma unroll
            for (int s = 0; s < 8; s++) { d2 = mfma_bf(xa[s], xa[s], d2); d0 = mfma_bf(xa[s], ones, d0); }
            asm volatile("" : "+v"(d0), "+v"(d2));
            __builtin_amdgcn_sched_barrier(0);
#pragma unroll
            for (int s = 0; s < 8; s++) { d1 = mfma_bf(xa[s], cb[s], d1); d3 = mfma_bf(ones, cb[s], d3); }
            STAMP(13);
            const bool hi = q & 2;
            const float da = (q & 1) ? d2[1] : d2[0], db = (q & 1) ? d2[3] : d2[2];
            float sq = (q >> 2) == g ? (hi ? db : da) : 0.0f;
            sq += __uint_as_float(dpp0<0xb1>(__float_as_uint(sq)));
            sq += __uint_as_float(dpp0<0x124>(__float_as_uint(sq)));
            sq += __uint_as_float(dpp0<0x128>(__float_as_uint(sq)));
            const float mean = (hi ? d0[2] + d0[3] : d0[0] + d0[1]) * (1.0f / D_);
            const float var = fmaxf(sq * (1.0f / D_) - mean * mean, 0.0f);
            const float sd = rbf(__builtin_amdgcn_sqrtf(rbf(rbf(var) + 1e-5f)));
            const u32 ms = pkbf(mean, sd);
            bst(true, o_st, ms);
            const float dot = (hi ? d1[3] : d1[0]) + __uint_as_float(dpp0<0x4e>(__float_as_uint(hi ? d1[1] : d1[2])));
            const float sw = d3[0] + __uint_as_float(dpp0<0x4e>(__float_as_uint(d3[0])));
            const float r = (dot - bflo(ms) * sw) * __builtin_amdgcn_rcpf(sd);
            const u32 rb = pkbf(r, r);
            const u32 key = ((rb ^ (((int)rb >> 31) | 0x80000000u)) & 0xffff0000u) | o_id;
            const u32 ko = dpp0<0x4e>(key);
            auto x1 = __builtin_amdgcn_permlane16_swap(max(key, ko), max(key, ko), false, false);
            auto x2 = __builtin_amdgcn_permlane16_swap(min(key, ko), min(key, ko), false, false);
            const u32 a1 = max(x1[0], x1[1]), a2 = max(min(x1[0], x1[1]), max(x2[0], x2[1]));
            auto y1 = __builtin_amdgcn_permlane32_swap(a1, a1, false, false);
            auto y2 = __builtin_amdgcn_permlane32_swap(a2, a2, false, false);
            bst(true, o_cd, (lane & 1) ? max(min(y1[0], y1[1]), max(y2[0], y2[1])) : max(y1[0], y1[1]));
            STAMP(1);
            FENCE();
            *(volatile LAS u32*)(lds + LDS_FLAG + 128 + ch * 4) = 1;
#pragma clang loop unroll(disable)
            for (u32 spin = 0; spin < SPIN_CAP; spin++) {
                const u32x4 f0 = *(volatile LAS u32x4*)(lds + LDS_FLAG + 128);
                if (__builtin_amdgcn_readfirstlane(f0[0] & f0[1] & f0[2] & f0[3])) break;
            }
            FENCE();
        } else {
            warm1 = ldn<u32>(a.x, heavy ? (jj * 128 + ch * 32 + (lane & 31)) * 1024 + xcd * 128 : 0);
            rawbar();
        }
        auto second = [&]() {
            if (!lnr) { first(); bst(lane == 0, WS_SEEN + ((blockIdx.x < 56 ? blockIdx.x : blockIdx.x - 64) * 4 + ch) * 8, 0); }
            *(LAS u32*)(lds + LDS_UPD + 48) = tag;
            *(LAS u32x2*)(lds + LDS_U + ch * U_CH + lane * 8) = u32x2{0, 0};
        };
        u32 yb = LDS_Y + (lt >> 6) * Y_TILE + (lt & 3) * 320 + ((lt >> 2) & 15) * 16;
        const u32 a_st = LDS_U + 512 + 8 * lt;
        auto tags4 = [&](const u64 (&v)[4]) { return (bad(v[0], tag) | bad(v[1], tag) | bad(v[2], tag) | bad(v[3], tag)) == 0; };
        auto sweep4 = [&](u64 (&v)[4], const u32 off, const u32 stride) {
            if (!tags4(v)) {
#pragma clang loop unroll(disable)
                for (u32 spin = 0; spin < SPIN_CAP; spin++) {
#pragma unroll
                    for (int i = 0; i < 4; i++) v[i] = gld(a.ws + stride * i, off);
                    if (tags4(v)) break;
                    __builtin_amdgcn_s_sleep(4);
                }
            }
        };
        auto stage = [&](const u64 (&st)[4], const u32x2 (&r)[4], const u32 dst, const bool keep) {
            f32x2 rs[2], nb[2];
#pragma unroll
            for (int p = 0; p < 2; p++) {
                rs[p] = f32x2{__builtin_amdgcn_rcpf(bfhi((u32)st[2 * p])), __builtin_amdgcn_rcpf(bfhi((u32)st[2 * p + 1]))};
                nb[p] = f32x2{bflo((u32)st[2 * p]), bflo((u32)st[2 * p + 1])} * rs[p];
            }
#pragma unroll
            for (int c = 0; c < 4; c++) {
                u32x2 o;
#pragma unroll
                for (int p = 0; p < 2; p++) {
                    const u32 w0 = r[2 * p][c >> 1], w1 = r[2 * p + 1][c >> 1];
                    const f32x2 xv = (c & 1) ? f32x2{bfhi(w0), bfhi(w1)} : f32x2{bflo(w0), bflo(w1)};
                    const f32x2 z = __builtin_elementwise_fma(xv, rs[p], -nb[p]);
                    if (keep) { xf[c][p] = xv; zn[c][p] = z; }
                    o[p] = pkbf(z.x, z.y);
                }
                *(LAS u32x2*)(lds + dst + c * U_CH) = o;
            }
        };
        auto gates = [&]() {
#pragma unroll
            for (int c = 0; c < 4; c++)
#pragma unroll
                for (int p = 0; p < 2; p++) {
                    const f32x2 e = zn[c][p] * -1.44269504089f;
                    const f32x2 d = f32x2{__builtin_amdgcn_exp2f(e.x), __builtin_amdgcn_exp2f(e.y)} + 1.0f;
                    gt[c][p] = zn[c][p] * f32x2{__builtin_amdgcn_rcpf(d.x), __builtin_amdgcn_rcpf(d.y)};
                    PIN(gt[c][p]);
                }
        };
        if (ffn) {
            const int f = xcd * 7 + jj;
            u32 wgu[2][4][8], wdn[4][8];
            const __amdgpu_buffer_rsrc_t rg = __builtin_amdgcn_make_buffer_rsrc((void*)((f & 1) ? a.wu : a.wg), 0, D_ * F_ * 2, 0x00020000);
            const __amdgpu_buffer_rsrc_t ru = __builtin_amdgcn_make_buffer_rsrc((void*)((f & 1) ? a.wg : a.wu), 0, D_ * F_ * 2, 0x00020000);
            const __amdgpu_buffer_rsrc_t rd = __builtin_amdgcn_make_buffer_rsrc((void*)a.wd, 0, D_ * F_ * 2, 0x00020000);
            const u32 og = ((128 * ch + 8 * g) * F_ + 32 * f + 2 * q) * 2, od = ((32 * f + 8 * g) * D_ + 128 * ch + 2 * q) * 2;
#pragma unroll
            for (int m = 0; m < 2; m++)
#pragma unroll
                for (int s = 0; s < 4; s++)
#pragma unroll
                    for (int i = 0; i < 8; i++) wgu[m][s][i] = __builtin_amdgcn_raw_buffer_load_b32(m ? ru : rg, og, (32 * s + i) * F_ * 2, 0);
#pragma unroll
            for (int i = 0; i < 8; i++)
#pragma unroll
                for (int u = 0; u < 4; u++) wdn[u][i] = __builtin_amdgcn_raw_buffer_load_b32(rd, od + ((i & 3) * D_ * 2 + u * 64), (i & 4) * D_ * 2, 0);
            second();
            STAMP(12);
            const u32 co = WS_CAND + xcd * 4096 + lane * 8;
#pragma unroll
            for (int i = 0; i < 4; i++) { cd[i] = gld(a.ws + 512 * i, co); ce[i] = gld(a.ws + 512 * i + 2048, co); }
            u32x4 fg[2][4][2], fd[4][2];
#pragma unroll
            for (int m = 0; m < 3; m++)
#pragma unroll
                for (int s = 0; s < 4; s++)
#pragma unroll
                    for (int p = 0; p < 2; p++) {
                        u32x4 o;
#pragma unroll
                        for (int d = 0; d < 4; d++)
                            o[d] = __builtin_amdgcn_perm(m < 2 ? wgu[m & 1][s][2 * d + 1] : wdn[s][2 * d + 1], m < 2 ? wgu[m & 1][s][2 * d] : wdn[s][2 * d], p ? 0x07060302u : 0x05040100u);
                        PIN(o);
                        if (m < 2) fg[m & 1][s][p] = o; else fd[s][p] = o;
                    }
            u32x4 an[4];
            const u32 o1 = q < 2 ? 0u : 0x3f803f80u;
            const __amdgpu_buffer_rsrc_t rx = __builtin_amdgcn_make_buffer_rsrc((void*)a.x, 0, 2048 * 1024, 0x00020000);
            asm volatile("" :: "v"(warm0), "v"(warm1));
            sweep4(cd, co, 512);
            sweep4(ce, co + 2048, 512);
            STAMP(4);
            u32 ka = max((u32)cd[0], (u32)ce[0]), kb = min((u32)cd[0], (u32)ce[0]);
#pragma unroll
            for (int i = 1; i < 4; i++) {
                const u32 ha = max((u32)cd[i], (u32)ce[i]), hb = min((u32)cd[i], (u32)ce[i]);
                kb = max(min(ka, ha), max(kb, hb)); ka = max(ka, ha);
            }
            const u32 k1 = wave_umax(ka), k2 = wave_umax(ka == k1 ? kb : ka);
            const u32 kk = (lane & 1) ? k2 : k1;
            const u32 row = ~kk & 2047u;
            const u32 xr = q < 2 ? row * 1024 + ch * 256 + g * 16 : 0xfffff000u;
#pragma unroll
            for (int s = 0; s < 4; s++) an[s] = __builtin_amdgcn_raw_buffer_load_b128(rx, xr + 64 * s, 0, 0);
            const u32 sro = WS_STATS + xcd * 16384 + row * 8;
            u64 sr = gld(a.ws, sro);
            STAMP(11);
            if (jj == 0 && ch == 0) {
                const float sv = sigm(__uint_as_float((kk ^ ((~(int)kk >> 31) | 0x80000000u)) & 0xffff0000u));
                bst(lane < 2, WS_SEL + xcd * 128 + lane * 8, row | (pkbf(sv, sv) << 16));
            }
#pragma unroll
            for (int i = 0; i < 4; i++) xc[i] = ldg<u32x2>(a.x, xh + 1024 * i);
#pragma unroll
            for (int i = 0; i < 4; i++) sg[i] = gld(a.ws + 8 * i, sh);
            f32x4 c[2][2];
#pragma unroll
            for (int s = 0; s < 4; s++)
#pragma unroll
                for (int mt = 0; mt < 2; mt++)
#pragma unroll
                    for (int p = 0; p < 2; p++) c[mt][p] = mfma_bf(an[s] | o1, fg[mt][s][p], s ? c[mt][p] : z4);
            if (bad(sr, tag)) {
#pragma clang loop unroll(disable)
                for (u32 spin = 0; spin < SPIN_CAP; spin++) { sr = gld(a.ws, sro); if (!bad(sr, tag)) break; }
            }
            PIN(c[0][0]); PIN(c[1][1]);
            STAMP(2);
            const u32 s0 = __builtin_amdgcn_readlane((u32)sr, 0), s1 = __builtin_amdgcn_readlane((u32)sr, 1);
            LAS float* gu = (LAS float*)(lds + LDS_GU);
            LAS float* gw = gu + (g == 0 ? ch * 128 + 2 * q : 512);
#pragma unroll
            for (int mt = 0; mt < 2; mt++) {
                *(LAS f32x2*)(gw + (mt ^ (f & 1)) * 64) = f32x2{c[mt][0][0], c[mt][1][0]} - bflo(s0) * f32x2{c[mt][0][2], c[mt][1][2]};
                *(LAS f32x2*)(gw + (mt ^ (f & 1)) * 64 + 32) = f32x2{c[mt][0][1], c[mt][1][1]} - bflo(s1) * f32x2{c[mt][0][2], c[mt][1][2]};
            }
            const float rs = __builtin_amdgcn_rcpf(bfhi(lane < 32 ? s0 : s1));
            bar();
            f32x2 gs = {0.0f, 0.0f};
#pragma unroll
            for (int i = 0; i < 4; i++) gs += f32x2{gu[i * 128 + lane], gu[i * 128 + 64 + lane]};
            const u32 gb = pkbf(gs.x * rs, gs.y * rs);
            const float ga = bflo(gb);
            const float hv = rbf(ga * sigm(ga)) * bfhi(gb);
            LAS u16* hb = (LAS u16*)(lds + LDS_H + ch * 128);
            hb[lane] = (u16)pkbf(hv, hv);
            FENCE();
            const u32x4 ah = *(const LAS u32x4*)(hb + (q & 1) * 32 + 8 * g);
            f32x4 y0 = z4, y1 = z4;
#pragma unroll
            for (int u = 0; u < 4; u++) {
                const f32x4 d0 = mfma_bf(ah, fd[u][0], z4), d1 = mfma_bf(ah, fd[u][1], z4);
                y0[0] = g == u ? d0[0] : y0[0]; y0[1] = g == u ? d0[1] : y0[1]; y1[0] = g == u ? d1[0] : y1[0]; y1[1] = g == u ? d1[1] : y1[1];
            }
            STAMP(3);
            asm volatile("" : "+v"(sg[0]), "+v"(sg[1]), "+v"(sg[2]), "+v"(sg[3]));
            const u32 pp = WS_PART + (((32 * ch + 8 * g + (q >> 1)) * NFFN + f) * 4 + 2 * (q & 1)) * 8;
            bst(true, pp, __float_as_uint(y0[0])); bst(true, pp + 8, __float_as_uint(y1[0]));
            bst(true, pp + 128 * NFFN * 32, __float_as_uint(y0[1])); bst(true, pp + 128 * NFFN * 32 + 8, __float_as_uint(y1[1]));
            STAMP(5);
            sweep4(sg, sh, 8);
            u32 t2 = tid;
            PIN(t2);
            stage(sg, xc, LDS_U + 512 + 8 * ((t2 + 64 * jj) & 255), true);
        } else {
            if (!heavy) {
                u32 s1 = ((j - 7) * 4 + ch) * 15, s2 = ((j - 7) * 4 + ch) * 50;
                asm volatile("" : "+s"(s1), "+s"(s2));
                warm3 = ldn<u32>((xcd & 1) ? a.wg : a.wu, __umul24(min(s1 + min(lane, 15), 511u), F_ * 2) + (xcd * 7 + ((xcd & 1) ? 0 : 6)) * 64);
                warm2 = ldn<u32>(a.wd, min(s2 + min(lane, 50), 1791u) * 128 + xcd * 224 * 1024);
            }
#pragma unroll
            for (int i = 0; i < 4; i++) xc[i] = ldg<u32x2>(a.x, xh + 1024 * i);
            if (heavy) {
#pragma unroll
                for (int i = 0; i < 4; i++) xl[i] = ldg<u32x2>(a.x, xo + 1024 * i);
                *(LAS u32x4*)(lds + LDS_GU + (tid & 127) * 16) = bnd;
            }
            second();
            STAMP(12);
#pragma unroll
            for (int i = 0; i < 4; i++) sg[i] = gld(a.ws + 8 * i, sh);
            if (heavy) {
#pragma unroll
                for (int i = 0; i < 4; i++) sl[i] = gld(a.ws + 8 * i, so);
            }
            sweep4(sg, sh, 8);
            asm volatile("" :: "v"(warm0), "v"(warm1), "v"(warm2), "v"(warm3));
            STAMP(4);
            stage(sg, xc, a_st + (heavy ? 2048 : 0), true);
            if (heavy) {
                sweep4(sl, so, 8);
                stage(sl, xl, a_st, false);
                if (blockIdx.x == 128) {
#pragma clang loop unroll(disable)
                    for (u32 spin = 0; spin < SPIN_CAP; spin++)
                        if (!(bad(gld(a.ws + WS_SEEN, (tid & 255) * 8), tag) | bad(gld(a.ws + WS_SEEN + 2048, (tid & 255) * 8), tag) | bad(gld(a.ws + WS_SEEN + 4096, (tid & 255) * 8), tag))) break;
                }
            }
        }
        STAMP(6);
        bar();
        if (blockIdx.x == 128 && ch == 0) __builtin_amdgcn_raw_buffer_store_b32(tag, rw, lane == 0 ? WS_EPOCH : 0xfffffff0u, 0, 16);
        PIN(yb);
        __builtin_amdgcn_s_setprio(0);
        gates();
#pragma unroll
        for (int c = 0; c < 4; c++) {
            const float dk = (c & 1) ? bfhi(dsk[c >> 1]) : bflo(dsk[c >> 1]);
#pragma unroll
            for (int p = 0; p < 2; p++) { xf[c][p] = __builtin_elementwise_fma(zn[c][p] * dk, gt[c][p], xf[c][p]); PIN(xf[c][p]); }
        }
        __builtin_amdgcn_s_setprio(3);
        STAMP(7);
        rawbar();
        STAMP(8);
        f32x2 yv[4][2];
        u32x2 o[4];
#pragma unroll
        for (int c = 0; c < 4; c++) {
            const f32x4 y = *(const LAS f32x4*)(lds + yb + c * Y_SZ) + *(const LAS f32x4*)(lds + yb + (c + 4) * Y_SZ);
            yv[c][0] = y.lo; yv[c][1] = y.hi;
        }
#pragma unroll
        for (int p = 0; p < 2; p++) {
            f32x2 t[4];
#pragma unroll
            for (int c = 0; c < 4; c++) t[c] = __builtin_elementwise_fma(yv[c][p], gt[c][p], xf[c][p]);
            o[2 * p] = u32x2{pkbf(t[0].x, t[1].x), pkbf(t[2].x, t[3].x)};
            o[2 * p + 1] = u32x2{pkbf(t[0].y, t[1].y), pkbf(t[2].y, t[3].y)};
        }
#pragma unroll
        for (int i = 0; i < 4; i++) __builtin_amdgcn_raw_buffer_store_b64(o[i], __builtin_amdgcn_make_buffer_rsrc((void*)a.out, 0, 2048 * 1024, 0x00020000), xh + 1024 * i, 0, 16);
        STAMP(9);
        rawbar();
        const u32x2 rows = *(const LAS u32x2*)(lds + LDS_UPD + 32);
#pragma clang loop unroll(disable)
        for (int t = 0; t < 2; t++) {
            const u32 e = (t ? rows.y : rows.x) - (xh >> 10);
            if (e < 4) {
                const f32x4 up = *(const LAS f32x4*)(lds + LDS_UPD + t * 16);
                float oe[4];
#pragma unroll
                for (int c = 0; c < 4; c++) {
                    auto pick = [&](const f32x2 v0, const f32x2 v1) { const float s0 = (e & 2) ? v1.x : v0.x, s1 = (e & 2) ? v1.y : v0.y; return (e & 1) ? s1 : s0; };
                    const float z = pick(zn[c][0], zn[c][1]), g0 = pick(gt[c][0], gt[c][1]), dk = (c & 1) ? bfhi(dsk[c >> 1]) : bflo(dsk[c >> 1]);
                    const float zz = rbf(rbf(z) + up[c]), g1 = zz * sigm(zz);
                    oe[c] = __builtin_fmaf(z * dk, g1 - g0, __builtin_fmaf(pick(yv[c][0], yv[c][1]), g1, pick(xf[c][0], xf[c][1])));
                }
                __builtin_amdgcn_raw_buffer_store_b64(u32x2{pkbf(oe[0], oe[1]), pkbf(oe[2], oe[3])}, __builtin_amdgcn_make_buffer_rsrc((void*)a.out, 0, 2048 * 1024, 0x00020000), xh + e * 1024, 0, 16);
            }
        }
    }
    while (__builtin_amdgcn_s_memrealtime() - tstart_ < 2500ull) __builtin_amdgcn_s_sleep(1);
    STAMPS_OUT;
}
#define ROLE_OF(b) (((b) >> 3) >= 16 ? 0 : (((b) >> 3) < 7 ? 2 : 1))
#define NWAVES 12

extern "C" void kernel_launch(void* const* d_in, const int* in_sizes, int n_in, void* d_out, int out_size, void* d_ws,
                              size_t ws_size, hipStream_t stream) {
    Args a;
    a.x = (const u16*)d_in[0]; a.are = (const u16*)d_in[1]; a.aim = (const u16*)d_in[2]; a.bre = (const u16*)d_in[3];
    a.bim = (const u16*)d_in[4]; a.cre = (const u16*)d_in[5]; a.cim = (const u16*)d_in[6]; a.logdt = (const u16*)d_in[7];
    a.dskip = (const u16*)d_in[8]; a.wr = (const u16*)d_in[10]; a.wg = (const u16*)d_in[11]; a.wu = (const u16*)d_in[12];
    a.wd = (const u16*)d_in[13]; a.out = (u16*)d_out; a.ws = (char*)d_ws;
    (void)hipFuncSetAttribute((const void*)s4_mod_fused, hipFuncAttributeMaxDynamicSharedMemorySize, LDS_TOTAL);
    hipLaunchKernelGGL(s4_mod_fused, dim3(256), dim3(768), LDS_TOTAL, stream, a);
}
```

```cpp
#include <hip/hip_runtime.h>
#include <stdint.h>

typedef unsigned short u16;
typedef unsigned int u32;
typedef unsigned long long u64;
typedef float f32x2 __attribute__((ext_vector_type(2)));
typedef float f32x4 __attribute__((ext_vector_type(4)));
typedef u32 u32x2 __attribute__((ext_vector_type(2)));
typedef u32 u32x3 __attribute__((ext_vector_type(3)));
typedef u32 u32x4 __attribute__((ext_vector_type(4)));
typedef __bf16 bf16x2 __attribute__((ext_vector_type(2)));
typedef __bf16 bf16x8 __attribute__((ext_vector_type(8)));
typedef _Float16 f16x2 __attribute__((ext_vector_type(2)));
typedef _Float16 f16x8 __attribute__((ext_vector_type(8)));
#define GAS __attribute__((address_space(1)))
#define LAS __attribute__((address_space(3)))
#define CAS __attribute__((address_space(4)))
typedef GAS u64 gu64;
typedef GAS u32 gu32;
#define RLX_AGENT __ATOMIC_RELAXED, __HIP_MEMORY_SCOPE_AGENT

constexpr int D_ = 512, F_ = 1792;
constexpr int NFFN = 56;
constexpr u32 WS_EPOCH = 0, WS_SEL = 1024, WS_SEEN = 2048, WS_STATS = 16384, WS_CAND = WS_STATS + 8 * 16384, WS_PART = WS_CAND + 8 * 4096;
constexpr u32 WS_END = WS_PART + 2 * 128 * NFFN * 32;
constexpr u32 WS_DBG = 1u << 21;
constexpr int LDS_LN = 0;
constexpr int LDS_H = 1024;
constexpr int LDS_UPD = LDS_H + 512;
constexpr int LDS_JUNK = LDS_UPD + 64;
constexpr int LDS_TBL = 2048;
constexpr int TBL_ROW = 1040, TBL_H = 57 * TBL_ROW;
constexpr int LDS_Y = LDS_TBL + TBL_H, Y_TILE = 1280, Y_SZ = 4 * Y_TILE;
constexpr int LDS_U = LDS_TBL + 2 * TBL_H;
constexpr int U_CH = (256 + 2048) * 2;
constexpr int LDS_GU = LDS_U + 4 * U_CH;
constexpr int LDS_P = LDS_GU + 2048 + 512, P_SZ = 5344;
constexpr int C_OFF = 4416 + 64 + 112, C_SZ = 432;
constexpr int LDS_TOTAL = LDS_P + 4 * P_SZ;
constexpr int LDS_FLAG = LDS_JUNK + 128;

struct LaneTab { u32 v[128][8]; };
constexpr LaneTab make_tab() {
    LaneTab t{};
    for (int h = 0; h < 2; h++)
        for (int l = 0; l < 64; l++) {
            const int q = l & 15, g = l >> 4;
            u32* o = t.v[h * 64 + l];
            for (int d = 0; d < 4; d++) {
                const int k0 = 8 * g + 2 * d - q - 8, k1 = k0 + 1;
                o[d] = ((k0 >= h - 4 && k0 <= 4 - h) ? 0x3f80u : 0u) | ((k1 >= h - 4 && k1 <= 4 - h) ? 0x3f800000u : 0u);
            }
            o[4] = 4 * (h ? 1080 - 8 * q - (g < 2 ? 256 + 128 * g : 256 * g) : 1084 - 128 * (g >> 1) - 8 * q - 4 * (g & 1));
            o[5] = h ? (g < 2 ? 4432 : o[4] - 512)
                     : (l < 16 ? l * 16 : (l < 20 ? 4096 + l * 16 : (l < 28 ? C_OFF + ((l - 20) >> 2) * C_SZ + 256 + (l & 3) * 16 : 4432)));
            o[6] = (g < 2 || l == 32) ? C_OFF + (q & 1) * C_SZ + 4 * (64 - (q & 1) - 32 * g - (q >> 1)) : C_OFF - 4;
            o[7] = 0;
        }
    return t;
}
static __device__ const LaneTab lane_tab = make_tab();
#define LIGHT_WAIT 16
#define NPRE 17
constexpr u32 SPIN_CAP = 1u << 16;
constexpr int CODE_BACK = 0, CODE_LINES = 150;

struct Args {
    const u16 *x, *are, *aim, *bre, *bim, *cre, *cim, *logdt, *dskip, *wr, *wg, *wu, *wd;
    u16* out;
    char* ws;
};

template <typename T> __device__ __forceinline__ T ldg(const void* base, u32 off) { return *(const GAS T*)((const GAS char*)base + off); }
template <typename T> __device__ __forceinline__ void stg(void* base, u32 off, T v) { *(GAS T*)((GAS char*)base + off) = v; }
__device__ __forceinline__ u64 gld(const char* base, u32 off) { return __hip_atomic_load((gu64*)((GAS char*)base + off), RLX_AGENT); }
__device__ __forceinline__ void gst(char* base, u32 off, u32 tag, u32 v) { __hip_atomic_store((gu64*)((GAS char*)base + off), ((u64)tag << 32) | v, RLX_AGENT); }
__device__ __forceinline__ float bflo(u32 v) { return __uint_as_float(v << 16); }
__device__ __forceinline__ float bfhi(u32 v) { return __uint_as_float(v & 0xffff0000u); }
__device__ __forceinline__ u32 pkbf(float a, float b) { f32x2 v = {a, b}; return __builtin_bit_cast(u32, __builtin_convertvector(v, bf16x2)); }
__device__ __forceinline__ u32 pkh(f32x2 v) { return __builtin_bit_cast(u32, __builtin_convertvector(v, f16x2)); }
__device__ __forceinline__ float rbf(float a) { return bflo(pkbf(a, a)); }
__device__ __forceinline__ f32x2 cmul(f32x2 a, f32x2 z, f32x2 zn) {
    return __builtin_elementwise_fma(a.yx, zn, a * z.xx);
}
__device__ __forceinline__ f32x2 csq(f32x2 a) { return cmul(a, a, f32x2{-a.y, a.y}); }
__device__ __forceinline__ void rawbar() { asm volatile("s_barrier" ::: "memory"); }
__device__ __forceinline__ void bar() { asm volatile("s_waitcnt lgkmcnt(0)\n\ts_barrier" ::: "memory"); }
#define PIN(x) asm volatile("" : "+v"(x))
#define FENCE() asm volatile("" ::: "memory")
__device__ __forceinline__ f32x4 mfma_bf(u32x4 a, u32x4 b, f32x4 c) {
    return __builtin_amdgcn_mfma_f32_16x16x32_bf16(__builtin_bit_cast(bf16x8, a), __builtin_bit_cast(bf16x8, b), c, 0, 0, 0);
}
__device__ __forceinline__ f32x4 mfma_h(u32x4 a, u32x4 b, f32x4 c) {
    return __builtin_amdgcn_mfma_f32_16x16x32_f16(__builtin_bit_cast(f16x8, a), __builtin_bit_cast(f16x8, b), c, 0, 0, 0);
}
template <int CTRL> __device__ __forceinline__ float dppf(float old, float src) {
    return __uint_as_float(__builtin_amdgcn_update_dpp(__float_as_uint(old), __float_as_uint(src), CTRL, 0xf, 0xf, false));
}
template <int CTRL, int RM = 0xf> __device__ __forceinline__ u32 dpp0(u32 src) { return __builtin_amdgcn_update_dpp(0u, src, CTRL, RM, 0xf, false); }
__device__ __forceinline__ u32 row_umax(u32 v) {
    v = max(v, dpp0<0x128>(v)); v = max(v, dpp0<0x124>(v)); v = max(v, dpp0<0x122>(v)); v = max(v, dpp0<0x121>(v));
    return v;
}
__device__ __forceinline__ u32 wave_umax(u32 v) {
    v = row_umax(v); v = max(v, dpp0<0x142, 0xa>(v)); v = max(v, dpp0<0x143, 0xc>(v));
    return __builtin_amdgcn_readlane(v, 63);
}
__device__ __forceinline__ float wave_fsum(float f) {
    f += __uint_as_float(dpp0<0x128>(__float_as_uint(f))); f += __uint_as_float(dpp0<0x124>(__float_as_uint(f)));
    f += __uint_as_float(dpp0<0x122>(__float_as_uint(f))); f += __uint_as_float(dpp0<0x121>(__float_as_uint(f)));
    f += __uint_as_float(dpp0<0x142, 0xa>(__float_as_uint(f))); f += __uint_as_float(dpp0<0x143, 0xc>(__float_as_uint(f)));
    return __uint_as_float(__builtin_amdgcn_readlane(__float_as_uint(f), 63));
}
__device__ __forceinline__ float sigm(float z) { return __builtin_amdgcn_rcpf(1.0f + __builtin_amdgcn_exp2f(z * -1.44269504089f)); }
__device__ __forceinline__ u64 bad(u64 granule, u32 tag) { return __builtin_amdgcn_ballot_w64((u32)(granule >> 32) != tag); }

#define STAMP(i)
#define STAMPS_DECL
#define STAMPS_OUT

__global__ void __launch_bounds__(768, 3) s4_mod_fused(Args a) {
    extern __shared__ __attribute__((aligned(16))) char smem[];
    LAS char* const lds = (LAS char*)smem;
    const int tid = threadIdx.x, lane = tid & 63;
    const int w = __builtin_amdgcn_readfirstlane(tid >> 6);
    const int xcd = blockIdx.x & 7, j = blockIdx.x >> 3, jj = j & 15;
    const bool heavy = j >= 16, ffn = j < 7;
    const int q = lane & 15, g = lane >> 4;
    const int ch = w & 3, h = w >> 2;
    const int G = xcd * 16 + jj;
    const f32x4 z4 = {0, 0, 0, 0};
    STAMPS_DECL;
    STAMP(0);
    u32 tag;
    const __amdgpu_buffer_rsrc_t rw = __builtin_amdgcn_make_buffer_rsrc((void*)a.ws, 0, WS_END, 0x00020000);
    auto bst = [&](const bool on, const u32 off, const u32 v) {
        __builtin_amdgcn_raw_buffer_store_b64(u32x2{v, tag}, rw, on ? off : 0xfffffff0u, 0, 16);
    };

    u32 p_ldt, p_are, p_aim, p_bre, p_bim, p_cre, p_cim, warm0;
    u32x3 adr;
    {
        warm0 = ldg<u32>((const char*)(__builtin_amdgcn_s_getpc() & ~0x7full), min(j * 5 + (tid & 7), CODE_LINES - 1) * 128);
        asm volatile("" :: "s"(a.x), "s"(a.are), "s"(a.aim), "s"(a.bre), "s"(a.bim), "s"(a.cre), "s"(a.cim), "s"(a.logdt), "s"(a.dskip), "s"(a.wr),
                     "s"(a.wg), "s"(a.wu), "s"(a.wd), "s"(a.out), "s"(a.ws) : "memory");
    }
    if (h < 2) {
        const u32 po = ((4 * G + ch) * 128 + 64 * h + lane) * 2;
        p_ldt = ldg<u16>(a.logdt, (4 * G + ch) * 2);
        p_are = ldg<u16>(a.are, po); p_aim = ldg<u16>(a.aim, po); p_bre = ldg<u16>(a.bre, po); p_bim = ldg<u16>(a.bim, po);
        p_cre = ldg<u16>(a.cre, po); p_cim = ldg<u16>(a.cim, po);
        adr = ldg<u32x4>(&lane_tab, (h * 64 + lane) * 32 + 16).xyz;
        *(LAS u32x4*)(lds + LDS_FLAG + (lane < 2 ? 128 + lane * 16 : w * 16)) = u32x4{0, 0, 0, 0};
    }
    int h2 = h;
    asm volatile("" : "+s"(h2) :: "memory");

    if (h2 < 2) {
        bar();
        const u32 tw = LDS_TBL + h * TBL_H + ch * 256, g16 = lane & 48, q16 = q * 16;
        u32 a_tf = __umul24(q, TBL_ROW) + g16 + tw, a_ln = __umul24(g, TBL_ROW) + q16 + tw, a_y = __umul24(g, 320) + q16 + (LDS_Y + w * Y_SZ);
        u32 a_cr = g16 - 4 * q + (LDS_P + ch * P_SZ + C_OFF + h * C_SZ + 60), a_pr = 2 * g16 - 4 * q + (LDS_P + ch * P_SZ + 188 - 128 * h);
        u32 a_u = 2 * q16 + g16 + (LDS_U + ch * U_CH + 464 + 16 * h), a_b = a_u + (16 - 80 * h - (heavy ? 0 : 2048));
        u32 prto = min(lane, NFFN - 1) * 32 + (WS_PART + (h * 128 + G) * NFFN * 32 + ch * 8), selo = (lane & 1) * 8 + (WS_SEL + xcd * 128);
        PIN(a_tf); PIN(a_ln); PIN(a_y); PIN(a_pr); PIN(a_cr); PIN(a_u); PIN(a_b); PIN(prto); PIN(selo);
        __builtin_amdgcn_sched_barrier(0);
        if (h == 0) __builtin_amdgcn_s_setprio(2); else __builtin_amdgcn_s_setprio(1);
        STAMP(11);
        PIN(p_ldt); PIN(p_are); PIN(p_aim); PIN(p_bre); PIN(p_bim); PIN(p_cre); PIN(p_cim);
        STAMP(3);
        const float ld = bflo(p_ldt);
        const float t0 = ld * 1.44269504089f;
        const float te = __builtin_fmaf(ld, 1.92596303e-8f, __builtin_fmaf(ld, 1.44269504089f, -t0));
        const float ex = __builtin_amdgcn_exp2f(t0);
        float dt = rbf(__builtin_fmaf(ex * te, 0.69314718056f, ex));
        const float lr = (dt * 1.44269504089f) * bflo(p_are);
        const float ai = dt * bflo(p_aim);
        const float th = ai * 0.15915494309f;
        const float tl = __builtin_fmaf(ai, 6.4206382e-9f, __builtin_fmaf(ai, 0.15915494309f, -th));
        const float bi = bflo(p_bim);
        const f32x2 W = cmul(f32x2{bflo(p_cre), bflo(p_cim)}, f32x2{bflo(p_bre), bi}, f32x2{-bi, bi}) * dt;
        const float gp = fmaxf(lr, 0.0f);
        const int en = __builtin_amdgcn_frexp_expf(fmaxf(fabsf(W.x), fabsf(W.y))) + (int)(2168.0f * gp);
        const int E = min(max((int)wave_umax((u32)(en + 201)) - 200, -100), 120);
        const float sA = __builtin_amdgcn_ldexpf(__builtin_amdgcn_exp2f(120.0f * gp), 13 - E);
        const float inv = __uint_as_float((u32)(E - 13 + 127) << 23);
        const float t1 = __builtin_amdgcn_fractf(th), e1 = __builtin_amdgcn_exp2f(lr);
        const f32x2 z1 = f32x2{__builtin_amdgcn_cosf(t1), __builtin_amdgcn_sinf(t1)} * e1;
        const float t7 = __builtin_fmaf(128.0f, tl, __builtin_amdgcn_fractf(128.0f * th)), e7 = __builtin_amdgcn_exp2f(128.0f * lr);
        const f32x2 z128 = f32x2{__builtin_amdgcn_cosf(t7), __builtin_amdgcn_sinf(t7)} * e7;
        const f32x2 z2 = csq(z1), zz4 = csq(z2), z8 = csq(zz4);
        f32x2 n1 = {-z1.y, z1.y}, n2 = {-z2.y, z2.y}, n4 = {-zz4.y, zz4.y}, n128 = {-z128.y, z128.y}, c8 = {z8.y, -z8.y};
        f32x2 X = W * sA, B = {__builtin_amdgcn_exp2f(-120.0f * gp), 0.0f};
        PIN(adr);
        const u32 a_e0 = adr[0] + (LDS_P + ch * P_SZ), a_e1 = adr[1] + (LDS_P + ch * P_SZ), a_cw = adr[2] + (LDS_P + ch * P_SZ);
        STAMP(12);
        {
            const u32 m0v = tw + (u32)(size_t)lds;
            asm volatile("s_mov_b32 m0, %0\n\ts_nop 0" :: "s"(m0v) : "memory");
        }
        auto put2 = [&](const int r0, const f32x2 v0, const int r1, const f32x2 v1) {
            asm volatile("ds_write_addtid_b32 %0 offset:%c2\n\tds_write_addtid_b32 %1 offset:%c3" :: "v"(pkh(v0)), "v"(pkh(v1)), "i"(r0 * TBL_ROW), "i"(r1 * TBL_ROW));
        };
        {
            f32x2 c0 = X, X1 = cmul(X, z128, n128), c1 = X1;
            X = cmul(X1, z128, n128);
            f32x2 d = X;
#pragma unroll
            for (int i = 0; i < 8; i++) {
                put2(i, c0, 8 + i, c1);
                put2(41 + i, B, 16 + i, d);
                if (i < 7) { c0 = cmul(c0, z1, n1); c1 = cmul(c1, z1, n1); }
                B = cmul(B, z8, c8);
                if (i == 3) d = X; else if (i < 7) d = cmul(d, z2, n2);
                if (i == 2 || i == 6) X = cmul(X, z128, n128);
            }
#pragma unroll
            for (int i = 0; i < 4; i++) {
                const f32x2 B1 = cmul(B, z8, c8), e = cmul(X, zz4, n4), Xn = cmul(X, z128, n128);
                put2(49 + 2 * i, B, 24 + 2 * i, X);
                if (i < 3) B = cmul(B1, z8, c8);
                put2(50 + 2 * i, B1, 25 + 2 * i, e);
                X = Xn;
            }
        }
        FENCE();
        STAMP(2);
        f32x4 ka, kb;
        float k255, k1024;
        {
            u32x4 fa[4][4];
#pragma unroll
            for (int s = 0; s < 4; s++) {
                fa[s][3] = *(const LAS u32x4*)(lds + a_tf + 41 * TBL_ROW + s * 64);
#pragma unroll
                for (int t = 0; t < 2; t++) fa[s][t] = *(const LAS u32x4*)(lds + a_tf + 16 * t * TBL_ROW + s * 64);
            }
            FENCE();
            const f32x2 X9 = cmul(X, z128, n128);
            put2(32, X, 33, X9);
            if (heavy) {
                const f32x2 z256 = csq(z128), n256 = {-z256.y, z256.y};
                f32x2 e = X, o = X9;
#pragma unroll
                for (int i = 0; i < 3; i++) { e = cmul(e, z256, n256); o = cmul(o, z256, n256); put2(34 + 2 * i, e, 35 + 2 * i, o); }
                e = cmul(e, z256, n256);
                put2(40, e, 40, e);
            }
            __builtin_amdgcn_s_setprio(0);
            FENCE();
#pragma unroll
            for (int s = 0; s < 4; s++) fa[s][2] = *(const LAS u32x4*)(lds + a_tf + 32 * TBL_ROW + s * 64);
            __builtin_amdgcn_sched_barrier(0);
            f32x4 k0, k1, k2;
#pragma unroll
            for (int s = 0; s < 4; s++) { k0 = mfma_h(fa[s][0], fa[s][3], s ? k0 : z4); k1 = mfma_h(fa[s][1], fa[s][3], s ? k1 : z4); }
#pragma unroll
            for (int s = 0; s < 4; s++) k2 = mfma_h(fa[s][2], fa[s][3], s ? k2 : z4);
            FENCE();
            const int dlt = h ? -TBL_H : TBL_H;
            LAS char* fo = lds + LDS_FLAG + w * 16;
            const LAS char* fi = lds + LDS_FLAG + (w ^ 4) * 16;
            if (h) {
                *(LAS f32x4*)(lds + a_ln) = k0 * inv; *(LAS f32x4*)(lds + a_ln + 4 * TBL_ROW) = k2 * inv;
                asm volatile("s_waitcnt lgkmcnt(0)" ::: "memory");
                *(volatile LAS u32*)fo = 1;
                STAMP(13);
                u32x4 fl; f32x4 o;
#pragma clang loop unroll(disable)
                for (u32 spin = 0; spin < SPIN_CAP; spin++) {
                    fl = *(volatile LAS u32x4*)fi; o = *(volatile LAS f32x4*)(lds + a_ln + dlt);
                    if (__builtin_amdgcn_readfirstlane(fl[0])) break;
                    __builtin_amdgcn_s_sleep(2);
                }
                ka = k1 * inv + o;
                k255 = __uint_as_float(__builtin_amdgcn_readlane(__float_as_uint(k0[3]), 63)) * inv + __uint_as_float(fl[1]);
                k1024 = __uint_as_float(__builtin_amdgcn_readlane(__float_as_uint(k2[0]), 0)) * inv + __uint_as_float(fl[2]);
            } else {
                *(LAS f32x4*)(lds + a_ln) = k1 * inv;
                const float s0 = __uint_as_float(__builtin_amdgcn_readlane(__float_as_uint(k0[3]), 63)) * inv;
                const float s1 = __uint_as_float(__builtin_amdgcn_readlane(__float_as_uint(k2[0]), 0)) * inv;
                asm volatile("s_waitcnt lgkmcnt(0)" ::: "memory");
                *(volatile LAS u32x4*)fo = u32x4{1, __float_as_uint(s0), __float_as_uint(s1), 0};
                STAMP(13);
                u32 fl; f32x4 o0, o1;
#pragma clang loop unroll(disable)
                for (u32 spin = 0; spin < SPIN_CAP; spin++) {
                    fl = *(volatile LAS u32*)fi; o0 = *(volatile LAS f32x4*)(lds + a_ln + dlt); o1 = *(volatile LAS f32x4*)(lds + a_ln + dlt + 4 * TBL_ROW);
                    if (__builtin_amdgcn_readfirstlane(fl)) break;
                    __builtin_amdgcn_s_sleep(2);
                }
                ka = k0 * inv + o0; kb = k2 * inv + o1;
                k1024 = __uint_as_float(__builtin_amdgcn_readlane(__float_as_uint(kb[0]), 0));
            }
        }
        STAMP(1);
        if (h == 0) {
            const f32x4 v = ka;
            auto sw = __builtin_amdgcn_permlane16_swap(__float_as_uint(v[3]), __float_as_uint(v[3]), false, false);
            const float pd = dppf<0x138>(0.0f, __uint_as_float(sw[1]));
            const float pv = (g & 1) ? __uint_as_float(sw[0]) : pd;
            *(LAS u32x4*)(lds + a_e0) = u32x4{pkbf(v[3], v[2]), pkbf(v[2], v[1]), pkbf(v[1], v[0]), pkbf(v[0], pv)};
            const u32 kk = pkbf(k1024, k1024);
            const u32 f7 = lane == 15 ? kk : 0u, f6 = lane == 14 ? kk : 0u;
            *(LAS u32x4*)(lds + a_e1) = u32x4{f7, f7, f7 | (f6 & 0xffff0000u), f7 | f6};
            const f32x4 u = {lane == 0 ? 0.0f : kb[0], kb[1], kb[2], kb[3]};
            const bool e = q >= 14;
            LAS u32* cw = (LAS u32*)(lds + a_cw);
            cw[0] = pkbf(u[0], dppf<0x138>(0.0f, dppf<0x138>(0.0f, e ? u[3] : u[0])));
#pragma unroll
            for (int i = 1; i < 4; i++) cw[-8 * i] = pkbf(u[i], dppf<0x122>(0.0f, e ? u[i - 1] : u[i]));
        } else {
            const f32x4 v = ka;
            const bool s2 = g < 2;
            const float nw = dppf<0x130>(v[0], v[0]);
            const float nr = dppf<0x12f>(v[0], q == 0 ? v[2] : v[0]);
            const float nA = s2 ? nw : nr;
            const float nB = dppf<0x130>(k1024, q == 0 ? v[0] : v[2]);
            const float x2 = s2 ? v[1] : v[0], x3 = s2 ? v[2] : v[1], x6 = s2 ? v[3] : nA;
            const u32 aa = pkbf(v[0], v[0]), cc = pkbf(x3, x3), a2 = pkbf(v[2], v[2]), b2 = pkbf(v[3], v[3]);
            *(LAS u32x4*)(lds + a_e0) = u32x4{pkbf(nA, x6), pkbf(x6, x3), cc, cc};
            *(LAS u32x4*)(lds + a_e0 + 16) = u32x4{pkbf(x3, x2), pkbf(x2, v[0]), aa, pkbf(v[0], lane == 0 ? k255 : v[0])};
            *(LAS u32x4*)(lds + a_e1) = u32x4{pkbf(nB, nB), pkbf(nB, v[3]), b2, b2};
            *(LAS u32x4*)(lds + a_e1 + 16) = u32x4{pkbf(v[3], v[2]), a2, a2, a2};
        }
        STAMP(4);
        u32x4 A[17];
        const LAS u32* pr = (const LAS u32*)(lds + a_pr);
        auto lda = [&](const int m) { A[m] = u32x4{pr[64 * (16 - m)], pr[64 * (16 - m) + 2], pr[64 * (16 - m) + 4], pr[64 * (16 - m) + 6]}; };
        {
            asm volatile("s_waitcnt lgkmcnt(0)" ::: "memory");
            *(volatile LAS u32*)(lds + LDS_FLAG + w * 16 + 12) = 2;
#pragma clang loop unroll(disable)
            for (u32 spin = 0; spin < SPIN_CAP; spin++)
                if (__builtin_amdgcn_readfirstlane(*(volatile LAS u32*)(lds + LDS_FLAG + (w ^ 4) * 16 + 12)) == 2) break;
            FENCE();
        }
#pragma unroll
        for (int m = 0; m < NPRE; m++) lda(m);
        asm volatile("" :: "v"(warm0) : "memory");
        STAMP(5);
        if (ffn) rawbar();
        rawbar();
        STAMP(7);

        f32x4 acc[4];
        u64 selv, prtv;
        {
            auto ldb = [&](const int k) { return *(const LAS u32x4*)(lds + a_b + 128 * (28 - k)); };
            constexpr int PF = 5;
            auto run = [&](const int hi, const int k0, const int k1, const bool pre, auto&& extra) {
                u32x4 Bf[29];
#pragma unroll
                for (int k = k0; k < k0 + PF; k++) Bf[k] = ldb(k);
                __builtin_amdgcn_sched_barrier(0);
#pragma unroll
                for (int k = k0; k <= k1; k++) {
                    if (pre && NPRE + k < 17) lda(NPRE + k);
                    if (k + PF <= k1) Bf[k + PF] = ldb(k + PF);
                    extra(k);
#pragma unroll
                    for (int i = hi; i >= hi - 1; i--) {
                        const int mp = 4 * i - 12 + k;
                        if (mp >= 0 && mp < 17) acc[i] = mfma_bf(A[mp], Bf[k], mp == 0 ? z4 : acc[i]);
                    }
                    __builtin_amdgcn_sched_barrier(0);
                }
            };
            auto put_y = [&](const int i) { *(LAS f32x4*)(lds + a_y + i * Y_TILE) = acc[i]; };
            if (heavy) {
                const LAS u32* cr = (const LAS u32*)(lds + a_cr);
                u32x4 Wd[4], KT[4], band, S0, S1;
                f32x4 dw[4];
                STAMP(6);
                run(3, 0, 20, true, [&](const int k) {
                    if (k == 11) band = *(const LAS u32x4*)(lds + LDS_GU + (tid & 63) * 16 + h * 1024);
                    if (k >= 11 && k < 15) Wd[k - 11] = *(const LAS u32x4*)(lds + a_u + 512 * (k - 11));
                    if (k >= 15 && k < 18) KT[18 - k] = u32x4{cr[16 * (k - 15)], cr[16 * (k - 15) + 2], cr[16 * (k - 15) + 16], cr[16 * (k - 15) + 18]};
                    if (k == 18) KT[0] = u32x4{cr[48], cr[50], 0u, 0u};
                    FENCE();
                    if (k >= 15 && k < 19) dw[k - 15] = mfma_bf(Wd[k - 15], band, z4);
                    if (k == 18) S0 = u32x4{pkbf(dw[0][0], dw[0][1]), pkbf(dw[0][2], dw[0][3]), pkbf(dw[1][0], dw[1][1]), pkbf(dw[1][2], dw[1][3])};
                    if (k == 20) S1 = u32x4{pkbf(dw[2][0], dw[2][1]), pkbf(dw[2][2], dw[2][3]), pkbf(dw[3][0], dw[3][1]), pkbf(dw[3][2], dw[3][3])};
                });
                acc[3] = mfma_bf(S0, KT[3], acc[3]); acc[2] = mfma_bf(S0, KT[2], acc[2]);
                acc[3] = mfma_bf(S1, KT[1], acc[3]); acc[2] = mfma_bf(S1, KT[0], acc[2]);
                run(1, 8, 28, false, [&](const int k) {
                    if (k == 10) { put_y(3); put_y(2); }
                    if (k == 13) asm volatile("s_waitcnt lgkmcnt(3)\n\ts_barrier" ::: "memory");
                });
                acc[1] = mfma_bf(S0, KT[1], acc[1]); acc[0] = mfma_bf(S0, KT[0], acc[0]);
            } else {
                run(3, 0, 15, true, [&](const int) {});
                run(1, 8, 15, false, [&](const int k) {
                    if (k == 10) { put_y(3); put_y(2); }
                    if (k == 13) bar();
                });
            }
            STAMP(9);
            selv = gld(a.ws, selo); prtv = gld(a.ws, prto);
            put_y(1); put_y(0);
        }
        tag = __builtin_amdgcn_readfirstlane(*(LAS u32*)(lds + LDS_UPD + 48));
        STAMP(8);
        bar();
        if (bad(selv, tag)) {
#pragma clang loop unroll(disable)
            for (u32 spin = 0; spin < SPIN_CAP; spin++) { selv = gld(a.ws, selo); if (!bad(selv, tag)) break; __builtin_amdgcn_s_sleep(2); }
        }
        const u32 sel = h ? __builtin_amdgcn_readlane((u32)selv, 1) : __builtin_amdgcn_readlane((u32)selv, 0);
        const bool own = ((sel & 2047) >= 1024) == heavy;
        if (own) {
            if (bad(prtv, tag)) {
#pragma clang loop unroll(disable)
                for (u32 spin = 0; spin < SPIN_CAP; spin++) { prtv = gld(a.ws, prto); if (!bad(prtv, tag)) break; __builtin_amdgcn_s_sleep(1); }
            }
            const float y = wave_fsum(lane < NFFN ? __uint_as_float((u32)prtv) : 0.0f);
            *(LAS float*)(lds + LDS_UPD + w * 4) = rbf(rbf(y) * bfhi(sel));
        }
        *(LAS u32*)(lds + LDS_UPD + 32 + h * 4) = own ? sel & 2047u : 0x7fffffffu;
        bar();
    } else {
        __builtin_amdgcn_s_setprio(3);
        const int lt = (tid + 64 * jj) & 255;
        const u32 xo = lt * 4096 + G * 8;
        u32 xh = xo + (heavy ? 1024 * 1024 : 0);
        const u32 so = WS_STATS + xcd * 16384 + lt * 32, sh = so + (heavy ? 8192 : 0);
        u32 warm1 = 0, warm2 = 0, warm3 = 0;
        u32x2 xc[4], xl[4];
        u64 sg[4], sl[4], cd[4], ce[4];
        f32x2 xf[4][2], zn[4][2], gt[4][2];
        u32 epoch;
        u32x2 dsk, dskr;
        asm volatile("s_load_dword %0, %2, 0x0\n\ts_load_dwordx2 %1, %3, %4" : "=&s"(epoch), "=&s"(dskr) : "s"(a.ws), "s"(a.dskip), "s"(G * 8));
        auto first = [&]() {
            asm volatile("s_waitcnt lgkmcnt(0)\n\ts_add_u32 %0, %2, 1\n\ts_mov_b64 %1, %3" : "=&s"(tag), "=&s"(dsk) : "s"(epoch), "s"(dskr) : "scc");
        };
        u32x4 bnd;
        if (heavy) bnd = ldg<u32x4>(&lane_tab, (tid & 127) * 32);
        const u32 o_w = heavy ? (jj * 128 + ch * 32 + (lane & 31)) * 1024 + xcd * 128 : 0;
        const bool lnr = !ffn && (ch == 0 || (ch == 1 && j < 14));
        if (lnr) {
            const int lnu = blockIdx.x - 56 * (1 - ch) + 200 * ch - 56 * ch;
            u32x4 xa[8], cb[8];
#pragma unroll
            for (int s = 0; s < 8; s++) xa[s] = ldg<u32x4>(a.x, (lnu * 8 + (q >> 1)) * 1024 + (q & 1) * 64 + g * 16 + 128 * s);
            rawbar();
            warm1 = ldg<u32>(a.x, o_w);
#pragma unroll
            for (int s = 0; s < 8; s++) cb[s] = ldg<u32x4>(a.wr, (q & 2) * 32 + g * 16 + 128 * s);
            const u32x4 ones = {0x3f803f80u, 0x3f803f80u, 0x3f803f80u, 0x3f803f80u};
            const int rr = 2 * g + ((q >> 1) & 1);
            u32 o_st = WS_STATS + ((q & 1) + 2 * (q >> 2)) * 16384 + (lnu * 8 + rr) * 8, o_id = 0xffffu - (lnu * 8 + rr);
            u32 o_cd = (WS_CAND + (lane >> 1) * 4096 + lnu * 16 + (lane & 1) * 8) | (u32)(((15 - lane) >> 31) << 20);
            PIN(o_st); PIN(o_id); PIN(o_cd);
            first();
            f32x4 d0 = z4, d1 = z4, d2 = z4, d3 = z4;
#pragma unroll
            for (int s = 0; s < 8; s++) { d2 = mfma_bf(xa[s], xa[s], d2); d0 = mfma_bf(xa[s], ones, d0); }
            asm volatile("" : "+v"(d0), "+v"(d2));
            __builtin_amdgcn_sched_barrier(0);
#pragma unroll
            for (int s = 0; s < 8; s++) { d1 = mfma_bf(xa[s], cb[s], d1); d3 = mfma_bf(ones, cb[s], d3); }
            STAMP(13);
            const bool hi = q & 2;
            const float da = (q & 1) ? d2[1] : d2[0], db = (q & 1) ? d2[3] : d2[2];
            float sq = (q >> 2) == g ? (hi ? db : da) : 0.0f;
            sq += __uint_as_float(dpp0<0xb1>(__float_as_uint(sq)));
            sq += __uint_as_float(dpp0<0x124>(__float_as_uint(sq)));
            sq += __uint_as_float(dpp0<0x128>(__float_as_uint(sq)));
            const float mean = (hi ? d0[2] + d0[3] : d0[0] + d0[1]) * (1.0f / D_);
            const float var = fmaxf(sq * (1.0f / D_) - mean * mean, 0.0f);
            const float sd = rbf(__builtin_amdgcn_sqrtf(rbf(rbf(var) + 1e-5f)));
            const u32 ms = pkbf(mean, sd);
            bst(true, o_st, ms);
            const float dot = (hi ? d1[3] : d1[0]) + __uint_as_float(dpp0<0x4e>(__float_as_uint(hi ? d1[1] : d1[2])));
            const float sw = d3[0] + __uint_as_float(dpp0<0x4e>(__float_as_uint(d3[0])));
            const float r = (dot - bflo(ms) * sw) * __builtin_amdgcn_rcpf(sd);
            const u32 rb = pkbf(r, r);
            const u32 key = ((rb ^ (((int)rb >> 31) | 0x80000000u)) & 0xffff0000u) | o_id;
            const u32 ko = dpp0<0x4e>(key);
            auto x1 = __builtin_amdgcn_permlane16_swap(max(key, ko), max(key, ko), false, false);
            auto x2 = __builtin_amdgcn_permlane16_swap(min(key, ko), min(key, ko), false, false);
            const u32 a1 = max(x1[0], x1[1]), a2 = max(min(x1[0], x1[1]), max(x2[0], x2[1]));
            auto y1 = __builtin_amdgcn_permlane32_swap(a1, a1, false, false);
            auto y2 = __builtin_amdgcn_permlane32_swap(a2, a2, false, false);
            bst(true, o_cd, (lane & 1) ? max(min(y1[0], y1[1]), max(y2[0], y2[1])) : max(y1[0], y1[1]));
            STAMP(1);
        } else {
            rawbar();
            warm1 = ldg<u32>(a.x, o_w);
        }
        if (!ffn) {
            FENCE();
            *(volatile LAS u32*)(lds + LDS_FLAG + 128 + ch * 4) = 1;
#pragma clang loop unroll(disable)
            for (u32 spin = 0; spin < SPIN_CAP; spin++) {
                const u32x2 f0 = *(volatile LAS u32x2*)(lds + LDS_FLAG + 128);
                if (__builtin_amdgcn_readfirstlane(f0[0] & f0[1])) break;
                __builtin_amdgcn_s_sleep(1);
            }
            FENCE();
        }
        auto second = [&]() {
            if (!lnr) first();
            bst(lane == 0, WS_SEEN + (blockIdx.x * 4 + ch) * 8, 0);
            *(LAS u32*)(lds + LDS_UPD + 48) = tag;
            *(LAS u32x2*)(lds + LDS_U + ch * U_CH + lane * 8) = u32x2{0, 0};
        };
        u32 yb = LDS_Y + (lt >> 6) * Y_TILE + (lt & 3) * 320 + ((lt >> 2) & 15) * 16;
        const u32 a_st = LDS_U + 512 + 8 * lt;
        auto tags4 = [&](const u64 (&v)[4]) { return (bad(v[0], tag) | bad(v[1], tag) | bad(v[2], tag) | bad(v[3], tag)) == 0; };
        auto sweep4 = [&](u64 (&v)[4], const u32 off, const u32 stride) {
            if (!tags4(v)) {
#pragma clang loop unroll(disable)
                for (u32 spin = 0; spin < SPIN_CAP; spin++) {
#pragma unroll
                    for (int i = 0; i < 4; i++) v[i] = gld(a.ws + stride * i, off);
                    if (tags4(v)) break;
                    __builtin_amdgcn_s_sleep(4);
                }
            }
        };
        auto stage = [&](const u64 (&st)[4], const u32x2 (&r)[4], const u32 dst, const bool keep) {
            f32x2 rs[2], nb[2];
#pragma unroll
            for (int p = 0; p < 2; p++) {
                rs[p] = f32x2{__builtin_amdgcn_rcpf(bfhi((u32)st[2 * p])), __builtin_amdgcn_rcpf(bfhi((u32)st[2 * p + 1]))};
                nb[p] = f32x2{bflo((u32)st[2 * p]), bflo((u32)st[2 * p + 1])} * rs[p];
            }
#pragma unroll
            for (int c = 0; c < 4; c++) {
                u32x2 o;
#pragma unroll
                for (int p = 0; p < 2; p++) {
                    const u32 w0 = r[2 * p][c >> 1], w1 = r[2 * p + 1][c >> 1];
                    const f32x2 xv = (c & 1) ? f32x2{bfhi(w0), bfhi(w1)} : f32x2{bflo(w0), bflo(w1)};
                    const f32x2 z = __builtin_elementwise_fma(xv, rs[p], -nb[p]);
                    if (keep) { xf[c][p] = xv; zn[c][p] = z; }
                    o[p] = pkbf(z.x, z.y);
                }
                *(LAS u32x2*)(lds + dst + c * U_CH) = o;
            }
        };
        auto gates = [&]() {
#pragma unroll
            for (int c = 0; c < 4; c++)
#pragma unroll
                for (int p = 0; p < 2; p++) {
                    const f32x2 e = zn[c][p] * -1.44269504089f;
                    const f32x2 d = f32x2{__builtin_amdgcn_exp2f(e.x), __builtin_amdgcn_exp2f(e.y)} + 1.0f;
                    gt[c][p] = zn[c][p] * f32x2{__builtin_amdgcn_rcpf(d.x), __builtin_amdgcn_rcpf(d.y)};
                    PIN(gt[c][p]);
                }
        };
        if (ffn) {
            const int f = xcd * 7 + jj;
            u32 wgu[2][4][8], wdn[4][8];
            const __amdgpu_buffer_rsrc_t rg = __builtin_amdgcn_make_buffer_rsrc((void*)((f & 1) ? a.wu : a.wg), 0, D_ * F_ * 2, 0x00020000);
            const __amdgpu_buffer_rsrc_t ru = __builtin_amdgcn_make_buffer_rsrc((void*)((f & 1) ? a.wg : a.wu), 0, D_ * F_ * 2, 0x00020000);
            const __amdgpu_buffer_rsrc_t rd = __builtin_amdgcn_make_buffer_rsrc((void*)a.wd, 0, D_ * F_ * 2, 0x00020000);
            const u32 og = ((128 * ch + 8 * g) * F_ + 32 * f + 2 * q) * 2, od = ((32 * f + 8 * g) * D_ + 128 * ch + 2 * q) * 2;
#pragma unroll
            for (int m = 0; m < 2; m++)
#pragma unroll
                for (int s = 0; s < 4; s++)
#pragma unroll
                    for (int i = 0; i < 8; i++) wgu[m][s][i] = __builtin_amdgcn_raw_buffer_load_b32(m ? ru : rg, og, (32 * s + i) * F_ * 2, 0);
#pragma unroll
            for (int i = 0; i < 8; i++)
#pragma unroll
                for (int u = 0; u < 4; u++) wdn[u][i] = __builtin_amdgcn_raw_buffer_load_b32(rd, od + ((i & 3) * D_ * 2 + u * 64), (i & 4) * D_ * 2, 0);
            second();
            STAMP(12);
            const u32 co = WS_CAND + xcd * 4096 + lane * 8;
#pragma unroll
            for (int i = 0; i < 4; i++) { cd[i] = gld(a.ws + 512 * i, co); ce[i] = gld(a.ws + 512 * i + 2048, co); }
            u32x4 fg[2][4][2], fd[4][2];
#pragma unroll
            for (int m = 0; m < 3; m++)
#pragma unroll
                for (int s = 0; s < 4; s++)
#pragma unroll
                    for (int p = 0; p < 2; p++) {
                        u32x4 o;
#pragma unroll
                        for (int d = 0; d < 4; d++)
                            o[d] = __builtin_amdgcn_perm(m < 2 ? wgu[m & 1][s][2 * d + 1] : wdn[s][2 * d + 1], m < 2 ? wgu[m & 1][s][2 * d] : wdn[s][2 * d], p ? 0x07060302u : 0x05040100u);
                        PIN(o);
                        if (m < 2) fg[m & 1][s][p] = o; else fd[s][p] = o;
                    }
            u32x4 an[4];
            const u32 o1 = q < 2 ? 0u : 0x3f803f80u;
            const __amdgpu_buffer_rsrc_t rx = __builtin_amdgcn_make_buffer_rsrc((void*)a.x, 0, 2048 * 1024, 0x00020000);
            asm volatile("" :: "v"(warm0), "v"(warm1));
            sweep4(cd, co, 512);
            sweep4(ce, co + 2048, 512);
            STAMP(4);
            u32 ka = max((u32)cd[0], (u32)ce[0]), kb = min((u32)cd[0], (u32)ce[0]);
#pragma unroll
            for (int i = 1; i < 4; i++) {
                const u32 ha = max((u32)cd[i], (u32)ce[i]), hb = min((u32)cd[i], (u32)ce[i]);
                kb = max(min(ka, ha), max(kb, hb)); ka = max(ka, ha);
            }
            const u32 k1 = wave_umax(ka), k2 = wave_umax(ka == k1 ? kb : ka);
            const u32 kk = (lane & 1) ? k2 : k1;
            const u32 row = ~kk & 2047u;
            const u32 xr = q < 2 ? row * 1024 + ch * 256 + g * 16 : 0xfffff000u;
#pragma unroll
            for (int s = 0; s < 4; s++) an[s] = __builtin_amdgcn_raw_buffer_load_b128(rx, xr + 64 * s, 0, 0);
            const u32 sro = WS_STATS + xcd * 16384 + row * 8;
            u64 sr = gld(a.ws, sro);
            STAMP(11);
            if (jj == 0 && ch == 0) {
                const float sv = sigm(__uint_as_float((kk ^ ((~(int)kk >> 31) | 0x80000000u)) & 0xffff0000u));
                bst(lane < 2, WS_SEL + xcd * 128 + lane * 8, row | (pkbf(sv, sv) << 16));
            }
#pragma unroll
            for (int i = 0; i < 4; i++) xc[i] = ldg<u32x2>(a.x, xh + 1024 * i);
#pragma unroll
            for (int i = 0; i < 4; i++) sg[i] = gld(a.ws + 8 * i, sh);
            f32x4 c[2][2];
#pragma unroll
            for (int s = 0; s < 4; s++)
#pragma unroll
                for (int mt = 0; mt < 2; mt++)
#pragma unroll
                    for (int p = 0; p < 2; p++) c[mt][p] = mfma_bf(an[s] | o1, fg[mt][s][p], s ? c[mt][p] : z4);
            if (bad(sr, tag)) {
#pragma clang loop unroll(disable)
                for (u32 spin = 0; spin < SPIN_CAP; spin++) { sr = gld(a.ws, sro); if (!bad(sr, tag)) break; }
            }
            PIN(c[0][0]); PIN(c[1][1]);
            STAMP(2);
            const u32 s0 = __builtin_amdgcn_readlane((u32)sr, 0), s1 = __builtin_amdgcn_readlane((u32)sr, 1);
            LAS float* gu = (LAS float*)(lds + LDS_GU);
            LAS float* gw = gu + (g == 0 ? ch * 128 + 2 * q : 512);
#pragma unroll
            for (int mt = 0; mt < 2; mt++) {
                *(LAS f32x2*)(gw + (mt ^ (f & 1)) * 64) = f32x2{c[mt][0][0], c[mt][1][0]} - bflo(s0) * f32x2{c[mt][0][2], c[mt][1][2]};
                *(LAS f32x2*)(gw + (mt ^ (f & 1)) * 64 + 32) = f32x2{c[mt][0][1], c[mt][1][1]} - bflo(s1) * f32x2{c[mt][0][2], c[mt][1][2]};
            }
            const float rs = __builtin_amdgcn_rcpf(bfhi(lane < 32 ? s0 : s1));
            bar();
            f32x2 gs = {0.0f, 0.0f};
#pragma unroll
            for (int i = 0; i < 4; i++) gs += f32x2{gu[i * 128 + lane], gu[i * 128 + 64 + lane]};
            const u32 gb = pkbf(gs.x * rs, gs.y * rs);
            const float ga = bflo(gb);
            const float hv = rbf(ga * sigm(ga)) * bfhi(gb);
            LAS u16* hb = (LAS u16*)(lds + LDS_H + ch * 128);
            hb[lane] = (u16)pkbf(hv, hv);
            FENCE();
            const u32x4 ah = *(const LAS u32x4*)(hb + (q & 1) * 32 + 8 * g);
            f32x4 y0 = z4, y1 = z4;
#pragma unroll
            for (int u = 0; u < 4; u++) {
                const f32x4 d0 = mfma_bf(ah, fd[u][0], z4), d1 = mfma_bf(ah, fd[u][1], z4);
                y0[0] = g == u ? d0[0] : y0[0]; y0[1] = g == u ? d0[1] : y0[1]; y1[0] = g == u ? d1[0] : y1[0]; y1[1] = g == u ? d1[1] : y1[1];
            }
            STAMP(3);
            const u32 pp = WS_PART + (((32 * ch + 8 * g + (q >> 1)) * NFFN + f) * 4 + 2 * (q & 1)) * 8;
            bst(true, pp, __float_as_uint(y0[0])); bst(true, pp + 8, __float_as_uint(y1[0]));
            bst(true, pp + 128 * NFFN * 32, __float_as_uint(y0[1])); bst(true, pp + 128 * NFFN * 32 + 8, __float_as_uint(y1[1]));
            STAMP(5);
            sweep4(sg, sh, 8);
            u32 t2 = tid;
            PIN(t2);
            stage(sg, xc, LDS_U + 512 + 8 * ((t2 + 64 * jj) & 255), true);
        } else {
            if (!heavy) {
                u32 s1 = ((j - 7) * 4 + ch) * 15, s2 = ((j - 7) * 4 + ch) * 50;
                asm volatile("" : "+s"(s1), "+s"(s2));
                warm3 = ldg<u32>((xcd & 1) ? a.wg : a.wu, __umul24(min(s1 + min(lane, 15), 511u), F_ * 2) + (xcd * 7 + ((xcd & 1) ? 0 : 6)) * 64);
                warm2 = ldg<u32>(a.wd, min(s2 + min(lane, 50), 1791u) * 128 + xcd * 224 * 1024);
                second();
                STAMP(12);
                __builtin_amdgcn_s_sleep(LIGHT_WAIT);
#pragma unroll
                for (int i = 0; i < 4; i++) sg[i] = gld(a.ws + 8 * i, sh);
                sweep4(sg, sh, 8);
                STAMP(4);
#pragma unroll
                for (int i = 0; i < 4; i++) xc[i] = ldg<u32x2>(a.x, xh + 1024 * i);
                asm volatile("" :: "v"(warm0), "v"(warm1), "v"(warm2), "v"(warm3));
                stage(sg, xc, a_st, true);
            } else {
#pragma unroll
                for (int i = 0; i < 4; i++) xc[i] = ldg<u32x2>(a.x, xh + 1024 * i);
#pragma unroll
                for (int i = 0; i < 4; i++) xl[i] = ldg<u32x2>(a.x, xo + 1024 * i);
                *(LAS u32x4*)(lds + LDS_GU + (tid & 127) * 16) = bnd;
                second();
                STAMP(12);
#pragma unroll
                for (int i = 0; i < 4; i++) sg[i] = gld(a.ws + 8 * i, sh);
#pragma unroll
                for (int i = 0; i < 4; i++) sl[i] = gld(a.ws + 8 * i, so);
                sweep4(sg, sh, 8);
                asm volatile("" :: "v"(warm0), "v"(warm1));
                STAMP(4);
                stage(sg, xc, a_st + 2048, true);
                sweep4(sl, so, 8);
                stage(sl, xl, a_st, false);
                if (blockIdx.x == 128) {
#pragma clang loop unroll(disable)
                    for (u32 spin = 0; spin < SPIN_CAP; spin++)
                        if (!(bad(gld(a.ws + WS_SEEN, (tid & 255) * 8), tag) | bad(gld(a.ws + WS_SEEN + 2048, (tid & 255) * 8), tag) | bad(gld(a.ws + WS_SEEN + 4096, (tid & 255) * 8), tag) | bad(gld(a.ws + WS_SEEN + 6144, (tid & 255) * 8), tag))) break;
                }
            }
        }
        STAMP(6);
        bar();
        if (blockIdx.x == 128 && ch == 0) __builtin_amdgcn_raw_buffer_store_b32(tag, rw, lane == 0 ? WS_EPOCH : 0xfffffff0u, 0, 16);
        PIN(yb);
        __builtin_amdgcn_s_setprio(0);
        gates();
#pragma unroll
        for (int c = 0; c < 4; c++) {
            const float dk = (c & 1) ? bfhi(dsk[c >> 1]) : bflo(dsk[c >> 1]);
#pragma unroll
            for (int p = 0; p < 2; p++) { xf[c][p] = __builtin_elementwise_fma(zn[c][p] * dk, gt[c][p], xf[c][p]); PIN(xf[c][p]); }
        }
        __builtin_amdgcn_s_setprio(3);
        STAMP(7);
        const bool early = __builtin_amdgcn_readfirstlane(lt) >= 128;
        rawbar();
        if (!early) rawbar();
        STAMP(8);
        f32x2 yv[4][2];
        u32x2 o[4];
#pragma unroll
        for (int c = 0; c < 4; c++) {
            const f32x4 y = *(const LAS f32x4*)(lds + yb + c * Y_SZ) + *(const LAS f32x4*)(lds + yb + (c + 4) * Y_SZ);
            yv[c][0] = y.lo; yv[c][1] = y.hi;
        }
#pragma unroll
        for (int p = 0; p < 2; p++) {
            f32x2 t[4];
#pragma unroll
            for (int c = 0; c < 4; c++) t[c] = __builtin_elementwise_fma(yv[c][p], gt[c][p], xf[c][p]);
            o[2 * p] = u32x2{pkbf(t[0].x, t[1].x), pkbf(t[2].x, t[3].x)};
            o[2 * p + 1] = u32x2{pkbf(t[0].y, t[1].y), pkbf(t[2].y, t[3].y)};
        }
#pragma unroll
        for (int i = 0; i < 4; i++) stg(a.out, xh + 1024 * i, o[i]);
        STAMP(9);
        if (early) rawbar();
        rawbar();
        STAMP(11);
        const u32x2 rows = *(const LAS u32x2*)(lds + LDS_UPD + 32);
#pragma clang loop unroll(disable)
        for (int t = 0; t < 2; t++) {
            const u32 e = (t ? rows.y : rows.x) - (xh >> 10);
            if (e < 4) {
                const f32x4 up = *(const LAS f32x4*)(lds + LDS_UPD + t * 16);
                float oe[4];
#pragma unroll
                for (int c = 0; c < 4; c++) {
                    auto pick = [&](const f32x2 v0, const f32x2 v1) { const float s0 = (e & 2) ? v1.x : v0.x, s1 = (e & 2) ? v1.y : v0.y; return (e & 1) ? s1 : s0; };
                    const float z = pick(zn[c][0], zn[c][1]), g0 = pick(gt[c][0], gt[c][1]), dk = (c & 1) ? bfhi(dsk[c >> 1]) : bflo(dsk[c >> 1]);
                    const float zz = rbf(rbf(z) + up[c]), g1 = zz * sigm(zz);
                    oe[c] = __builtin_fmaf(z * dk, g1 - g0, __builtin_fmaf(pick(yv[c][0], yv[c][1]), g1, pick(xf[c][0], xf[c][1])));
                }
                stg(a.out, xh + e * 1024, u32x2{pkbf(oe[0], oe[1]), pkbf(oe[2], oe[3])});
            }
        }
    }
    STAMPS_OUT;
}
#define ROLE_OF(b) (((b) >> 3) >= 16 ? 0 : (((b) >> 3) < 7 ? 2 : 1))
#define NWAVES 12

extern "C" void kernel_launch(void* const* d_in, const int* in_sizes, int n_in, void* d_out, int out_size, void* d_ws,
                              size_t ws_size, hipStream_t stream) {
    Args a;
    a.x = (const u16*)d_in[0]; a.are = (const u16*)d_in[1]; a.aim = (const u16*)d_in[2]; a.bre = (const u16*)d_in[3];
    a.bim = (const u16*)d_in[4]; a.cre = (const u16*)d_in[5]; a.cim = (const u16*)d_in[6]; a.logdt = (const u16*)d_in[7];
    a.dskip = (const u16*)d_in[8]; a.wr = (const u16*)d_in[10]; a.wg = (const u16*)d_in[11]; a.wu = (const u16*)d_in[12];
    a.wd = (const u16*)d_in[13]; a.out = (u16*)d_out; a.ws = (char*)d_ws;
    (void)hipFuncSetAttribute((const void*)s4_mod_fused, hipFuncAttributeMaxDynamicSharedMemorySize, LDS_TOTAL);
    hipLaunchKernelGGL(s4_mod_fused, dim3(256), dim3(768), LDS_TOTAL, stream, a);
}
```

```cpp
#include <hip/hip_runtime.h>
#include <stdint.h>

typedef unsigned short u16;
typedef unsigned int u32;
typedef unsigned long long u64;
typedef float f32x2 __attribute__((ext_vector_type(2)));
typedef float f32x4 __attribute__((ext_vector_type(4)));
typedef u32 u32x2 __attribute__((ext_vector_type(2)));
typedef u32 u32x3 __attribute__((ext_vector_type(3)));
typedef u32 u32x4 __attribute__((ext_vector_type(4)));
typedef __bf16 bf16x2 __attribute__((ext_vector_type(2)));
typedef __bf16 bf16x8 __attribute__((ext_vector_type(8)));
typedef _Float16 f16x2 __attribute__((ext_vector_type(2)));
typedef _Float16 f16x8 __attribute__((ext_vector_type(8)));
#define GAS __attribute__((address_space(1)))
#define LAS __attribute__((address_space(3)))
#define CAS __attribute__((address_space(4)))
typedef GAS u64 gu64;
typedef GAS u32 gu32;
#define RLX_AGENT __ATOMIC_RELAXED, __HIP_MEMORY_SCOPE_AGENT

constexpr int D_ = 512, F_ = 1792;
constexpr int NFFN = 56;
constexpr u32 WS_EPOCH = 0, WS_SEL = 1024, WS_SEEN = 2048, WS_STATS = 16384, WS_CAND = WS_STATS + 8 * 16384, WS_PART = WS_CAND + 8 * 4096;
constexpr u32 WS_END = WS_PART + 2 * 128 * NFFN * 32;
constexpr u32 WS_DBG = 1u << 21;
constexpr int LDS_LN = 0;
constexpr int LDS_H = 1024;
constexpr int LDS_UPD = LDS_H + 512;
constexpr int LDS_JUNK = LDS_UPD + 64;
constexpr int LDS_TBL = 2048;
constexpr int TBL_ROW = 1040, TBL_H = 57 * TBL_ROW;
constexpr int LDS_Y = LDS_TBL + TBL_H, Y_TILE = 1280, Y_SZ = 4 * Y_TILE;
constexpr int LDS_U = LDS_TBL + 2 * TBL_H;
constexpr int U_CH = (256 + 2048) * 2;
constexpr int LDS_GU = LDS_U + 4 * U_CH;
constexpr int LDS_P = LDS_GU + 2048 + 512, P_SZ = 5344;
constexpr int C_OFF = 4416 + 64 + 112, C_SZ = 432;
constexpr int LDS_TOTAL = LDS_P + 4 * P_SZ;
constexpr int LDS_FLAG = LDS_JUNK + 128;

struct LaneTab { u32 v[128][8]; };
constexpr LaneTab make_tab() {
    LaneTab t{};
    for (int h = 0; h < 2; h++)
        for (int l = 0; l < 64; l++) {
            const int q = l & 15, g = l >> 4;
            u32* o = t.v[h * 64 + l];
            for (int d = 0; d < 4; d++) {
                const int k0 = 8 * g + 2 * d - q - 8, k1 = k0 + 1;
                o[d] = ((k0 >= h - 4 && k0 <= 4 - h) ? 0x3f80u : 0u) | ((k1 >= h - 4 && k1 <= 4 - h) ? 0x3f800000u : 0u);
            }
            o[4] = 4 * (h ? 1080 - 8 * q - (g < 2 ? 256 + 128 * g : 256 * g) : 1084 - 128 * (g >> 1) - 8 * q - 4 * (g & 1));
            o[5] = h ? (g < 2 ? 4432 : o[4] - 512)
                     : (l < 16 ? l * 16 : (l < 20 ? 4096 + l * 16 : (l < 28 ? C_OFF + ((l - 20) >> 2) * C_SZ + 256 + (l & 3) * 16 : 4432)));
            o[6] = (g < 2 || l == 32) ? C_OFF + (q & 1) * C_SZ + 4 * (64 - (q & 1) - 32 * g - (q >> 1)) : C_OFF - 4;
            o[7] = 0;
        }
    return t;
}
static __device__ const LaneTab lane_tab = make_tab();
#define LIGHT_WAIT 16
#define GATE_PRIO 3
#define SLOWWARM 2
#define SELFWARM 0
#define NPRE 17
constexpr u32 SPIN_CAP = 1u << 16;
constexpr int CODE_BACK = 0, CODE_LINES = 150;

struct Args {
    const u16 *x, *are, *aim, *bre, *bim, *cre, *cim, *logdt, *dskip, *wr, *wg, *wu, *wd;
    u16* out;
    char* ws;
};

template <typename T> __device__ __forceinline__ T ldg(const void* base, u32 off) { return *(const GAS T*)((const GAS char*)base + off); }
template <typename T> __device__ __forceinline__ void stg(void* base, u32 off, T v) { *(GAS T*)((GAS char*)base + off) = v; }
__device__ __forceinline__ u64 gld(const char* base, u32 off) { return __hip_atomic_load((gu64*)((GAS char*)base + off), RLX_AGENT); }
__device__ __forceinline__ void gst(char* base, u32 off, u32 tag, u32 v) { __hip_atomic_store((gu64*)((GAS char*)base + off), ((u64)tag << 32) | v, RLX_AGENT); }
__device__ __forceinline__ float bflo(u32 v) { return __uint_as_float(v << 16); }
__device__ __forceinline__ float bfhi(u32 v) { return __uint_as_float(v & 0xffff0000u); }
__device__ __forceinline__ u32 pkbf(float a, float b) { f32x2 v = {a, b}; return __builtin_bit_cast(u32, __builtin_convertvector(v, bf16x2)); }
__device__ __forceinline__ u32 pkh(f32x2 v) { return __builtin_bit_cast(u32, __builtin_convertvector(v, f16x2)); }
__device__ __forceinline__ float rbf(float a) { return bflo(pkbf(a, a)); }
__device__ __forceinline__ f32x2 cmul(f32x2 a, f32x2 z, f32x2 zn) {
    return __builtin_elementwise_fma(a.yx, zn, a * z.xx);
}
__device__ __forceinline__ f32x2 csq(f32x2 a) { return cmul(a, a, f32x2{-a.y, a.y}); }
__device__ __forceinline__ void rawbar() { asm volatile("s_barrier" ::: "memory"); }
__device__ __forceinline__ void bar() { asm volatile("s_waitcnt lgkmcnt(0)\n\ts_barrier" ::: "memory"); }
#define PIN(x) asm volatile("" : "+v"(x))
#define FENCE() asm volatile("" ::: "memory")
__device__ __forceinline__ f32x4 mfma_bf(u32x4 a, u32x4 b, f32x4 c) {
    return __builtin_amdgcn_mfma_f32_16x16x32_bf16(__builtin_bit_cast(bf16x8, a), __builtin_bit_cast(bf16x8, b), c, 0, 0, 0);
}
__device__ __forceinline__ f32x4 mfma_h(u32x4 a, u32x4 b, f32x4 c) {
    return __builtin_amdgcn_mfma_f32_16x16x32_f16(__builtin_bit_cast(f16x8, a), __builtin_bit_cast(f16x8, b), c, 0, 0, 0);
}
template <int CTRL> __device__ __forceinline__ float dppf(float old, float src) {
    return __uint_as_float(__builtin_amdgcn_update_dpp(__float_as_uint(old), __float_as_uint(src), CTRL, 0xf, 0xf, false));
}
template <int CTRL, int RM = 0xf> __device__ __forceinline__ u32 dpp0(u32 src) { return __builtin_amdgcn_update_dpp(0u, src, CTRL, RM, 0xf, false); }
__device__ __forceinline__ u32 row_umax(u32 v) {
    v = max(v, dpp0<0x128>(v)); v = max(v, dpp0<0x124>(v)); v = max(v, dpp0<0x122>(v)); v = max(v, dpp0<0x121>(v));
    return v;
}
__device__ __forceinline__ u32 wave_umax(u32 v) {
    v = row_umax(v); v = max(v, dpp0<0x142, 0xa>(v)); v = max(v, dpp0<0x143, 0xc>(v));
    return __builtin_amdgcn_readlane(v, 63);
}
__device__ __forceinline__ float wave_fsum(float f) {
    f += __uint_as_float(dpp0<0x128>(__float_as_uint(f))); f += __uint_as_float(dpp0<0x124>(__float_as_uint(f)));
    f += __uint_as_float(dpp0<0x122>(__float_as_uint(f))); f += __uint_as_float(dpp0<0x121>(__float_as_uint(f)));
    f += __uint_as_float(dpp0<0x142, 0xa>(__float_as_uint(f))); f += __uint_as_float(dpp0<0x143, 0xc>(__float_as_uint(f)));
    return __uint_as_float(__builtin_amdgcn_readlane(__float_as_uint(f), 63));
}
__device__ __forceinline__ float sigm(float z) { return __builtin_amdgcn_rcpf(1.0f + __builtin_amdgcn_exp2f(z * -1.44269504089f)); }
__device__ __forceinline__ u64 bad(u64 granule, u32 tag) { return __builtin_amdgcn_ballot_w64((u32)(granule >> 32) != tag); }

#define STAMP(i)
#define STAMPS_DECL
#define STAMPS_OUT

__global__ void __launch_bounds__(768, 3) s4_mod_fused(Args a) {
    extern __shared__ __attribute__((aligned(16))) char smem[];
    LAS char* const lds = (LAS char*)smem;
    const int tid = threadIdx.x, lane = tid & 63;
    const int w = __builtin_amdgcn_readfirstlane(tid >> 6);
    const int xcd = blockIdx.x & 7, j = blockIdx.x >> 3, jj = j & 15;
    const bool heavy = j >= 16, ffn = j < 7;
    const int q = lane & 15, g = lane >> 4;
    const int ch = w & 3, h = w >> 2;
    const int G = xcd * 16 + jj;
    const f32x4 z4 = {0, 0, 0, 0};
    STAMPS_DECL;
    STAMP(0);
    u32 tag;
    const __amdgpu_buffer_rsrc_t rw = __builtin_amdgcn_make_buffer_rsrc((void*)a.ws, 0, WS_END, 0x00020000);
    auto bst = [&](const bool on, const u32 off, const u32 v) {
        __builtin_amdgcn_raw_buffer_store_b64(u32x2{v, tag}, rw, on ? off : 0xfffffff0u, 0, 16);
    };

    u32 p_ldt, p_are, p_aim, p_bre, p_bim, p_cre, p_cim, warm0;
    u32x3 adr;
    {
        warm0 = 0;
        if (h < 2)
            warm0 = ldg<u32>((const char*)(__builtin_amdgcn_s_getpc() & ~0x7full), min(j * 5 + (tid & 7), CODE_LINES - 1) * 128);
        asm volatile("" :: "s"(a.x), "s"(a.are), "s"(a.aim), "s"(a.bre), "s"(a.bim), "s"(a.cre), "s"(a.cim), "s"(a.logdt), "s"(a.dskip), "s"(a.wr),
                     "s"(a.wg), "s"(a.wu), "s"(a.wd), "s"(a.out), "s"(a.ws) : "memory");
    }
    if (h < 2) {
        const u32 po = ((4 * G + ch) * 128 + 64 * h + lane) * 2;
        p_ldt = ldg<u16>(a.logdt, (4 * G + ch) * 2);
        p_are = ldg<u16>(a.are, po); p_aim = ldg<u16>(a.aim, po); p_bre = ldg<u16>(a.bre, po); p_bim = ldg<u16>(a.bim, po);
        p_cre = ldg<u16>(a.cre, po); p_cim = ldg<u16>(a.cim, po);
        adr = ldg<u32x4>(&lane_tab, (h * 64 + lane) * 32 + 16).xyz;
        *(LAS u32x4*)(lds + LDS_FLAG + (lane < 2 ? 128 + lane * 16 : w * 16)) = u32x4{0, 0, 0, 0};
    }
    int h2 = h;
    asm volatile("" : "+s"(h2) :: "memory");

    if (h2 < 2) {
        bar();
        const u32 tw = LDS_TBL + h * TBL_H + ch * 256, g16 = lane & 48, q16 = q * 16;
        u32 a_tf = __umul24(q, TBL_ROW) + g16 + tw, a_ln = __umul24(g, TBL_ROW) + q16 + tw, a_y = __umul24(g, 320) + q16 + (LDS_Y + w * Y_SZ);
        u32 a_cr = g16 - 4 * q + (LDS_P + ch * P_SZ + C_OFF + h * C_SZ + 60), a_pr = 2 * g16 - 4 * q + (LDS_P + ch * P_SZ + 188 - 128 * h);
        u32 a_u = 2 * q16 + g16 + (LDS_U + ch * U_CH + 464 + 16 * h), a_b = a_u + (16 - 80 * h - (heavy ? 0 : 2048));
        u32 prto = min(lane, NFFN - 1) * 32 + (WS_PART + (h * 128 + G) * NFFN * 32 + ch * 8), selo = (lane & 1) * 8 + (WS_SEL + xcd * 128);
        PIN(a_tf); PIN(a_ln); PIN(a_y); PIN(a_pr); PIN(a_cr); PIN(a_u); PIN(a_b); PIN(prto); PIN(selo);
        __builtin_amdgcn_sched_barrier(0);
        if (h == 0) __builtin_amdgcn_s_setprio(2); else __builtin_amdgcn_s_setprio(1);
        STAMP(11);
        PIN(p_ldt); PIN(p_are); PIN(p_aim); PIN(p_bre); PIN(p_bim); PIN(p_cre); PIN(p_cim);
        STAMP(3);
        const float ld = bflo(p_ldt);
        const float t0 = ld * 1.44269504089f;
        const float te = __builtin_fmaf(ld, 1.92596303e-8f, __builtin_fmaf(ld, 1.44269504089f, -t0));
        const float ex = __builtin_amdgcn_exp2f(t0);
        float dt = rbf(__builtin_fmaf(ex * te, 0.69314718056f, ex));
        const float lr = (dt * 1.44269504089f) * bflo(p_are);
        const float ai = dt * bflo(p_aim);
        const float th = ai * 0.15915494309f;
        const float tl = __builtin_fmaf(ai, 6.4206382e-9f, __builtin_fmaf(ai, 0.15915494309f, -th));
        const float bi = bflo(p_bim);
        const f32x2 W = cmul(f32x2{bflo(p_cre), bflo(p_cim)}, f32x2{bflo(p_bre), bi}, f32x2{-bi, bi}) * dt;
        const float gp = fmaxf(lr, 0.0f);
        const int en = __builtin_amdgcn_frexp_expf(fmaxf(fabsf(W.x), fabsf(W.y))) + (int)(2168.0f * gp);
        const int E = min(max((int)wave_umax((u32)(en + 201)) - 200, -100), 120);
        const float sA = __builtin_amdgcn_ldexpf(__builtin_amdgcn_exp2f(120.0f * gp), 13 - E);
        const float inv = __uint_as_float((u32)(E - 13 + 127) << 23);
        const float t1 = __builtin_amdgcn_fractf(th), e1 = __builtin_amdgcn_exp2f(lr);
        const f32x2 z1 = f32x2{__builtin_amdgcn_cosf(t1), __builtin_amdgcn_sinf(t1)} * e1;
        const float t7 = __builtin_fmaf(128.0f, tl, __builtin_amdgcn_fractf(128.0f * th)), e7 = __builtin_amdgcn_exp2f(128.0f * lr);
        const f32x2 z128 = f32x2{__builtin_amdgcn_cosf(t7), __builtin_amdgcn_sinf(t7)} * e7;
        const f32x2 z2 = csq(z1), zz4 = csq(z2), z8 = csq(zz4);
        f32x2 n1 = {-z1.y, z1.y}, n2 = {-z2.y, z2.y}, n4 = {-zz4.y, zz4.y}, n128 = {-z128.y, z128.y}, c8 = {z8.y, -z8.y};
        f32x2 X = W * sA, B = {__builtin_amdgcn_exp2f(-120.0f * gp), 0.0f};
        PIN(adr);
        const u32 a_e0 = adr[0] + (LDS_P + ch * P_SZ), a_e1 = adr[1] + (LDS_P + ch * P_SZ), a_cw = adr[2] + (LDS_P + ch * P_SZ);
        STAMP(12);
        {
            const u32 m0v = tw + (u32)(size_t)lds;
            asm volatile("s_mov_b32 m0, %0\n\ts_nop 0" :: "s"(m0v) : "memory");
        }
        auto put2 = [&](const int r0, const f32x2 v0, const int r1, const f32x2 v1) {
            asm volatile("ds_write_addtid_b32 %0 offset:%c2\n\tds_write_addtid_b32 %1 offset:%c3" :: "v"(pkh(v0)), "v"(pkh(v1)), "i"(r0 * TBL_ROW), "i"(r1 * TBL_ROW));
        };
        {
            f32x2 c0 = X, X1 = cmul(X, z128, n128), c1 = X1;
            X = cmul(X1, z128, n128);
            f32x2 d = X;
#pragma unroll
            for (int i = 0; i < 8; i++) {
                put2(i, c0, 8 + i, c1);
                put2(41 + i, B, 16 + i, d);
                if (i < 7) { c0 = cmul(c0, z1, n1); c1 = cmul(c1, z1, n1); }
                B = cmul(B, z8, c8);
                if (i == 3) d = X; else if (i < 7) d = cmul(d, z2, n2);
                if (i == 2 || i == 6) X = cmul(X, z128, n128);
            }
#pragma unroll
            for (int i = 0; i < 4; i++) {
                const f32x2 B1 = cmul(B, z8, c8), e = cmul(X, zz4, n4), Xn = cmul(X, z128, n128);
                put2(49 + 2 * i, B, 24 + 2 * i, X);
                if (i < 3) B = cmul(B1, z8, c8);
                put2(50 + 2 * i, B1, 25 + 2 * i, e);
                X = Xn;
            }
        }
        FENCE();
        STAMP(2);
        f32x4 ka, kb;
        float k255, k1024;
        {
            u32x4 fa[4][4];
#pragma unroll
            for (int s = 0; s < 4; s++) {
                fa[s][3] = *(const LAS u32x4*)(lds + a_tf + 41 * TBL_ROW + s * 64);
#pragma unroll
                for (int t = 0; t < 2; t++) fa[s][t] = *(const LAS u32x4*)(lds + a_tf + 16 * t * TBL_ROW + s * 64);
            }
            FENCE();
            const f32x2 X9 = cmul(X, z128, n128);
            put2(32, X, 33, X9);
            if (heavy) {
                const f32x2 z256 = csq(z128), n256 = {-z256.y, z256.y};
                f32x2 e = X, o = X9;
#pragma unroll
                for (int i = 0; i < 3; i++) { e = cmul(e, z256, n256); o = cmul(o, z256, n256); put2(34 + 2 * i, e, 35 + 2 * i, o); }
                e = cmul(e, z256, n256);
                put2(40, e, 40, e);
            }
            __builtin_amdgcn_s_setprio(0);
            FENCE();
#pragma unroll
            for (int s = 0; s < 4; s++) fa[s][2] = *(const LAS u32x4*)(lds + a_tf + 32 * TBL_ROW + s * 64);
            __builtin_amdgcn_sched_barrier(0);
            f32x4 k0, k1, k2;
#pragma unroll
            for (int s = 0; s < 4; s++) { k0 = mfma_h(fa[s][0], fa[s][3], s ? k0 : z4); k1 = mfma_h(fa[s][1], fa[s][3], s ? k1 : z4); }
#pragma unroll
            for (int s = 0; s < 4; s++) k2 = mfma_h(fa[s][2], fa[s][3], s ? k2 : z4);
            FENCE();
            const int dlt = h ? -TBL_H : TBL_H;
            LAS char* fo = lds + LDS_FLAG + w * 16;
            const LAS char* fi = lds + LDS_FLAG + (w ^ 4) * 16;
            if (h) {
                *(LAS f32x4*)(lds + a_ln) = k0 * inv; *(LAS f32x4*)(lds + a_ln + 4 * TBL_ROW) = k2 * inv;
                asm volatile("s_waitcnt lgkmcnt(0)" ::: "memory");
                *(volatile LAS u32*)fo = 1;
                STAMP(13);
                u32x4 fl; f32x4 o;
#pragma clang loop unroll(disable)
                for (u32 spin = 0; spin < SPIN_CAP; spin++) {
                    fl = *(volatile LAS u32x4*)fi; o = *(volatile LAS f32x4*)(lds + a_ln + dlt);
                    if (__builtin_amdgcn_readfirstlane(fl[0])) break;
                    __builtin_amdgcn_s_sleep(2);
                }
                ka = k1 * inv + o;
                k255 = __uint_as_float(__builtin_amdgcn_readlane(__float_as_uint(k0[3]), 63)) * inv + __uint_as_float(fl[1]);
                k1024 = __uint_as_float(__builtin_amdgcn_readlane(__float_as_uint(k2[0]), 0)) * inv + __uint_as_float(fl[2]);
            } else {
                *(LAS f32x4*)(lds + a_ln) = k1 * inv;
                const float s0 = __uint_as_float(__builtin_amdgcn_readlane(__float_as_uint(k0[3]), 63)) * inv;
                const float s1 = __uint_as_float(__builtin_amdgcn_readlane(__float_as_uint(k2[0]), 0)) * inv;
                asm volatile("s_waitcnt lgkmcnt(0)" ::: "memory");
                *(volatile LAS u32x4*)fo = u32x4{1, __float_as_uint(s0), __float_as_uint(s1), 0};
                STAMP(13);
                u32 fl; f32x4 o0, o1;
#pragma clang loop unroll(disable)
                for (u32 spin = 0; spin < SPIN_CAP; spin++) {
                    fl = *(volatile LAS u32*)fi; o0 = *(volatile LAS f32x4*)(lds + a_ln + dlt); o1 = *(volatile LAS f32x4*)(lds + a_ln + dlt + 4 * TBL_ROW);
                    if (__builtin_amdgcn_readfirstlane(fl)) break;
                    __builtin_amdgcn_s_sleep(2);
                }
                ka = k0 * inv + o0; kb = k2 * inv + o1;
                k1024 = __uint_as_float(__builtin_amdgcn_readlane(__float_as_uint(kb[0]), 0));
            }
        }
        STAMP(1);
        if (h == 0) {
            const f32x4 v = ka;
            auto sw = __builtin_amdgcn_permlane16_swap(__float_as_uint(v[3]), __float_as_uint(v[3]), false, false);
            const float pd = dppf<0x138>(0.0f, __uint_as_float(sw[1]));
            const float pv = (g & 1) ? __uint_as_float(sw[0]) : pd;
            *(LAS u32x4*)(lds + a_e0) = u32x4{pkbf(v[3], v[2]), pkbf(v[2], v[1]), pkbf(v[1], v[0]), pkbf(v[0], pv)};
            const u32 kk = pkbf(k1024, k1024);
            const u32 f7 = lane == 15 ? kk : 0u, f6 = lane == 14 ? kk : 0u;
            *(LAS u32x4*)(lds + a_e1) = u32x4{f7, f7, f7 | (f6 & 0xffff0000u), f7 | f6};
            const f32x4 u = {lane == 0 ? 0.0f : kb[0], kb[1], kb[2], kb[3]};
            const bool e = q >= 14;
            LAS u32* cw = (LAS u32*)(lds + a_cw);
            cw[0] = pkbf(u[0], dppf<0x138>(0.0f, dppf<0x138>(0.0f, e ? u[3] : u[0])));
#pragma unroll
            for (int i = 1; i < 4; i++) cw[-8 * i] = pkbf(u[i], dppf<0x122>(0.0f, e ? u[i - 1] : u[i]));
        } else {
            const f32x4 v = ka;
            const bool s2 = g < 2;
            const float nw = dppf<0x130>(v[0], v[0]);
            const float nr = dppf<0x12f>(v[0], q == 0 ? v[2] : v[0]);
            const float nA = s2 ? nw : nr;
            const float nB = dppf<0x130>(k1024, q == 0 ? v[0] : v[2]);
            const float x2 = s2 ? v[1] : v[0], x3 = s2 ? v[2] : v[1], x6 = s2 ? v[3] : nA;
            const u32 aa = pkbf(v[0], v[0]), cc = pkbf(x3, x3), a2 = pkbf(v[2], v[2]), b2 = pkbf(v[3], v[3]);
            *(LAS u32x4*)(lds + a_e0) = u32x4{pkbf(nA, x6), pkbf(x6, x3), cc, cc};
            *(LAS u32x4*)(lds + a_e0 + 16) = u32x4{pkbf(x3, x2), pkbf(x2, v[0]), aa, pkbf(v[0], lane == 0 ? k255 : v[0])};
            *(LAS u32x4*)(lds + a_e1) = u32x4{pkbf(nB, nB), pkbf(nB, v[3]), b2, b2};
            *(LAS u32x4*)(lds + a_e1 + 16) = u32x4{pkbf(v[3], v[2]), a2, a2, a2};
        }
        STAMP(4);
        u32x4 A[17];
        const LAS u32* pr = (const LAS u32*)(lds + a_pr);
        auto lda = [&](const int m) { A[m] = u32x4{pr[64 * (16 - m)], pr[64 * (16 - m) + 2], pr[64 * (16 - m) + 4], pr[64 * (16 - m) + 6]}; };
        {
            asm volatile("s_waitcnt lgkmcnt(0)" ::: "memory");
            *(volatile LAS u32*)(lds + LDS_FLAG + w * 16 + 12) = 2;
#pragma clang loop unroll(disable)
            for (u32 spin = 0; spin < SPIN_CAP; spin++)
                if (__builtin_amdgcn_readfirstlane(*(volatile LAS u32*)(lds + LDS_FLAG + (w ^ 4) * 16 + 12)) >= 2) break;
            FENCE();
        }
#pragma unroll
        for (int m = 0; m < NPRE; m++) lda(m);
        asm volatile("" :: "v"(warm0) : "memory");
        STAMP(5);
        if (ffn) rawbar();
        rawbar();
        STAMP(7);

        f32x4 acc[4];
        u64 selv, prtv;
        {
            auto ldb = [&](const int k) { return *(const LAS u32x4*)(lds + a_b + 128 * (28 - k)); };
            constexpr int PF = 5;
            auto run = [&](const int hi, const int k0, const int k1, const bool pre, auto&& extra) {
                u32x4 Bf[29];
#pragma unroll
                for (int k = k0; k < k0 + PF; k++) Bf[k] = ldb(k);
                __builtin_amdgcn_sched_barrier(0);
#pragma unroll
                for (int k = k0; k <= k1; k++) {
                    if (pre && NPRE + k < 17) lda(NPRE + k);
                    if (k + PF <= k1) Bf[k + PF] = ldb(k + PF);
                    extra(k);
#pragma unroll
                    for (int i = hi; i >= hi - 1; i--) {
                        const int mp = 4 * i - 12 + k;
                        if (mp >= 0 && mp < 17) acc[i] = mfma_bf(A[mp], Bf[k], mp == 0 ? z4 : acc[i]);
                    }
                    __builtin_amdgcn_sched_barrier(0);
                }
            };
            auto put_y = [&](const int i) { *(LAS f32x4*)(lds + a_y + i * Y_TILE) = acc[i]; };
            if (heavy) {
                const LAS u32* cr = (const LAS u32*)(lds + a_cr);
                u32x4 Wd[4], KT[4], band, S0, S1;
                f32x4 dw[4];
                STAMP(6);
                run(3, 0, 20, true, [&](const int k) {
                    if (k == 11) band = *(const LAS u32x4*)(lds + LDS_GU + (tid & 63) * 16 + h * 1024);
                    if (k >= 11 && k < 15) Wd[k - 11] = *(const LAS u32x4*)(lds + a_u + 512 * (k - 11));
                    if (k >= 15 && k < 18) KT[18 - k] = u32x4{cr[16 * (k - 15)], cr[16 * (k - 15) + 2], cr[16 * (k - 15) + 16], cr[16 * (k - 15) + 18]};
                    if (k == 18) KT[0] = u32x4{cr[48], cr[50], 0u, 0u};
                    FENCE();
                    if (k >= 15 && k < 19) dw[k - 15] = mfma_bf(Wd[k - 15], band, z4);
                    if (k == 18) S0 = u32x4{pkbf(dw[0][0], dw[0][1]), pkbf(dw[0][2], dw[0][3]), pkbf(dw[1][0], dw[1][1]), pkbf(dw[1][2], dw[1][3])};
                    if (k == 20) S1 = u32x4{pkbf(dw[2][0], dw[2][1]), pkbf(dw[2][2], dw[2][3]), pkbf(dw[3][0], dw[3][1]), pkbf(dw[3][2], dw[3][3])};
                });
                acc[3] = mfma_bf(S0, KT[3], acc[3]); acc[2] = mfma_bf(S0, KT[2], acc[2]);
                acc[3] = mfma_bf(S1, KT[1], acc[3]); acc[2] = mfma_bf(S1, KT[0], acc[2]);
                run(1, 8, 28, false, [&](const int k) {
                    if (k == 10) { put_y(3); put_y(2); }
                    if (k == 13) { asm volatile("s_waitcnt lgkmcnt(3)" ::: "memory"); *(volatile LAS u32*)(lds + LDS_FLAG + w * 16 + 12) = 3; }
                });
                acc[1] = mfma_bf(S0, KT[1], acc[1]); acc[0] = mfma_bf(S0, KT[0], acc[0]);
            } else {
                run(3, 0, 15, true, [&](const int) {});
                run(1, 8, 15, false, [&](const int k) {
                    if (k == 10) { put_y(3); put_y(2); }
                    if (k == 13) { asm volatile("s_waitcnt lgkmcnt(0)" ::: "memory"); *(volatile LAS u32*)(lds + LDS_FLAG + w * 16 + 12) = 3; }
                });
            }
            STAMP(9);
            selv = gld(a.ws, selo); prtv = gld(a.ws, prto);
            put_y(1); put_y(0);
        }
        tag = __builtin_amdgcn_readfirstlane(*(LAS u32*)(lds + LDS_UPD + 48));
        STAMP(8);
        bar();
        if (bad(selv, tag)) {
#pragma clang loop unroll(disable)
            for (u32 spin = 0; spin < SPIN_CAP; spin++) { selv = gld(a.ws, selo); if (!bad(selv, tag)) break; __builtin_amdgcn_s_sleep(2); }
        }
        const u32 sel = h ? __builtin_amdgcn_readlane((u32)selv, 1) : __builtin_amdgcn_readlane((u32)selv, 0);
        const bool own = ((sel & 2047) >= 1024) == heavy;
        if (own) {
            if (bad(prtv, tag)) {
#pragma clang loop unroll(disable)
                for (u32 spin = 0; spin < SPIN_CAP; spin++) { prtv = gld(a.ws, prto); if (!bad(prtv, tag)) break; __builtin_amdgcn_s_sleep(1); }
            }
            const float y = wave_fsum(lane < NFFN ? __uint_as_float((u32)prtv) : 0.0f);
            *(LAS float*)(lds + LDS_UPD + w * 4) = rbf(rbf(y) * bfhi(sel));
        }
        *(LAS u32*)(lds + LDS_UPD + 32 + h * 4) = own ? sel & 2047u : 0x7fffffffu;
        bar();
    } else {
        __builtin_amdgcn_s_setprio(3);
        const int lt = (tid + 64 * jj) & 255;
        const u32 xo = lt * 4096 + G * 8;
        u32 xh = xo + (heavy ? 1024 * 1024 : 0);
        const u32 so = WS_STATS + xcd * 16384 + lt * 32, sh = so + (heavy ? 8192 : 0);
        u32 warm1 = 0, warm2 = 0, warm3 = 0;
        u32x2 xc[4], xl[4];
        u64 sg[4], sl[4];
        f32x2 xf[4][2], zn[4][2], gt[4][2];
        u32 epoch;
        u32x2 dsk, dskr;
        asm volatile("s_load_dword %0, %2, 0x0\n\ts_load_dwordx2 %1, %3, %4" : "=&s"(epoch), "=&s"(dskr) : "s"(a.ws), "s"(a.dskip), "s"(G * 8));
        auto first = [&]() {
            asm volatile("s_waitcnt lgkmcnt(0)\n\ts_add_u32 %0, %2, 1\n\ts_mov_b64 %1, %3" : "=&s"(tag), "=&s"(dsk) : "s"(epoch), "s"(dskr) : "scc");
        };
        u32x4 bnd;
        const bool lnr = !ffn && !heavy && (ch < 2 || j < 14);
        if (lnr) {
            const int lnu = blockIdx.x - 56 + (ch < 2 ? 72 * ch : 32 + 56 * ch);
            u32x4 xa[8], cb[8];
#pragma unroll
            for (int s = 0; s < 8; s++) xa[s] = ldg<u32x4>(a.x, (lnu * 8 + (q >> 1)) * 1024 + (q & 1) * 64 + g * 16 + 128 * s);
            rawbar();
#pragma unroll
            for (int s = 0; s < 8; s++) cb[s] = ldg<u32x4>(a.wr, (q & 2) * 32 + g * 16 + 128 * s);
            const u32x4 ones = {0x3f803f80u, 0x3f803f80u, 0x3f803f80u, 0x3f803f80u};
            const int rr = 2 * g + ((q >> 1) & 1);
            u32 o_st = WS_STATS + ((q & 1) + 2 * (q >> 2)) * 16384 + (lnu * 8 + rr) * 8, o_id = 0xffffu - (lnu * 8 + rr);
            u32 o_cd = (WS_CAND + (lane >> 1) * 4096 + lnu * 16 + (lane & 1) * 8) | (u32)(((15 - lane) >> 31) << 20);
            PIN(o_st); PIN(o_id); PIN(o_cd);
            first();
            f32x4 d0 = z4, d1 = z4, d2 = z4, d3 = z4;
#pragma unroll
            for (int s = 0; s < 8; s++) { d2 = mfma_bf(xa[s], xa[s], d2); d0 = mfma_bf(xa[s], ones, d0); }
            asm volatile("" : "+v"(d0), "+v"(d2));
            __builtin_amdgcn_sched_barrier(0);
#pragma unroll
            for (int s = 0; s < 8; s++) { d1 = mfma_bf(xa[s], cb[s], d1); d3 = mfma_bf(ones, cb[s], d3); }
            STAMP(13);
            const bool hi = q & 2;
            const float da = (q & 1) ? d2[1] : d2[0], db = (q & 1) ? d2[3] : d2[2];
            float sq = (q >> 2) == g ? (hi ? db : da) : 0.0f;
            sq += __uint_as_float(dpp0<0xb1>(__float_as_uint(sq)));
            sq += __uint_as_float(dpp0<0x124>(__float_as_uint(sq)));
            sq += __uint_as_float(dpp0<0x128>(__float_as_uint(sq)));
            const float mean = (hi ? d0[2] + d0[3] : d0[0] + d0[1]) * (1.0f / D_);
            const float var = fmaxf(sq * (1.0f / D_) - mean * mean, 0.0f);
            const float sd = rbf(__builtin_amdgcn_sqrtf(rbf(rbf(var) + 1e-5f)));
            const u32 ms = pkbf(mean, sd);
            bst(true, o_st, ms);
            const float dot = (hi ? d1[3] : d1[0]) + __uint_as_float(dpp0<0x4e>(__float_as_uint(hi ? d1[1] : d1[2])));
            const float sw = d3[0] + __uint_as_float(dpp0<0x4e>(__float_as_uint(d3[0])));
            const float r = (dot - bflo(ms) * sw) * __builtin_amdgcn_rcpf(sd);
            const u32 rb = pkbf(r, r);
            const u32 key = ((rb ^ (((int)rb >> 31) | 0x80000000u)) & 0xffff0000u) | o_id;
            const u32 ko = dpp0<0x4e>(key);
            auto x1 = __builtin_amdgcn_permlane16_swap(max(key, ko), max(key, ko), false, false);
            auto x2 = __builtin_amdgcn_permlane16_swap(min(key, ko), min(key, ko), false, false);
            const u32 a1 = max(x1[0], x1[1]), a2 = max(min(x1[0], x1[1]), max(x2[0], x2[1]));
            auto y1 = __builtin_amdgcn_permlane32_swap(a1, a1, false, false);
            auto y2 = __builtin_amdgcn_permlane32_swap(a2, a2, false, false);
            bst(true, o_cd, (lane & 1) ? max(min(y1[0], y1[1]), max(y2[0], y2[1])) : max(y1[0], y1[1]));
            STAMP(1);
        } else if (heavy) {
            warm1 = ldg<u32>(a.x, (jj * 128 + ch * 32 + (lane & 31)) * 1024 + xcd * 128);
            bnd = ldg<u32x4>(&lane_tab, (tid & 127) * 32);
            rawbar();
            const u32 c0 = (xcd * 448) & ~127;
            const u16* wp = (jj & 8) ? a.wu : a.wg;
            warm2 = ldg<u32>(wp, __umul24((jj & 7) * 64 + ch * 16 + q, F_ * 2) + c0 + ((384 - c0 - (u32)(size_t)wp + min(g, SLOWWARM - 1) * 128) & 384));
        } else rawbar();
        if (!ffn && !heavy) {
            FENCE();
            *(volatile LAS u32*)(lds + LDS_FLAG + 128 + ch * 4) = 1;
#pragma clang loop unroll(disable)
            for (u32 spin = 0; spin < SPIN_CAP; spin++) {
                const u32x4 f0 = *(volatile LAS u32x4*)(lds + LDS_FLAG + 128);
                if (__builtin_amdgcn_readfirstlane(f0[0] & f0[1] & f0[2] & f0[3])) break;
                __builtin_amdgcn_s_sleep(1);
            }
            FENCE();
        }
        auto second = [&]() {
            if (!lnr) first();
            bst(lane == 0, WS_SEEN + (blockIdx.x * 4 + ch) * 8, 0);
            *(LAS u32*)(lds + LDS_UPD + 48) = tag;
            *(LAS u32x2*)(lds + LDS_U + ch * U_CH + lane * 8) = u32x2{0, 0};
        };
        u32 yb = LDS_Y + (lt >> 6) * Y_TILE + (lt & 3) * 320 + ((lt >> 2) & 15) * 16;
        const u32 a_st = LDS_U + 512 + 8 * lt;
        auto tags4 = [&](const u64 (&v)[4]) { return (bad(v[0], tag) | bad(v[1], tag) | bad(v[2], tag) | bad(v[3], tag)) == 0; };
        auto ld4 = [&](u64 (&v)[4], const u32 off) {
            const u32x4 lo = __builtin_amdgcn_raw_buffer_load_b128(rw, off, 0, 16), hi = __builtin_amdgcn_raw_buffer_load_b128(rw, off + 16, 0, 16);
            v[0] = lo[0] | ((u64)lo[1] << 32); v[1] = lo[2] | ((u64)lo[3] << 32); v[2] = hi[0] | ((u64)hi[1] << 32); v[3] = hi[2] | ((u64)hi[3] << 32);
        };
        auto wait4 = [&](u64 (&v)[4], const u32 off) {
            if (!tags4(v)) {
#pragma clang loop unroll(disable)
                for (u32 spin = 0; spin < SPIN_CAP; spin++) {
                    FENCE();
                    ld4(v, off);
                    if (tags4(v)) break;
                    __builtin_amdgcn_s_sleep(4);
                }
            }
        };
        auto stage = [&](const u64 (&st)[4], const u32x2 (&r)[4], const u32 dst, const bool keep) {
            f32x2 rs[2], nb[2];
#pragma unroll
            for (int p = 0; p < 2; p++) {
                rs[p] = f32x2{__builtin_amdgcn_rcpf(bfhi((u32)st[2 * p])), __builtin_amdgcn_rcpf(bfhi((u32)st[2 * p + 1]))};
                nb[p] = f32x2{bflo((u32)st[2 * p]), bflo((u32)st[2 * p + 1])} * rs[p];
            }
#pragma unroll
            for (int c = 0; c < 4; c++) {
                u32x2 o;
#pragma unroll
                for (int p = 0; p < 2; p++) {
                    const u32 w0 = r[2 * p][c >> 1], w1 = r[2 * p + 1][c >> 1];
                    const f32x2 xv = (c & 1) ? f32x2{bfhi(w0), bfhi(w1)} : f32x2{bflo(w0), bflo(w1)};
                    const f32x2 z = __builtin_elementwise_fma(xv, rs[p], -nb[p]);
                    if (keep) { xf[c][p] = xv; zn[c][p] = z; }
                    o[p] = pkbf(z.x, z.y);
                }
                *(LAS u32x2*)(lds + dst + c * U_CH) = o;
            }
        };
        auto gates = [&]() {
#pragma unroll
            for (int c = 0; c < 4; c++)
#pragma unroll
                for (int p = 0; p < 2; p++) {
                    const f32x2 e = zn[c][p] * -1.44269504089f;
                    const f32x2 d = f32x2{__builtin_amdgcn_exp2f(e.x), __builtin_amdgcn_exp2f(e.y)} + 1.0f;
                    gt[c][p] = zn[c][p] * f32x2{__builtin_amdgcn_rcpf(d.x), __builtin_amdgcn_rcpf(d.y)};
                    PIN(gt[c][p]);
                }
        };
        if (ffn) {
            const int f = xcd * 7 + jj;
            u32 wgu[2][4][8], wdn[4][8];
            const __amdgpu_buffer_rsrc_t rg = __builtin_amdgcn_make_buffer_rsrc((void*)((f & 1) ? a.wu : a.wg), 0, D_ * F_ * 2, 0x00020000);
            const __amdgpu_buffer_rsrc_t ru = __builtin_amdgcn_make_buffer_rsrc((void*)((f & 1) ? a.wg : a.wu), 0, D_ * F_ * 2, 0x00020000);
            const __amdgpu_buffer_rsrc_t rd = __builtin_amdgcn_make_buffer_rsrc((void*)a.wd, 0, D_ * F_ * 2, 0x00020000);
            const u32 og = ((128 * ch + 8 * g) * F_ + 32 * f + 2 * q) * 2, od = ((32 * f + 8 * g) * D_ + 128 * ch + 2 * q) * 2;
#pragma unroll
            for (int m = 0; m < 2; m++)
#pragma unroll
                for (int s = 0; s < 4; s++)
#pragma unroll
                    for (int i = 0; i < 8; i++) wgu[m][s][i] = __builtin_amdgcn_raw_buffer_load_b32(m ? ru : rg, og, (32 * s + i) * F_ * 2, 0);
            const u32 co = WS_CAND + xcd * 4096 + lane * 16;
            u32x4 cd[4];
            auto ldc = [&]() {
#pragma unroll
                for (int i = 0; i < 4; i++) cd[i] = __builtin_amdgcn_raw_buffer_load_b128(rw, co + 1024 * i, 0, 16);
            };
            auto badc = [&]() {
                u64 nb = 0;
#pragma unroll
                for (int i = 0; i < 4; i++) nb |= __builtin_amdgcn_ballot_w64(cd[i][1] != tag) | __builtin_amdgcn_ballot_w64(cd[i][3] != tag);
                return nb;
            };
            FENCE();
            ldc();
            FENCE();
#pragma unroll
            for (int i = 0; i < 8; i++)
#pragma unroll
                for (int u = 0; u < 4; u++) wdn[u][i] = __builtin_amdgcn_raw_buffer_load_b32(rd, od + ((i & 3) * D_ * 2 + u * 64), (i & 4) * D_ * 2, 0);
            second();
            u32 pp = WS_PART + (((32 * ch + 8 * g + (q >> 1)) * NFFN + f) * 4 + 2 * (q & 1)) * 8;
            PIN(pp);
            STAMP(12);
            if (badc()) {
#pragma clang loop unroll(disable)
                for (u32 spin = 0; spin < SPIN_CAP; spin++) { FENCE(); ldc(); if (!badc()) break; __builtin_amdgcn_s_sleep(2); }
            }
            STAMP(4);
            u32 ka = cd[0][0], kb = cd[0][2];
#pragma unroll
            for (int i = 1; i < 4; i++) { kb = max(min(ka, cd[i][0]), max(kb, cd[i][2])); ka = max(ka, cd[i][0]); }
            const u32 k1 = wave_umax(ka), k2 = wave_umax(ka == k1 ? kb : ka);
            const u32 kk = (lane & 1) ? k2 : k1, row = ~kk & 2047u;
            const __amdgpu_buffer_rsrc_t rx = __builtin_amdgcn_make_buffer_rsrc((void*)a.x, 0, 2048 * 1024, 0x00020000);
            const u32 xr = q < 2 ? row * 1024 + ch * 256 + g * 16 : 0xfffff000u;
            u32x4 an[4];
#pragma unroll
            for (int s = 0; s < 4; s++) an[s] = __builtin_amdgcn_raw_buffer_load_b128(rx, xr + 64 * s, 0, 0);
            const u32 sro = WS_STATS + xcd * 16384 + row * 8;
            u64 sr = gld(a.ws, sro);
            if (jj == 0 && ch == 0) {
                const float sv = sigm(__uint_as_float((kk ^ ((~(int)kk >> 31) | 0x80000000u)) & 0xffff0000u));
                bst(lane < 2, WS_SEL + xcd * 128 + lane * 8, row | (pkbf(sv, sv) << 16));
            }
            STAMP(11);
            u32x4 fg[2][4][2], fd[4][2];
#pragma unroll
            for (int m = 0; m < 3; m++)
#pragma unroll
                for (int s = 0; s < 4; s++)
#pragma unroll
                    for (int p = 0; p < 2; p++) {
                        u32x4 o;
#pragma unroll
                        for (int d = 0; d < 4; d++)
                            o[d] = __builtin_amdgcn_perm(m < 2 ? wgu[m & 1][s][2 * d + 1] : wdn[s][2 * d + 1], m < 2 ? wgu[m & 1][s][2 * d] : wdn[s][2 * d], p ? 0x07060302u : 0x05040100u);
                        PIN(o);
                        if (m < 2) fg[m & 1][s][p] = o; else fd[s][p] = o;
                    }
            asm volatile("" :: "v"(warm1), "v"(warm2));
            const u32 o1 = q < 2 ? 0u : 0x3f803f80u;
            f32x4 c[2][2];
#pragma unroll
            for (int s = 0; s < 4; s++)
#pragma unroll
                for (int mt = 0; mt < 2; mt++)
#pragma unroll
                    for (int p = 0; p < 2; p++) c[mt][p] = mfma_bf(an[s] | o1, fg[mt][s][p], s ? c[mt][p] : z4);
            if (bad(sr, tag)) {
#pragma clang loop unroll(disable)
                for (u32 spin = 0; spin < SPIN_CAP; spin++) { sr = gld(a.ws, sro); if (!bad(sr, tag)) break; }
            }
            PIN(c[0][0]); PIN(c[0][1]); PIN(c[1][0]); PIN(c[1][1]);
            FENCE();
#pragma unroll
            for (int i = 0; i < 4; i++) xc[i] = ldg<u32x2>(a.x, xh + 1024 * i);
            ld4(sg, sh);
            STAMP(2);
            const u32 s0 = __builtin_amdgcn_readlane((u32)sr, 0), s1 = __builtin_amdgcn_readlane((u32)sr, 1);
            LAS float* gu = (LAS float*)(lds + LDS_GU);
            LAS float* gw = gu + (g == 0 ? ch * 128 + 2 * q : 512);
#pragma unroll
            for (int mt = 0; mt < 2; mt++) {
                *(LAS f32x2*)(gw + (mt ^ (f & 1)) * 64) = f32x2{c[mt][0][0], c[mt][1][0]} - bflo(s0) * f32x2{c[mt][0][2], c[mt][1][2]};
                *(LAS f32x2*)(gw + (mt ^ (f & 1)) * 64 + 32) = f32x2{c[mt][0][1], c[mt][1][1]} - bflo(s1) * f32x2{c[mt][0][2], c[mt][1][2]};
            }
            const float rs = __builtin_amdgcn_rcpf(bfhi(lane < 32 ? s0 : s1));
            bar();
            f32x2 gs = {0.0f, 0.0f};
#pragma unroll
            for (int i = 0; i < 4; i++) gs += f32x2{gu[i * 128 + lane], gu[i * 128 + 64 + lane]};
            const u32 gb = pkbf(gs.x * rs, gs.y * rs);
            const float ga = bflo(gb);
            const float hv = rbf(ga * sigm(ga)) * bfhi(gb);
            LAS u16* hb = (LAS u16*)(lds + LDS_H + ch * 128);
            hb[lane] = (u16)pkbf(hv, hv);
            FENCE();
            const u32x4 ah = *(const LAS u32x4*)(hb + (q & 1) * 32 + 8 * g);
            f32x4 y0 = z4, y1 = z4;
#pragma unroll
            for (int u = 0; u < 4; u++) {
                const f32x4 d0 = mfma_bf(ah, fd[u][0], z4), d1 = mfma_bf(ah, fd[u][1], z4);
                y0[0] = g == u ? d0[0] : y0[0]; y0[1] = g == u ? d0[1] : y0[1]; y1[0] = g == u ? d1[0] : y1[0]; y1[1] = g == u ? d1[1] : y1[1];
            }
            STAMP(3);
            bst(true, pp, __float_as_uint(y0[0])); bst(true, pp + 8, __float_as_uint(y1[0]));
            bst(true, pp + 128 * NFFN * 32, __float_as_uint(y0[1])); bst(true, pp + 128 * NFFN * 32 + 8, __float_as_uint(y1[1]));
            STAMP(5);
            asm volatile("" : "+v"(sg[0]), "+v"(sg[1]), "+v"(sg[2]), "+v"(sg[3]) :: "memory");
            wait4(sg, sh);
            u32 t2 = tid;
            PIN(t2);
            stage(sg, xc, LDS_U + 512 + 8 * ((t2 + 64 * jj) & 255), true);
        } else {
            if (!heavy) {
                u32 s1 = ((j - 7) * 4 + ch) * 15, s2 = ((j - 7) * 4 + ch) * 50;
                asm volatile("" : "+s"(s1), "+s"(s2));
                warm3 = ldg<u32>((xcd & 1) ? a.wg : a.wu, __umul24(min(s1 + min(lane, 15), 511u), F_ * 2) + (xcd * 7 + ((xcd & 1) ? 0 : 6)) * 64);
                warm2 = ldg<u32>(a.wd, min(s2 + min(lane, 50), 1791u) * 128 + xcd * 224 * 1024);
                second();
                STAMP(12);
                __builtin_amdgcn_s_sleep(LIGHT_WAIT);
                ld4(sg, sh);
                wait4(sg, sh);
                STAMP(4);
#pragma unroll
                for (int i = 0; i < 4; i++) xc[i] = ldg<u32x2>(a.x, xh + 1024 * i);
                asm volatile("" :: "v"(warm2), "v"(warm3));
                stage(sg, xc, a_st, true);
            } else {
#pragma unroll
                for (int i = 0; i < 4; i++) xc[i] = ldg<u32x2>(a.x, xh + 1024 * i);
#pragma unroll
                for (int i = 0; i < 4; i++) xl[i] = ldg<u32x2>(a.x, xo + 1024 * i);
                *(LAS u32x4*)(lds + LDS_GU + (tid & 127) * 16) = bnd;
                second();
                STAMP(12);
                ld4(sg, sh);
                ld4(sl, so);
                wait4(sg, sh);
                asm volatile("" :: "v"(warm1), "v"(warm2));
                STAMP(4);
                stage(sg, xc, a_st + 2048, true);
                wait4(sl, so);
                stage(sl, xl, a_st, false);
                if (blockIdx.x == 128) {
#pragma clang loop unroll(disable)
                    for (u32 spin = 0; spin < SPIN_CAP; spin++)
                        if (!(bad(gld(a.ws + WS_SEEN, (tid & 255) * 8), tag) | bad(gld(a.ws + WS_SEEN + 2048, (tid & 255) * 8), tag) | bad(gld(a.ws + WS_SEEN + 4096, (tid & 255) * 8), tag) | bad(gld(a.ws + WS_SEEN + 6144, (tid & 255) * 8), tag))) break;
                }
            }
        }
        STAMP(6);
        bar();
        if (blockIdx.x == 128 && ch == 0) __builtin_amdgcn_raw_buffer_store_b32(tag, rw, lane == 0 ? WS_EPOCH : 0xfffffff0u, 0, 16);
        PIN(yb);
        __builtin_amdgcn_s_setprio(GATE_PRIO);
        gates();
        __builtin_amdgcn_s_setprio(3);
        STAMP(7);
        const bool early = __builtin_amdgcn_readfirstlane(lt) >= 128;
        if (early) {
#pragma clang loop unroll(disable)
            for (u32 spin = 0; spin < SPIN_CAP; spin++)
                if (!__builtin_amdgcn_ballot_w64(*(volatile LAS u32*)(lds + LDS_FLAG + (lane & 7) * 16 + 12) != 3)) break;
            FENCE();
        } else rawbar();
        STAMP(8);
        f32x2 yv[4][2];
        u32x2 o[4];
#pragma unroll
        for (int c = 0; c < 4; c++) {
            const float dk = (c & 1) ? bfhi(dsk[c >> 1]) : bflo(dsk[c >> 1]);
            const f32x4 ya = *(const LAS f32x4*)(lds + yb + c * Y_SZ), yc = *(const LAS f32x4*)(lds + yb + (c + 4) * Y_SZ);
            yv[c][0] = __builtin_elementwise_fma(zn[c][0], f32x2{dk, dk}, ya.lo) + yc.lo;
            yv[c][1] = __builtin_elementwise_fma(zn[c][1], f32x2{dk, dk}, ya.hi) + yc.hi;
        }
#pragma unroll
        for (int p = 0; p < 2; p++) {
            f32x2 t[4];
#pragma unroll
            for (int c = 0; c < 4; c++) t[c] = __builtin_elementwise_fma(yv[c][p], gt[c][p], xf[c][p]);
            o[2 * p] = u32x2{pkbf(t[0].x, t[1].x), pkbf(t[2].x, t[3].x)};
            o[2 * p + 1] = u32x2{pkbf(t[0].y, t[1].y), pkbf(t[2].y, t[3].y)};
        }
#pragma unroll
        for (int i = 0; i < 4; i++) stg(a.out, xh + 1024 * i, o[i]);
        STAMP(9);
        if (early) rawbar();
        rawbar();
        STAMP(11);
        const u32x2 rows = *(const LAS u32x2*)(lds + LDS_UPD + 32);
#pragma clang loop unroll(disable)
        for (int t = 0; t < 2; t++) {
            const u32 e = (t ? rows.y : rows.x) - (xh >> 10);
            if (e < 4) {
                const f32x4 up = *(const LAS f32x4*)(lds + LDS_UPD + t * 16);
                float oe[4];
#pragma unroll
                for (int c = 0; c < 4; c++) {
                    auto pick = [&](const f32x2 v0, const f32x2 v1) { const float s0 = (e & 2) ? v1.x : v0.x, s1 = (e & 2) ? v1.y : v0.y; return (e & 1) ? s1 : s0; };
                    const float zz = rbf(rbf(pick(zn[c][0], zn[c][1])) + up[c]);
                    oe[c] = __builtin_fmaf(pick(yv[c][0], yv[c][1]), zz * sigm(zz), pick(xf[c][0], xf[c][1]));
                }
                stg(a.out, xh + e * 1024, u32x2{pkbf(oe[0], oe[1]), pkbf(oe[2], oe[3])});
            }
        }
    }
    STAMPS_OUT;
}
#define ROLE_OF(b) (((b) >> 3) >= 16 ? 0 : (((b) >> 3) < 7 ? 2 : 1))
#define NWAVES 12

extern "C" void kernel_launch(void* const* d_in, const int* in_sizes, int n_in, void* d_out, int out_size, void* d_ws,
                              size_t ws_size, hipStream_t stream) {
    Args a;
    a.x = (const u16*)d_in[0]; a.are = (const u16*)d_in[1]; a.aim = (const u16*)d_in[2]; a.bre = (const u16*)d_in[3];
    a.bim = (const u16*)d_in[4]; a.cre = (const u16*)d_in[5]; a.cim = (const u16*)d_in[6]; a.logdt = (const u16*)d_in[7];
    a.dskip = (const u16*)d_in[8]; a.wr = (const u16*)d_in[10]; a.wg = (const u16*)d_in[11]; a.wu = (const u16*)d_in[12];
    a.wd = (const u16*)d_in[13]; a.out = (u16*)d_out; a.ws = (char*)d_ws;
    (void)hipFuncSetAttribute((const void*)s4_mod_fused, hipFuncAttributeMaxDynamicSharedMemorySize, LDS_TOTAL);
    hipLaunchKernelGGL(s4_mod_fused, dim3(256), dim3(768), LDS_TOTAL, stream, a);
}
```

```cpp
#include <hip/hip_runtime.h>
#include <stdint.h>

typedef unsigned short u16;
typedef unsigned int u32;
typedef unsigned long long u64;
typedef float f32x2 __attribute__((ext_vector_type(2)));
typedef float f32x4 __attribute__((ext_vector_type(4)));
typedef u32 u32x2 __attribute__((ext_vector_type(2)));
typedef u32 u32x3 __attribute__((ext_vector_type(3)));
typedef u32 u32x4 __attribute__((ext_vector_type(4)));
typedef __bf16 bf16x2 __attribute__((ext_vector_type(2)));
typedef __bf16 bf16x8 __attribute__((ext_vector_type(8)));
typedef _Float16 f16x2 __attribute__((ext_vector_type(2)));
typedef _Float16 f16x8 __attribute__((ext_vector_type(8)));
#define GAS __attribute__((address_space(1)))
#define LAS __attribute__((address_space(3)))
#define CAS __attribute__((address_space(4)))
typedef GAS u64 gu64;
typedef GAS u32 gu32;
#define RLX_AGENT __ATOMIC_RELAXED, __HIP_MEMORY_SCOPE_AGENT

constexpr int D_ = 512, F_ = 1792;
constexpr int NFFN = 56;
constexpr u32 WS_EPOCH = 0, WS_SEL = 1024, WS_SEEN = 2048, WS_STATS = 16384, WS_CAND = WS_STATS + 8 * 16384, WS_PART = WS_CAND + 8 * 4096;
constexpr u32 WS_END = WS_PART + 2 * 128 * NFFN * 32;
constexpr u32 WS_DBG = 1u << 21;
constexpr int LDS_LN = 0;
constexpr int LDS_H = 1024;
constexpr int LDS_UPD = LDS_H + 512;
constexpr int LDS_JUNK = LDS_UPD + 64;
constexpr int LDS_TBL = 2048;
constexpr int TBL_ROW = 1040, TBL_H = 57 * TBL_ROW;
constexpr int LDS_Y = LDS_TBL + TBL_H, Y_TILE = 1280, Y_SZ = 4 * Y_TILE;
constexpr int LDS_U = LDS_TBL + 2 * TBL_H;
constexpr int U_CH = (256 + 2048) * 2;
constexpr int LDS_GU = LDS_U + 4 * U_CH;
constexpr int LDS_P = LDS_GU + 2048 + 512, P_SZ = 5344;
constexpr int C_OFF = 4416 + 64 + 112, C_SZ = 432;
constexpr int LDS_TOTAL = LDS_P + 4 * P_SZ;
constexpr int LDS_FLAG = LDS_JUNK + 128;

struct LaneTab { u32 v[128][8]; };
constexpr LaneTab make_tab() {
    LaneTab t{};
    for (int h = 0; h < 2; h++)
        for (int l = 0; l < 64; l++) {
            const int q = l & 15, g = l >> 4;
            u32* o = t.v[h * 64 + l];
            for (int d = 0; d < 4; d++) {
                const int k0 = 8 * g + 2 * d - q - 8, k1 = k0 + 1;
                o[d] = ((k0 >= h - 4 && k0 <= 4 - h) ? 0x3f80u : 0u) | ((k1 >= h - 4 && k1 <= 4 - h) ? 0x3f800000u : 0u);
            }
            o[4] = 4 * (h ? 1080 - 8 * q - (g < 2 ? 256 + 128 * g : 256 * g) : 1084 - 128 * (g >> 1) - 8 * q - 4 * (g & 1));
            o[5] = h ? (g < 2 ? 4432 : o[4] - 512)
                     : (l < 16 ? l * 16 : (l < 20 ? 4096 + l * 16 : (l < 28 ? C_OFF + ((l - 20) >> 2) * C_SZ + 256 + (l & 3) * 16 : 4432)));
            o[6] = (g < 2 || l == 32) ? C_OFF + (q & 1) * C_SZ + 4 * (64 - (q & 1) - 32 * g - (q >> 1)) : C_OFF - 4;
            o[7] = 0;
        }
    return t;
}
static __device__ const LaneTab lane_tab = make_tab();
#define LIGHT_WAIT 80
#define PHASES 2
#define HG 1
#define EARLYP 0
#define YSLEEP 1
#define DLATE 1
#define POLL2 0
#define ROT 1
#define HW 1
#define NPRE 17
constexpr u32 SPIN_CAP = 1u << 16;
constexpr int CODE_BACK = 0, CODE_LINES = 150;

struct Args {
    const u16 *x, *are, *aim, *bre, *bim, *cre, *cim, *logdt, *dskip, *wr, *wg, *wu, *wd;
    u16* out;
    char* ws;
};

template <typename T> __device__ __forceinline__ T ldg(const void* base, u32 off) { return *(const GAS T*)((const GAS char*)base + off); }
template <typename T> __device__ __forceinline__ void stg(void* base, u32 off, T v) { *(GAS T*)((GAS char*)base + off) = v; }
__device__ __forceinline__ u64 gld(const char* base, u32 off) { return __hip_atomic_load((gu64*)((GAS char*)base + off), RLX_AGENT); }
__device__ __forceinline__ void gst(char* base, u32 off, u32 tag, u32 v) { __hip_atomic_store((gu64*)((GAS char*)base + off), ((u64)tag << 32) | v, RLX_AGENT); }
__device__ __forceinline__ float bflo(u32 v) { return __uint_as_float(v << 16); }
__device__ __forceinline__ float bfhi(u32 v) { return __uint_as_float(v & 0xffff0000u); }
__device__ __forceinline__ u32 pkbf(float a, float b) { f32x2 v = {a, b}; return __builtin_bit_cast(u32, __builtin_convertvector(v, bf16x2)); }
__device__ __forceinline__ u32 pkh(f32x2 v) { return __builtin_bit_cast(u32, __builtin_convertvector(v, f16x2)); }
__device__ __forceinline__ float rbf(float a) { return bflo(pkbf(a, a)); }
__device__ __forceinline__ f32x2 cmul(f32x2 a, f32x2 z, f32x2 zn) {
    return __builtin_elementwise_fma(a.yx, zn, a * z.xx);
}
__device__ __forceinline__ f32x2 csq(f32x2 a) { return cmul(a, a, f32x2{-a.y, a.y}); }
__device__ __forceinline__ void rawbar() { asm volatile("s_barrier" ::: "memory"); }
__device__ __forceinline__ void bar() { asm volatile("s_waitcnt lgkmcnt(0)\n\ts_barrier" ::: "memory"); }
#define PIN(x) asm volatile("" : "+v"(x))
#define FENCE() asm volatile("" ::: "memory")
__device__ __forceinline__ f32x4 mfma_bf(u32x4 a, u32x4 b, f32x4 c) {
    return __builtin_amdgcn_mfma_f32_16x16x32_bf16(__builtin_bit_cast(bf16x8, a), __builtin_bit_cast(bf16x8, b), c, 0, 0, 0);
}
__device__ __forceinline__ f32x4 mfma_h(u32x4 a, u32x4 b, f32x4 c) {
    return __builtin_amdgcn_mfma_f32_16x16x32_f16(__builtin_bit_cast(f16x8, a), __builtin_bit_cast(f16x8, b), c, 0, 0, 0);
}
template <int CTRL> __device__ __forceinline__ float dppf(float old, float src) {
    return __uint_as_float(__builtin_amdgcn_update_dpp(__float_as_uint(old), __float_as_uint(src), CTRL, 0xf, 0xf, false));
}
template <int CTRL, int RM = 0xf> __device__ __forceinline__ u32 dpp0(u32 src) { return __builtin_amdgcn_update_dpp(0u, src, CTRL, RM, 0xf, false); }
__device__ __forceinline__ u32 row_umax(u32 v) {
    v = max(v, dpp0<0x128>(v)); v = max(v, dpp0<0x124>(v)); v = max(v, dpp0<0x122>(v)); v = max(v, dpp0<0x121>(v));
    return v;
}
__device__ __forceinline__ u32 wave_umax(u32 v) {
    v = row_umax(v); v = max(v, dpp0<0x142, 0xa>(v)); v = max(v, dpp0<0x143, 0xc>(v));
    return __builtin_amdgcn_readlane(v, 63);
}
__device__ __forceinline__ float wave_fsum(float f) {
    f += __uint_as_float(dpp0<0x128>(__float_as_uint(f))); f += __uint_as_float(dpp0<0x124>(__float_as_uint(f)));
    f += __uint_as_float(dpp0<0x122>(__float_as_uint(f))); f += __uint_as_float(dpp0<0x121>(__float_as_uint(f)));
    f += __uint_as_float(dpp0<0x142, 0xa>(__float_as_uint(f))); f += __uint_as_float(dpp0<0x143, 0xc>(__float_as_uint(f)));
    return __uint_as_float(__builtin_amdgcn_readlane(__float_as_uint(f), 63));
}
__device__ __forceinline__ float sigm(float z) { return __builtin_amdgcn_rcpf(1.0f + __builtin_amdgcn_exp2f(z * -1.44269504089f)); }
__device__ __forceinline__ u64 bad(u64 granule, u32 tag) { return __builtin_amdgcn_ballot_w64((u32)(granule >> 32) != tag); }

#define STAMP(i)
#define STAMPS_DECL
#define STAMPS_OUT

__global__ void __launch_bounds__(768, 3) s4_mod_fused(Args a) {
    extern __shared__ __attribute__((aligned(16))) char smem[];
    LAS char* const lds = (LAS char*)smem;
    const int tid = threadIdx.x, lane = tid & 63;
    const int w = __builtin_amdgcn_readfirstlane(tid >> 6);
    const int xcd = blockIdx.x & 7, j = blockIdx.x >> 3, jj = j & 15;
    const bool heavy = j >= 16, ffn = j < 7;
    const int q = lane & 15, g = lane >> 4;
    const int ch = w & 3, h = w >> 2;
    const int G = xcd * 16 + jj;
    const f32x4 z4 = {0, 0, 0, 0};
    STAMPS_DECL;
    STAMP(0);
    u32 tag;
    const __amdgpu_buffer_rsrc_t rw = __builtin_amdgcn_make_buffer_rsrc((void*)a.ws, 0, WS_END, 0x00020000);
    auto bst = [&](const bool on, const u32 off, const u32 v) {
        __builtin_amdgcn_raw_buffer_store_b64(u32x2{v, tag}, rw, on ? off : 0xfffffff0u, 0, 16);
    };

    u32 p_ldt, p_are, p_aim, p_bre, p_bim, p_cre, p_cim, warm0;
    u32x3 adr;
    {
        warm0 = 0;
        if (h < 2)
            warm0 = ldg<u32>((const char*)(__builtin_amdgcn_s_getpc() & ~0x7full), min(j * 5 + (tid & 7), CODE_LINES - 1) * 128);
        asm volatile("" :: "s"(a.x), "s"(a.are), "s"(a.aim), "s"(a.bre), "s"(a.bim), "s"(a.cre), "s"(a.cim), "s"(a.logdt), "s"(a.dskip), "s"(a.wr),
                     "s"(a.wg), "s"(a.wu), "s"(a.wd), "s"(a.out), "s"(a.ws) : "memory");
    }
    if (h < 2) {
        const u32 po = ((4 * G + ch) * 128 + 64 * h + lane) * 2;
        p_ldt = ldg<u16>(a.logdt, (4 * G + ch) * 2);
        p_are = ldg<u16>(a.are, po); p_aim = ldg<u16>(a.aim, po); p_bre = ldg<u16>(a.bre, po); p_bim = ldg<u16>(a.bim, po);
        p_cre = ldg<u16>(a.cre, po); p_cim = ldg<u16>(a.cim, po);
        adr = ldg<u32x4>(&lane_tab, (h * 64 + lane) * 32 + 16).xyz;
        *(LAS u32x4*)(lds + LDS_FLAG + (lane < 2 ? 128 + lane * 16 : w * 16)) = u32x4{0, 0, 0, 0};
    }
    int h2 = h;
    asm volatile("" : "+s"(h2) :: "memory");

    if (h2 < 2) {
        bar();
        const u32 tw = LDS_TBL + h * TBL_H + ch * 256, g16 = lane & 48, q16 = q * 16;
        u32 a_tf = __umul24(q, TBL_ROW) + g16 + tw, a_ln = __umul24(g, TBL_ROW) + q16 + tw, a_y = __umul24(g, 320) + q16 + (LDS_Y + w * Y_SZ);
        u32 a_cr = g16 - 4 * q + (LDS_P + ch * P_SZ + C_OFF + h * C_SZ + 60), a_pr = 2 * g16 - 4 * q + (LDS_P + ch * P_SZ + 188 - 128 * h);
        u32 a_u = 2 * q16 + g16 + (LDS_U + ch * U_CH + 464 + 16 * h), a_b = a_u + (16 - 80 * h - (heavy ? 0 : 2048));
        u32 prto = min(lane, NFFN - 1) * 32 + (WS_PART + (h * 128 + G) * NFFN * 32 + ch * 8), selo = (lane & 1) * 8 + (WS_SEL + xcd * 128);
        PIN(a_tf); PIN(a_ln); PIN(a_y); PIN(a_pr); PIN(a_cr); PIN(a_u); PIN(a_b); PIN(prto); PIN(selo);
        __builtin_amdgcn_sched_barrier(0);
        if (h == 0) __builtin_amdgcn_s_setprio(2); else __builtin_amdgcn_s_setprio(1);
        STAMP(11);
        PIN(p_ldt); PIN(p_are); PIN(p_aim); PIN(p_bre); PIN(p_bim); PIN(p_cre); PIN(p_cim);
        STAMP(3);
        const float ld = bflo(p_ldt);
        const float t0 = ld * 1.44269504089f;
        const float te = __builtin_fmaf(ld, 1.92596303e-8f, __builtin_fmaf(ld, 1.44269504089f, -t0));
        const float ex = __builtin_amdgcn_exp2f(t0);
        float dt = rbf(__builtin_fmaf(ex * te, 0.69314718056f, ex));
        const float lr = (dt * 1.44269504089f) * bflo(p_are);
        const float ai = dt * bflo(p_aim);
        const float th = ai * 0.15915494309f;
        const float tl = __builtin_fmaf(ai, 6.4206382e-9f, __builtin_fmaf(ai, 0.15915494309f, -th));
        const float bi = bflo(p_bim);
        const f32x2 W = cmul(f32x2{bflo(p_cre), bflo(p_cim)}, f32x2{bflo(p_bre), bi}, f32x2{-bi, bi}) * dt;
        const float gp = fmaxf(lr, 0.0f);
        const int en = __builtin_amdgcn_frexp_expf(fmaxf(fabsf(W.x), fabsf(W.y))) + (int)(2168.0f * gp);
        const int E = min(max((int)wave_umax((u32)(en + 201)) - 200, -100), 120);
        const float sA = __builtin_amdgcn_ldexpf(__builtin_amdgcn_exp2f(120.0f * gp), 13 - E);
        const float inv = __uint_as_float((u32)(E - 13 + 127) << 23);
        const float t1 = __builtin_amdgcn_fractf(th), e1 = __builtin_amdgcn_exp2f(lr);
        const f32x2 z1 = f32x2{__builtin_amdgcn_cosf(t1), __builtin_amdgcn_sinf(t1)} * e1;
        const float t7 = __builtin_fmaf(128.0f, tl, __builtin_amdgcn_fractf(128.0f * th)), e7 = __builtin_amdgcn_exp2f(128.0f * lr);
        const f32x2 z128 = f32x2{__builtin_amdgcn_cosf(t7), __builtin_amdgcn_sinf(t7)} * e7;
        const f32x2 z2 = csq(z1), zz4 = csq(z2), z8 = csq(zz4);
        f32x2 n1 = {-z1.y, z1.y}, n2 = {-z2.y, z2.y}, n4 = {-zz4.y, zz4.y}, n128 = {-z128.y, z128.y}, c8 = {z8.y, -z8.y};
        f32x2 X = W * sA, B = {__builtin_amdgcn_exp2f(-120.0f * gp), 0.0f};
        PIN(adr);
        const u32 a_e0 = adr[0] + (LDS_P + ch * P_SZ), a_e1 = adr[1] + (LDS_P + ch * P_SZ), a_cw = adr[2] + (LDS_P + ch * P_SZ);
        STAMP(12);
        {
            const u32 m0v = tw + (u32)(size_t)lds;
            asm volatile("s_mov_b32 m0, %0\n\ts_nop 0" :: "s"(m0v) : "memory");
        }
        auto put2 = [&](const int r0, const f32x2 v0, const int r1, const f32x2 v1) {
            asm volatile("ds_write_addtid_b32 %0 offset:%c2\n\tds_write_addtid_b32 %1 offset:%c3" :: "v"(pkh(v0)), "v"(pkh(v1)), "i"(r0 * TBL_ROW), "i"(r1 * TBL_ROW));
        };
        f32x4 ka, kb;
        float k255, k1024;
        {
            u32x4 fa[3][4], fb[4];
            f32x4 k0, k1, k2;
            auto ldf = [&](const int row, const int s) { return *(const LAS u32x4*)(lds + a_tf + row * TBL_ROW + s * 64); };
            f32x2 c0 = X, c1 = cmul(X, z128, n128);
            X = cmul(c1, z128, n128);
#pragma unroll
            for (int i = 0; i < 8; i++) {
                const f32x2 B1 = cmul(B, z8, c8);
                put2(i, c0, 8 + i, c1);
                if (i < 7) { c0 = cmul(c0, z1, n1); c1 = cmul(c1, z1, n1); }
                put2(41 + 2 * i, B, 42 + 2 * i, B1);
                if (i < 7) B = cmul(B1, z8, c8);
            }
            FENCE();
#pragma unroll
            for (int s = 0; s < 4; s++) { fb[s] = ldf(41, s); fa[0][s] = ldf(0, s); }
            FENCE();
            c0 = X; c1 = cmul(X, z128, n128);
            X = cmul(c1, z128, n128);
#pragma unroll
            for (int i = 0; i < 4; i++) {
                const f32x2 e = cmul(X, zz4, n4), Xn = cmul(X, z128, n128);
                put2(16 + i, c0, 20 + i, c1);
                if (i < 3) { c0 = cmul(c0, z2, n2); c1 = cmul(c1, z2, n2); }
                put2(24 + 2 * i, X, 25 + 2 * i, e);
                X = Xn;
                k0 = mfma_h(fa[0][i], fb[i], i ? k0 : z4);
                PIN(k0);
            }
            FENCE();
#pragma unroll
            for (int s = 0; s < 4; s++) fa[1][s] = ldf(16, s);
            FENCE();
            STAMP(2);
            const f32x2 X9 = cmul(X, z128, n128);
            put2(32, X, 33, X9);
            if (heavy) {
                const f32x2 z256 = csq(z128), n256 = {-z256.y, z256.y};
                f32x2 e = X, o = X9;
#pragma unroll
                for (int i = 0; i < 3; i++) { e = cmul(e, z256, n256); o = cmul(o, z256, n256); put2(34 + 2 * i, e, 35 + 2 * i, o); }
                e = cmul(e, z256, n256);
                put2(40, e, 40, e);
            }
            __builtin_amdgcn_s_setprio(0);
            FENCE();
#pragma unroll
            for (int s = 0; s < 4; s++) fa[2][s] = ldf(32, s);
            __builtin_amdgcn_sched_barrier(0);
#pragma unroll
            for (int s = 0; s < 4; s++) k1 = mfma_h(fa[1][s], fb[s], s ? k1 : z4);
#pragma unroll
            for (int s = 0; s < 4; s++) k2 = mfma_h(fa[2][s], fb[s], s ? k2 : z4);
            FENCE();
            const int dlt = h ? -TBL_H : TBL_H;
            LAS char* fo = lds + LDS_FLAG + w * 16;
            const LAS char* fi = lds + LDS_FLAG + (w ^ 4) * 16;
            if (h) {
                *(LAS f32x4*)(lds + a_ln) = k0 * inv; *(LAS f32x4*)(lds + a_ln + 4 * TBL_ROW) = k2 * inv;
                asm volatile("s_waitcnt lgkmcnt(0)" ::: "memory");
                *(volatile LAS u32*)fo = 1;
                STAMP(13);
                u32x4 fl; f32x4 o;
#pragma clang loop unroll(disable)
                for (u32 spin = 0; spin < SPIN_CAP; spin++) {
                    fl = *(volatile LAS u32x4*)fi; o = *(volatile LAS f32x4*)(lds + a_ln + dlt);
                    if (__builtin_amdgcn_readfirstlane(fl[0])) break;
                    __builtin_amdgcn_s_sleep(2);
                }
                ka = k1 * inv + o;
                k255 = __uint_as_float(__builtin_amdgcn_readlane(__float_as_uint(k0[3]), 63)) * inv + __uint_as_float(fl[1]);
                k1024 = __uint_as_float(__builtin_amdgcn_readlane(__float_as_uint(k2[0]), 0)) * inv + __uint_as_float(fl[2]);
            } else {
                *(LAS f32x4*)(lds + a_ln) = k1 * inv;
                const float s0 = __uint_as_float(__builtin_amdgcn_readlane(__float_as_uint(k0[3]), 63)) * inv;
                const float s1 = __uint_as_float(__builtin_amdgcn_readlane(__float_as_uint(k2[0]), 0)) * inv;
                asm volatile("s_waitcnt lgkmcnt(0)" ::: "memory");
                *(volatile LAS u32x4*)fo = u32x4{1, __float_as_uint(s0), __float_as_uint(s1), 0};
                STAMP(13);
                u32 fl; f32x4 o0, o1;
#pragma clang loop unroll(disable)
                for (u32 spin = 0; spin < SPIN_CAP; spin++) {
                    fl = *(volatile LAS u32*)fi; o0 = *(volatile LAS f32x4*)(lds + a_ln + dlt); o1 = *(volatile LAS f32x4*)(lds + a_ln + dlt + 4 * TBL_ROW);
                    if (__builtin_amdgcn_readfirstlane(fl)) break;
                    __builtin_amdgcn_s_sleep(2);
                }
                ka = k0 * inv + o0; kb = k2 * inv + o1;
                k1024 = __uint_as_float(__builtin_amdgcn_readlane(__float_as_uint(kb[0]), 0));
            }
        }
        STAMP(1);
        if (h == 0) {
            const f32x4 v = ka;
            auto sw = __builtin_amdgcn_permlane16_swap(__float_as_uint(v[3]), __float_as_uint(v[3]), false, false);
            const float pd = dppf<0x138>(0.0f, __uint_as_float(sw[1]));
            const float pv = (g & 1) ? __uint_as_float(sw[0]) : pd;
            *(LAS u32x4*)(lds + a_e0) = u32x4{pkbf(v[3], v[2]), pkbf(v[2], v[1]), pkbf(v[1], v[0]), pkbf(v[0], pv)};
            const u32 kk = pkbf(k1024, k1024);
            const u32 f7 = lane == 15 ? kk : 0u, f6 = lane == 14 ? kk : 0u;
            *(LAS u32x4*)(lds + a_e1) = u32x4{f7, f7, f7 | (f6 & 0xffff0000u), f7 | f6};
            const f32x4 u = {lane == 0 ? 0.0f : kb[0], kb[1], kb[2], kb[3]};
            const bool e = q >= 14;
            LAS u32* cw = (LAS u32*)(lds + a_cw);
            cw[0] = pkbf(u[0], dppf<0x138>(0.0f, dppf<0x138>(0.0f, e ? u[3] : u[0])));
#pragma unroll
            for (int i = 1; i < 4; i++) cw[-8 * i] = pkbf(u[i], dppf<0x122>(0.0f, e ? u[i - 1] : u[i]));
        } else {
            const f32x4 v = ka;
            const bool s2 = g < 2;
            const float nw = dppf<0x130>(v[0], v[0]);
            const float nr = dppf<0x12f>(v[0], q == 0 ? v[2] : v[0]);
            const float nA = s2 ? nw : nr;
            const float nB = dppf<0x130>(k1024, q == 0 ? v[0] : v[2]);
            const float x2 = s2 ? v[1] : v[0], x3 = s2 ? v[2] : v[1], x6 = s2 ? v[3] : nA;
            const u32 aa = pkbf(v[0], v[0]), cc = pkbf(x3, x3), a2 = pkbf(v[2], v[2]), b2 = pkbf(v[3], v[3]);
            *(LAS u32x4*)(lds + a_e0) = u32x4{pkbf(nA, x6), pkbf(x6, x3), cc, cc};
            *(LAS u32x4*)(lds + a_e0 + 16) = u32x4{pkbf(x3, x2), pkbf(x2, v[0]), aa, pkbf(v[0], lane == 0 ? k255 : v[0])};
            *(LAS u32x4*)(lds + a_e1) = u32x4{pkbf(nB, nB), pkbf(nB, v[3]), b2, b2};
            *(LAS u32x4*)(lds + a_e1 + 16) = u32x4{pkbf(v[3], v[2]), a2, a2, a2};
        }
        STAMP(4);
        u32x4 A[17];
        const LAS u32* pr = (const LAS u32*)(lds + a_pr);
        auto lda = [&](const int m) { A[m] = u32x4{pr[64 * (16 - m)], pr[64 * (16 - m) + 2], pr[64 * (16 - m) + 4], pr[64 * (16 - m) + 6]}; };
        {
            asm volatile("s_waitcnt lgkmcnt(0)" ::: "memory");
            *(volatile LAS u32*)(lds + LDS_FLAG + w * 16 + 12) = 2;
#pragma clang loop unroll(disable)
            for (u32 spin = 0; spin < SPIN_CAP; spin++)
                if (__builtin_amdgcn_readfirstlane(*(volatile LAS u32*)(lds + LDS_FLAG + (w ^ 4) * 16 + 12)) >= 2) break;
            FENCE();
        }
#pragma unroll
        for (int m = 0; m < NPRE; m++) lda(m);
        asm volatile("" :: "v"(warm0) : "memory");
        STAMP(5);
        if (ffn) rawbar();
        rawbar();
        STAMP(7);

        f32x4 acc[4];
        u64 selv, prtv;
        {
            auto ldb = [&](const int k) { return *(const LAS u32x4*)(lds + a_b + 128 * (28 - k)); };
            constexpr int PF = 5;
            auto run = [&](const int hi, const int lo, const int k0, const int k1, auto&& extra) {
                u32x4 Bf[29];
#pragma unroll
                for (int k = k0; k < k0 + PF && k <= k1; k++) Bf[k] = ldb(k);
                __builtin_amdgcn_sched_barrier(0);
#pragma unroll
                for (int k = k0; k <= k1; k++) {
                    if (hi == 3 && NPRE + k < 17) lda(NPRE + k);
                    if (k + PF <= k1) Bf[k + PF] = ldb(k + PF);
                    extra(k);
#pragma unroll
                    for (int i = hi; i >= lo; i--) {
                        const int mp = 4 * i - 12 + k;
                        if (mp >= 0 && mp < 17) acc[i] = mfma_bf(A[mp], Bf[k], mp == 0 ? z4 : acc[i]);
                    }
                    __builtin_amdgcn_sched_barrier(0);
                }
            };
            auto put_y = [&](const int i) { *(LAS f32x4*)(lds + a_y + i * Y_TILE) = acc[i]; };
            auto done = [&](const u32 level) { *(volatile LAS u32*)(lds + LDS_FLAG + w * 16 + 12) = level; };
            if (heavy) {
                const LAS u32* cr = (const LAS u32*)(lds + a_cr);
                u32x4 Wd[4], KT[4], band, S0, S1;
                f32x4 dw[4];
                STAMP(6);
                run(3, 2, 0, 20, [&](const int k) {
                    if (k == 10) band = *(const LAS u32x4*)(lds + LDS_GU + (tid & 63) * 16 + h * 1024);
                    if (k >= 11 && k < 15) Wd[k - 11] = *(const LAS u32x4*)(lds + a_u + 512 * (k - 11));
                    if (k >= 15 && k < 18) KT[18 - k] = u32x4{cr[16 * (k - 15)], cr[16 * (k - 15) + 2], cr[16 * (k - 15) + 16], cr[16 * (k - 15) + 18]};
                    if (k == 18) KT[0] = u32x4{cr[48], cr[50], 0u, 0u};
                    FENCE();
                    if (k >= 15 && k < 19) dw[k - 15] = mfma_bf(Wd[k - 15], band, z4);
                    if (k == 18) S0 = u32x4{pkbf(dw[0][0], dw[0][1]), pkbf(dw[0][2], dw[0][3]), pkbf(dw[1][0], dw[1][1]), pkbf(dw[1][2], dw[1][3])};
                    if (k == 20) S1 = u32x4{pkbf(dw[2][0], dw[2][1]), pkbf(dw[2][2], dw[2][3]), pkbf(dw[3][0], dw[3][1]), pkbf(dw[3][2], dw[3][3])};
                });
                acc[3] = mfma_bf(S0, KT[3], acc[3]); acc[2] = mfma_bf(S0, KT[2], acc[2]);
                acc[3] = mfma_bf(S1, KT[1], acc[3]); acc[2] = mfma_bf(S1, KT[0], acc[2]);
                run(1, 0, 8, 28, [&](const int k) {
                    if (k == 10) { put_y(3); put_y(2); }
                    if (k == 28 && !EARLYP) { selv = gld(a.ws, selo); prtv = gld(a.ws, prto); }
                    if (k == 13) {
                        if (EARLYP) { selv = gld(a.ws, selo); prtv = gld(a.ws, prto); }
                        asm volatile("s_waitcnt lgkmcnt(3)" ::: "memory"); done(3);
                    }
                });
                acc[1] = mfma_bf(S0, KT[1], acc[1]); acc[0] = mfma_bf(S0, KT[0], acc[0]);
                put_y(1);
            } else {
                run(3, 2, 0, 15, [&](const int) {});
                run(1, 0, 8, 15, [&](const int k) {
                    if (k == 10) { put_y(3); put_y(2); }
                    if (k == 13) { asm volatile("s_waitcnt lgkmcnt(0)" ::: "memory"); done(3); }
                });
                selv = gld(a.ws, selo); prtv = gld(a.ws, prto);
                put_y(1);
            }
            STAMP(9);
            put_y(0);
            asm volatile("s_waitcnt lgkmcnt(0)" ::: "memory");
            done(5);
        }
        tag = __builtin_amdgcn_readfirstlane(*(LAS u32*)(lds + LDS_UPD + 48));
        u64 prt2 = 0;
        if (POLL2) { __builtin_amdgcn_s_sleep(POLL2); prt2 = gld(a.ws, prto); }
        STAMP(8);
        if (blockIdx.x == 120 && w == 7) {
#pragma clang loop unroll(disable)
            for (u32 spin = 0; spin < SPIN_CAP; spin++) {
                FENCE();
                u32x4 seen[8];
                u64 nb = 0;
#pragma unroll
                for (int i = 0; i < 8; i++) seen[i] = __builtin_amdgcn_raw_buffer_load_b128(rw, WS_SEEN + i * 1024 + lane * 16, 0, 16);
#pragma unroll
                for (int i = 0; i < 8; i++) nb |= __builtin_amdgcn_ballot_w64(seen[i][1] != tag) | __builtin_amdgcn_ballot_w64(seen[i][3] != tag);
                if (!nb) break;
            }
            __builtin_amdgcn_raw_buffer_store_b32(tag, rw, lane == 0 ? WS_EPOCH : 0xfffffff0u, 0, 16);
        }
        if (bad(selv, tag)) {
#pragma clang loop unroll(disable)
            for (u32 spin = 0; spin < SPIN_CAP; spin++) { selv = gld(a.ws, selo); if (!bad(selv, tag)) break; __builtin_amdgcn_s_sleep(2); }
        }
        const u32 sel = h ? __builtin_amdgcn_readlane((u32)selv, 1) : __builtin_amdgcn_readlane((u32)selv, 0);
        const bool own = ((sel & 2047) >= 1024) == heavy;
        if (own) {
            if (POLL2 && bad(prtv, tag)) prtv = prt2;
            if (bad(prtv, tag)) {
#pragma clang loop unroll(disable)
                for (u32 spin = 0; spin < SPIN_CAP; spin++) { prtv = gld(a.ws, prto); if (!bad(prtv, tag)) break; __builtin_amdgcn_s_sleep(1); }
            }
            const float y = wave_fsum(lane < NFFN ? __uint_as_float((u32)prtv) : 0.0f);
            *(LAS float*)(lds + LDS_UPD + w * 4) = rbf(rbf(y) * bfhi(sel));
        }
        *(LAS u32*)(lds + LDS_UPD + 32 + h * 4) = own ? sel & 2047u : 0x7fffffffu;
        bar();
    } else {
        __builtin_amdgcn_s_setprio(3);
        const int lt = (tid + 64 * jj) & 255;
        const u32 xo = lt * 4096 + G * 8;
        u32 xh = xo + (heavy ? 1024 * 1024 : 0);
        const u32 so = WS_STATS + xcd * 16384 + lt * 32, sh = so + (heavy ? 8192 : 0);
        u32 warm1 = 0, warm2 = 0, warm3 = 0;
        u32x2 xc[4], xl[4];
        u64 sg[4], sl[4];
        f32x2 xf[4][2], zn[4][2], gt[4][2];
        u32 epoch;
        u32x2 dsk, dskr;
        asm volatile("s_load_dword %0, %2, 0x0\n\ts_load_dwordx2 %1, %3, %4" : "=&s"(epoch), "=&s"(dskr) : "s"(a.ws), "s"(a.dskip), "s"(G * 8));
        auto first = [&]() {
            asm volatile("s_waitcnt lgkmcnt(0)\n\ts_add_u32 %0, %2, 1\n\ts_mov_b64 %1, %3" : "=&s"(tag), "=&s"(dsk) : "s"(epoch), "s"(dskr) : "scc");
        };
        u32x4 bnd;
        const bool lnr = heavy && ch < 2;
        if (lnr) {
            const int lnu = (blockIdx.x - 128) * 2 + ch;
            const __amdgpu_buffer_rsrc_t rr_ = __builtin_amdgcn_make_buffer_rsrc((void*)a.wr, 0, D_ * 2, 0x00020000);
            u32x4 xa[8], cb[8];
#pragma unroll
            for (int s = 0; s < 8; s++) xa[s] = ldg<u32x4>(a.x, (lnu * 8 + (q >> 1)) * 1024 + (q & 1) * 64 + g * 16 + 128 * s);
            rawbar();
#pragma unroll
            for (int s = 0; s < 8; s++) cb[s] = __builtin_amdgcn_raw_buffer_load_b128(rr_, ((q & 13) ? 4096 : q * 32 + g * 16) + 128 * s, 0, 0);
            const u32x4 ones = {0x3f803f80u, 0x3f803f80u, 0x3f803f80u, 0x3f803f80u};
            const int rr = 2 * g + ((q >> 1) & 1);
            u32 o_st = WS_STATS + ((q & 1) + 2 * (q >> 2)) * 16384 + (lnu * 8 + rr) * 8, o_id = 0xffffu - (lnu * 8 + rr);
            u32 o_cd = (WS_CAND + (lane >> 1) * 4096 + lnu * 16 + (lane & 1) * 8) | (u32)(((15 - lane) >> 31) << 20);
            PIN(o_st); PIN(o_id); PIN(o_cd);
            first();
            f32x4 d0 = z4, d1 = z4, d2 = z4, d3 = z4;
#pragma unroll
            for (int s = 0; s < 8; s++) { d2 = mfma_bf(xa[s], xa[s], d2); d0 = mfma_bf(xa[s], ones, d0); }
            asm volatile("" : "+v"(d0), "+v"(d2));
            __builtin_amdgcn_sched_barrier(0);
#pragma unroll
            for (int s = 0; s < 8; s++) { d1 = mfma_bf(xa[s], cb[s], d1); d3 = mfma_bf(ones, cb[s], d3); }
            STAMP(13);
            const bool hi = q & 2;
            const float da = (q & 1) ? d2[1] : d2[0], db = (q & 1) ? d2[3] : d2[2];
            float sq = (q >> 2) == g ? (hi ? db : da) : 0.0f;
            sq += __uint_as_float(dpp0<0xb1>(__float_as_uint(sq)));
            sq += __uint_as_float(dpp0<0x124>(__float_as_uint(sq)));
            sq += __uint_as_float(dpp0<0x128>(__float_as_uint(sq)));
            const float mean = (hi ? d0[2] + d0[3] : d0[0] + d0[1]) * (1.0f / D_);
            const float var = fmaxf(sq * (1.0f / D_) - mean * mean, 0.0f);
            const float sd = rbf(__builtin_amdgcn_sqrtf(rbf(rbf(var) + 1e-5f)));
            const u32 ms = pkbf(mean, sd);
            bst(true, o_st, ms);
            const float ta = dppf<0x150>(0.0f, d1[0]) + dppf<0x152>(0.0f, d1[1]), tb = dppf<0x150>(0.0f, d1[2]) + dppf<0x152>(0.0f, d1[3]);
            const float dot = hi ? tb : ta, sw = dppf<0x150>(0.0f, d3[0]) + dppf<0x152>(0.0f, d3[0]);
            const float r = (dot - bflo(ms) * sw) * __builtin_amdgcn_rcpf(sd);
            const u32 rb = pkbf(r, r);
            const u32 key = ((rb ^ (((int)rb >> 31) | 0x80000000u)) & 0xffff0000u) | o_id;
            const u32 ko = dpp0<0x4e>(key);
            auto x1 = __builtin_amdgcn_permlane16_swap(max(key, ko), max(key, ko), false, false);
            auto x2 = __builtin_amdgcn_permlane16_swap(min(key, ko), min(key, ko), false, false);
            const u32 a1 = max(x1[0], x1[1]), a2 = max(min(x1[0], x1[1]), max(x2[0], x2[1]));
            auto y1 = __builtin_amdgcn_permlane32_swap(a1, a1, false, false);
            auto y2 = __builtin_amdgcn_permlane32_swap(a2, a2, false, false);
            bst(true, o_cd, (lane & 1) ? max(min(y1[0], y1[1]), max(y2[0], y2[1])) : max(y1[0], y1[1]));
            STAMP(1);
        } else {
            rawbar();
            if (HW && heavy) {
                const u32 r1 = (jj * 2 + (ch & 1)) * 16 + q;
                if (HW == 1) warm1 = ldg<u32>(a.wg, __umul24((r1 & 3) * 128 + (r1 >> 2), F_ * 2) + g * 128 + ((xcd * 448) & ~127));
                if (HW == 2) {
                    const u32 c0 = (xcd * 448) & ~127;
                    const u16* wp = (ch & 1) ? a.wu : a.wg;
                    warm1 = ldg<u32>(wp, __umul24(jj * 32 + (lane & 31), F_ * 2) + c0 + ((384 - c0 - (u32)(size_t)wp) & 384));
                }
            }
        }
        if (heavy) {
            bnd = ldg<u32x4>(&lane_tab, (tid & 127) * 32);
            FENCE();
            *(volatile LAS u32*)(lds + LDS_FLAG + 128 + ch * 4) = 1;
#pragma clang loop unroll(disable)
            for (u32 spin = 0; spin < SPIN_CAP; spin++) {
                const u32x2 f0 = *(volatile LAS u32x2*)(lds + LDS_FLAG + 128);
                if (__builtin_amdgcn_readfirstlane(f0[0] & f0[1])) break;
                __builtin_amdgcn_s_sleep(1);
            }
            FENCE();
        }
        auto second = [&]() {
            if (!lnr) first();
            bst(lane == 0, WS_SEEN + (blockIdx.x * 4 + ch) * 8, 0);
            *(LAS u32*)(lds + LDS_UPD + 48) = tag;
            *(LAS u32x2*)(lds + LDS_U + ch * U_CH + lane * 8) = u32x2{0, 0};
        };
        u32 yb = LDS_Y + (lt >> 6) * Y_TILE + (lt & 3) * 320 + ((lt >> 2) & 15) * 16;
        const u32 a_st = LDS_U + 512 + 8 * lt;
        auto tags4 = [&](const u64 (&v)[4]) { return (bad(v[0], tag) | bad(v[1], tag) | bad(v[2], tag) | bad(v[3], tag)) == 0; };
        auto ld4 = [&](u64 (&v)[4], const u32 off) {
            const u32x4 lo = __builtin_amdgcn_raw_buffer_load_b128(rw, off, 0, 16), hi = __builtin_amdgcn_raw_buffer_load_b128(rw, off + 16, 0, 16);
            v[0] = lo[0] | ((u64)lo[1] << 32); v[1] = lo[2] | ((u64)lo[3] << 32); v[2] = hi[0] | ((u64)hi[1] << 32); v[3] = hi[2] | ((u64)hi[3] << 32);
        };
        auto wait4 = [&](u64 (&v)[4], const u32 off) {
            if (!tags4(v)) {
#pragma clang loop unroll(disable)
                for (u32 spin = 0; spin < SPIN_CAP; spin++) {
                    FENCE();
                    ld4(v, off);
                    if (tags4(v)) break;
                    __builtin_amdgcn_s_sleep(4);
                }
            }
        };
        auto stage = [&](const u64 (&st)[4], const u32x2 (&r)[4], const u32 dst, const bool keep) {
            f32x2 rs[2], nb[2];
#pragma unroll
            for (int p = 0; p < 2; p++) {
                rs[p] = f32x2{__builtin_amdgcn_rcpf(bfhi((u32)st[2 * p])), __builtin_amdgcn_rcpf(bfhi((u32)st[2 * p + 1]))};
                nb[p] = f32x2{bflo((u32)st[2 * p]), bflo((u32)st[2 * p + 1])} * rs[p];
            }
#pragma unroll
            for (int c = 0; c < 4; c++) {
                u32x2 o;
#pragma unroll
                for (int p = 0; p < 2; p++) {
                    const u32 w0 = r[2 * p][c >> 1], w1 = r[2 * p + 1][c >> 1];
                    const f32x2 xv = (c & 1) ? f32x2{bfhi(w0), bfhi(w1)} : f32x2{bflo(w0), bflo(w1)};
                    const f32x2 z = __builtin_elementwise_fma(xv, rs[p], -nb[p]);
                    if (keep) { xf[c][p] = xv; zn[c][p] = z; }
                    o[p] = pkbf(z.x, z.y);
                }
                *(LAS u32x2*)(lds + dst + c * U_CH) = o;
            }
        };
        auto gates = [&]() {
#pragma unroll
            for (int i = 0; i < 8; i++) gt[i >> 1][i & 1] = zn[i >> 1][i & 1] * -1.44269504089f;
            __builtin_amdgcn_sched_barrier(0);
#pragma unroll
            for (int i = 0; i < 8; i++) { f32x2& e = gt[i >> 1][i & 1]; e = f32x2{__builtin_amdgcn_exp2f(e.x), __builtin_amdgcn_exp2f(e.y)}; }
            __builtin_amdgcn_sched_barrier(0);
#pragma unroll
            for (int i = 0; i < 8; i++) gt[i >> 1][i & 1] += 1.0f;
            __builtin_amdgcn_sched_barrier(0);
#pragma unroll
            for (int i = 0; i < 8; i++) { f32x2& e = gt[i >> 1][i & 1]; e = f32x2{__builtin_amdgcn_rcpf(e.x), __builtin_amdgcn_rcpf(e.y)}; }
            __builtin_amdgcn_sched_barrier(0);
#pragma unroll
            for (int i = 0; i < 8; i++) { gt[i >> 1][i & 1] *= zn[i >> 1][i & 1]; PIN(gt[i >> 1][i & 1]); }
        };
        if (ffn) {
            const int f = xcd * 7 + jj;
            u32 wgu[2][4][8], wdn[4][8];
            const __amdgpu_buffer_rsrc_t rg = __builtin_amdgcn_make_buffer_rsrc((void*)a.wg, 0, D_ * F_ * 2, 0x00020000);
            const __amdgpu_buffer_rsrc_t ru = __builtin_amdgcn_make_buffer_rsrc((void*)a.wu, 0, D_ * F_ * 2, 0x00020000);
            const __amdgpu_buffer_rsrc_t rd = __builtin_amdgcn_make_buffer_rsrc((void*)a.wd, 0, D_ * F_ * 2, 0x00020000);
            const u32 ro = ROT * (f & 1) * 64 * F_ * 2;
            const u32 og = ((128 * ch + 8 * g) * F_ + 32 * f + 2 * q) * 2, od = ((32 * f + 8 * g) * D_ + 128 * ch + 2 * q) * 2;
#pragma unroll
            for (int m = 0; m < 2; m++)
#pragma unroll
                for (int s = 0; s < 4; s++)
#pragma unroll
                    for (int i = 0; i < 8; i++) wgu[m][s][i] = __builtin_amdgcn_raw_buffer_load_b32(m ? ru : rg, s < 2 ? og + ro : og + (64 * F_ * 2 - ro), (32 * (s & 1) + i) * F_ * 2, 0);
            const u32 co = WS_CAND + xcd * 4096 + lane * 16;
            u32x4 cd[4];
            auto ldc = [&]() {
#pragma unroll
                for (int i = 0; i < 4; i++) cd[i] = __builtin_amdgcn_raw_buffer_load_b128(rw, co + 1024 * i, 0, 16);
            };
            auto badc = [&]() {
                u64 nb = 0;
#pragma unroll
                for (int i = 0; i < 4; i++) nb |= __builtin_amdgcn_ballot_w64(cd[i][1] != tag) | __builtin_amdgcn_ballot_w64(cd[i][3] != tag);
                return nb;
            };
            FENCE();
            ldc();
            FENCE();
            auto ld_down = [&]() {
#pragma unroll
            for (int i = 0; i < 8; i++)
#pragma unroll
                for (int u = 0; u < 4; u++) wdn[u][i] = __builtin_amdgcn_raw_buffer_load_b32(rd, od + ((i & 3) * D_ * 2 + u * 64), (i & 4) * D_ * 2, 0);
            };
            if (!DLATE) ld_down();
            second();
            u32 pp = WS_PART + (((32 * ch + 8 * g + (q >> 1)) * NFFN + f) * 4 + 2 * (q & 1)) * 8;
            u32 a_h = LDS_H + ch * 128 + (q & 1) * 64 + g * 16;
            PIN(pp); PIN(a_h);
            STAMP(12);
            if (badc()) {
#pragma clang loop unroll(disable)
                for (u32 spin = 0; spin < SPIN_CAP; spin++) { FENCE(); ldc(); if (!badc()) break; __builtin_amdgcn_s_sleep(2); }
            }
            STAMP(4);
            u32 ka = cd[0][0], kb = cd[0][2];
#pragma unroll
            for (int i = 1; i < 4; i++) { kb = max(min(ka, cd[i][0]), max(kb, cd[i][2])); ka = max(ka, cd[i][0]); }
            const u32 k1 = wave_umax(ka), k2 = wave_umax(ka == k1 ? kb : ka);
            const u32 kk = (lane & 1) ? k2 : k1, row = ~kk & 2047u;
            const __amdgpu_buffer_rsrc_t rx = __builtin_amdgcn_make_buffer_rsrc((void*)a.x, 0, 2048 * 1024, 0x00020000);
            const u32 xr = q < 2 ? row * 1024 + ch * 256 + g * 16 : 0xfffff000u;
            u32x4 an[4];
#pragma unroll
            for (int s = 0; s < 4; s++) an[s] = __builtin_amdgcn_raw_buffer_load_b128(rx, (s < 2 ? xr + ROT * (f & 1) * 128 : xr - ROT * (f & 1) * 128) + 64 * s, 0, 0);
            const u32 sro = WS_STATS + xcd * 16384 + row * 8;
            u64 sr = gld(a.ws, sro);
            FENCE();
            if (DLATE) ld_down();
            if (jj == 0 && ch == 0) {
                const float sv = sigm(__uint_as_float((kk ^ ((~(int)kk >> 31) | 0x80000000u)) & 0xffff0000u));
                bst(lane < 2, WS_SEL + xcd * 128 + lane * 8, row | (pkbf(sv, sv) << 16));
            }
            STAMP(11);
            u32x4 fg[2][4][2], fd[4][2];
            auto shuffle = [&](const int m0, const int m1) {
#pragma unroll
            for (int m = m0; m < m1; m++)
#pragma unroll
                for (int s = 0; s < 4; s++)
#pragma unroll
                    for (int p = 0; p < 2; p++) {
                        u32x4 o;
#pragma unroll
                        for (int d = 0; d < 4; d++)
                            o[d] = __builtin_amdgcn_perm(m < 2 ? wgu[m & 1][s][2 * d + 1] : wdn[s][2 * d + 1], m < 2 ? wgu[m & 1][s][2 * d] : wdn[s][2 * d], p ? 0x07060302u : 0x05040100u);
                        PIN(o);
                        if (m < 2) fg[m & 1][s][p] = o; else fd[s][p] = o;
                    }
            };
            shuffle(0, DLATE ? 2 : 3);
            const u32 o1 = q < 2 ? 0u : 0x3f803f80u;
            f32x4 c[2][2];
#pragma unroll
            for (int s = 0; s < 4; s++)
#pragma unroll
                for (int mt = 0; mt < 2; mt++)
#pragma unroll
                    for (int p = 0; p < 2; p++) c[mt][p] = mfma_bf(an[s] | o1, fg[mt][s][p], s ? c[mt][p] : z4);
            if (bad(sr, tag)) {
#pragma clang loop unroll(disable)
                for (u32 spin = 0; spin < SPIN_CAP; spin++) { sr = gld(a.ws, sro); if (!bad(sr, tag)) break; }
            }
            PIN(c[0][0]); PIN(c[0][1]); PIN(c[1][0]); PIN(c[1][1]);
            FENCE();
#pragma unroll
            for (int i = 0; i < 4; i++) xc[i] = ldg<u32x2>(a.x, xh + 1024 * i);
            ld4(sg, sh);
            STAMP(2);
            const u32 s0 = __builtin_amdgcn_readlane((u32)sr, 0), s1 = __builtin_amdgcn_readlane((u32)sr, 1);
            LAS float* gu = (LAS float*)(lds + LDS_GU);
            LAS float* gw = gu + (g == 0 ? ch * 128 + 2 * q : 512);
#pragma unroll
            for (int mt = 0; mt < 2; mt++) {
                *(LAS f32x2*)(gw + mt * 64) = f32x2{c[mt][0][0], c[mt][1][0]} - bflo(s0) * f32x2{c[mt][0][2], c[mt][1][2]};
                *(LAS f32x2*)(gw + mt * 64 + 32) = f32x2{c[mt][0][1], c[mt][1][1]} - bflo(s1) * f32x2{c[mt][0][2], c[mt][1][2]};
            }
            const float rs = __builtin_amdgcn_rcpf(bfhi(lane < 32 ? s0 : s1));
            asm volatile("s_waitcnt lgkmcnt(0)" ::: "memory");
            if (DLATE) shuffle(2, 3);
            rawbar();
            f32x2 gs = {0.0f, 0.0f};
#pragma unroll
            for (int i = 0; i < 4; i++) gs += f32x2{gu[i * 128 + lane], gu[i * 128 + 64 + lane]};
            const u32 gb = pkbf(gs.x * rs, gs.y * rs);
            const float ga = bflo(gb);
            const float hv = rbf(ga * sigm(ga)) * bfhi(gb);
            LAS u16* hb = (LAS u16*)(lds + LDS_H + ch * 128);
            hb[lane] = (u16)pkbf(hv, hv);
            FENCE();
            f32x4 y0 = z4, y1 = z4;
            u32x4 ah[4];
#pragma unroll
            for (int u = 0; u < 4; u++) ah[u] = *(const LAS u32x4*)(lds + ((q >> 2) == u ? a_h : (u32)LDS_U));
#pragma unroll
            for (int u = 0; u < 4; u++) { y0 = mfma_bf(ah[u], fd[u][0], y0); y1 = mfma_bf(ah[u], fd[u][1], y1); }
            STAMP(3);
            bst(true, pp, __float_as_uint(y0[0])); bst(true, pp + 8, __float_as_uint(y1[0]));
            bst(true, pp + 128 * NFFN * 32, __float_as_uint(y0[1])); bst(true, pp + 128 * NFFN * 32 + 8, __float_as_uint(y1[1]));
            STAMP(5);
            asm volatile("" : "+v"(sg[0]), "+v"(sg[1]), "+v"(sg[2]), "+v"(sg[3]) :: "memory");
            wait4(sg, sh);
            u32 t2 = tid;
            PIN(t2);
            stage(sg, xc, LDS_U + 512 + 8 * ((t2 + 64 * jj) & 255), true);
        } else {
            if (!heavy) {
                u32 s1 = ((j - 7) * 4 + ch) * 57, s2 = ((j - 7) * 4 + ch) * 50;
                asm volatile("" : "+s"(s1), "+s"(s2));
                const u32 i1 = min(s1 + min(lane, 56), 2047u), r1 = i1 >> 2;
                warm1 = ldg<u32>(a.x, (i1 ^ 1024) * 1024 + xcd * 128);
                warm3 = ldg<u32>(a.wu, __umul24((r1 & 3) * 128 + (r1 >> 2), F_ * 2) + (i1 & 3) * 128 + ((xcd * 448) & ~127));
                warm2 = ldg<u32>(a.wd, min(s2 + min(lane, 50), 1791u) * 128 + xcd * 224 * 1024);
                second();
                STAMP(12);
                __builtin_amdgcn_s_sleep(LIGHT_WAIT);
                ld4(sg, sh);
                wait4(sg, sh);
                STAMP(4);
#pragma unroll
                for (int i = 0; i < 4; i++) xc[i] = ldg<u32x2>(a.x, xh + 1024 * i);
                asm volatile("" :: "v"(warm1), "v"(warm2), "v"(warm3));
                stage(sg, xc, a_st, true);
            } else {
#pragma unroll
                for (int i = 0; i < 4; i++) xc[i] = ldg<u32x2>(a.x, xh + 1024 * i);
#pragma unroll
                for (int i = 0; i < 2; i++) xl[i] = ldg<u32x2>(a.x, xo + 1024 * i);
                FENCE();
                ld4(sg, sh);
                FENCE();
#pragma unroll
                for (int i = 2; i < 4; i++) xl[i] = ldg<u32x2>(a.x, xo + 1024 * i);
                FENCE();
                ld4(sl, so);
                *(LAS u32x4*)(lds + LDS_GU + (tid & 127) * 16) = bnd;
                second();
                STAMP(12);
                wait4(sg, sh);
                asm volatile("" :: "v"(warm1));
                STAMP(4);
                stage(sg, xc, a_st + 2048, true);
                if (HG) gates();
                asm volatile("" : "+v"(sl[0]), "+v"(sl[1]), "+v"(sl[2]), "+v"(sl[3]));
                wait4(sl, so);
                stage(sl, xl, a_st, false);
            }
        }
        STAMP(6);
        bar();
        PIN(yb);
        if (!HG || !heavy) gates();
        STAMP(7);
        {
            const u32 need = __builtin_amdgcn_readfirstlane(lt) >= 128 ? 3 : 5;
#pragma clang loop unroll(disable)
            for (u32 spin = 0; spin < SPIN_CAP; spin++) {
                if (!__builtin_amdgcn_ballot_w64(*(volatile LAS u32*)(lds + LDS_FLAG + (lane & 7) * 16 + 12) < need)) break;
                __builtin_amdgcn_s_sleep(YSLEEP);
            }
            FENCE();
        }
        STAMP(8);
        f32x2 yv[4][2];
        u32x2 o[4];
#pragma unroll
        for (int c = 0; c < 4; c++) {
            const float dk = (c & 1) ? bfhi(dsk[c >> 1]) : bflo(dsk[c >> 1]);
            const f32x4 ya = *(const LAS f32x4*)(lds + yb + c * Y_SZ), yc = *(const LAS f32x4*)(lds + yb + (c + 4) * Y_SZ);
            yv[c][0] = __builtin_elementwise_fma(zn[c][0], f32x2{dk, dk}, ya.lo) + yc.lo;
            yv[c][1] = __builtin_elementwise_fma(zn[c][1], f32x2{dk, dk}, ya.hi) + yc.hi;
        }
#pragma unroll
        for (int p = 0; p < 2; p++) {
            f32x2 t[4];
#pragma unroll
            for (int c = 0; c < 4; c++) t[c] = __builtin_elementwise_fma(yv[c][p], gt[c][p], xf[c][p]);
            o[2 * p] = u32x2{pkbf(t[0].x, t[1].x), pkbf(t[2].x, t[3].x)};
            o[2 * p + 1] = u32x2{pkbf(t[0].y, t[1].y), pkbf(t[2].y, t[3].y)};
        }
#pragma unroll
        for (int i = 0; i < 4; i++) stg(a.out, xh + 1024 * i, o[i]);
        STAMP(9);
        rawbar();
        STAMP(11);
        const u32x2 rows = *(const LAS u32x2*)(lds + LDS_UPD + 32);
#pragma clang loop unroll(disable)
        for (int t = 0; t < 2; t++) {
            const u32 e = (t ? rows.y : rows.x) - (xh >> 10);
            if (e < 4) {
                const f32x4 up = *(const LAS f32x4*)(lds + LDS_UPD + t * 16);
                float oe[4];
#pragma unroll
                for (int c = 0; c < 4; c++) {
                    auto pick = [&](const f32x2 v0, const f32x2 v1) { const float s0 = (e & 2) ? v1.x : v0.x, s1 = (e & 2) ? v1.y : v0.y; return (e & 1) ? s1 : s0; };
                    const float zz = rbf(rbf(pick(zn[c][0], zn[c][1])) + up[c]);
                    oe[c] = __builtin_fmaf(pick(yv[c][0], yv[c][1]), zz * sigm(zz), pick(xf[c][0], xf[c][1]));
                }
                stg(a.out, xh + e * 1024, u32x2{pkbf(oe[0], oe[1]), pkbf(oe[2], oe[3])});
            }
        }
    }
    STAMPS_OUT;
}
#define ROLE_OF(b) (((b) >> 3) >= 16 ? 0 : (((b) >> 3) < 7 ? 2 : 1))
#define NWAVES 12

extern "C" void kernel_launch(void* const* d_in, const int* in_sizes, int n_in, void* d_out, int out_size, void* d_ws,
                              size_t ws_size, hipStream_t stream) {
    Args a;
    a.x = (const u16*)d_in[0]; a.are = (const u16*)d_in[1]; a.aim = (const u16*)d_in[2]; a.bre = (const u16*)d_in[3];
    a.bim = (const u16*)d_in[4]; a.cre = (const u16*)d_in[5]; a.cim = (const u16*)d_in[6]; a.logdt = (const u16*)d_in[7];
    a.dskip = (const u16*)d_in[8]; a.wr = (const u16*)d_in[10]; a.wg = (const u16*)d_in[11]; a.wu = (const u16*)d_in[12];
    a.wd = (const u16*)d_in[13]; a.out = (u16*)d_out; a.ws = (char*)d_ws;
    (void)hipFuncSetAttribute((const void*)s4_mod_fused, hipFuncAttributeMaxDynamicSharedMemorySize, LDS_TOTAL);
    hipLaunchKernelGGL(s4_mod_fused, dim3(256), dim3(768), LDS_TOTAL, stream, a);
}
```
